```python
import torch

def _softmax(x, axis=-1):
    return torch.softmax(x, dim=axis)
def _gelu(x):
    return torch.nn.functional.gelu(x, approximate="tanh")
def _silu(x):
    return torch.nn.functional.silu(x)
def _sigmoid(x):
    return torch.sigmoid(x)
def _layernorm(x, axis=-1, eps=1e-5):
    m = x.mean(dim=axis, keepdim=True)
    v = x.var(dim=axis, keepdim=True, unbiased=False)
    return (x - m) / torch.sqrt(v + eps)
def _scatter_add(tgt, idx, src, axis=0):
    return tgt.clone().index_add_(axis, idx, src)
def _segment_sum(src, seg, n):
    out = torch.zeros((n,) + src.shape[1:], dtype=src.dtype)
    return out.index_add_(0, seg, src)
def _pad(x, pw):
    flat = tuple(v for pair in reversed(pw) for v in pair)
    return torch.nn.functional.pad(x, flat)

kernel_name = 'interleave_layers__spectral_gfnet__dense_activation_silu__prompt'

def setup_inputs():

    _w = (lambda a: (lambda t: t.to(torch.bfloat16) if t.is_floating_point() else t)(torch.as_tensor(a)))
    def _A_setup():
        B, S, D = 1, 2048, 640
        Df = D // 2 + 1
        return {
            "x": _w((lambda *s: torch.randn(*s, dtype=torch.bfloat16) * 0.05)(B, S, D)),
            "w_r": _w((lambda *s: torch.randn(*s, dtype=torch.bfloat16) * 0.4)(S, Df) * 0.02),
            "w_i": _w((lambda *s: torch.randn(*s, dtype=torch.bfloat16) * 0.4)(S, Df) * 0.02),
        }
    _A_in = _A_setup()

    def _B_setup():
        B, S, D, F = 1, 2048, 640, 1536
        return {
            "x": _w((lambda *s: torch.randn(*s, dtype=torch.bfloat16) * 0.05)(B, S, D)),
            "w1": _w((lambda *s: torch.randn(*s, dtype=torch.bfloat16) * 0.4)(D, F) * (D ** -0.5)),
            "w2": _w((lambda *s: torch.randn(*s, dtype=torch.bfloat16) * 0.1)(F, D) * (F ** -0.5)),
        }
    _B_in = _B_setup()

    return {
        "A_x": _A_in["x"],
        "A_w_r": _A_in["w_r"],
        "A_w_i": _A_in["w_i"],
        "B_x": _B_in["x"],
        "B_w1": _B_in["w1"],
        "B_w2": _B_in["w2"],
    }

def reference(A_x, A_w_r, A_w_i, B_x, B_w1, B_w2):
    def _A_ref(x, w_r, w_i):
        B, S, D = x.shape
        X = (lambda x, axes=(-2,-1): torch.fft.rfft2(x.float(), dim=axes))(x, axes=(-2, -1))
        W = w_r + 1j * w_i
        X = X * W
        y = (lambda x, s, axes=(-2,-1): torch.fft.irfft2(x, s=s, dim=axes).to(torch.bfloat16))(X, (S, D), axes=(-2, -1))
        return y

    def _B_ref(x, w1, w2):
        h = torch.einsum("bsd,df->bsf", x, w1)
        h = _silu(h)
        out = torch.einsum("bsf,fd->bsd", h, w2)
        return out

    x = A_x
    x = x + _A_ref(_layernorm(x, axis=-1), A_w_r, A_w_i)
    x = x + _B_ref(_layernorm(x, axis=-1), B_w1, B_w2)
    x = x + _A_ref(_layernorm(x, axis=-1), A_w_r, A_w_i)
    x = x + _B_ref(_layernorm(x, axis=-1), B_w1, B_w2)
    return x
```

```cpp
#include <hip/hip_runtime.h>
#include <hip/hip_bf16.h>
#include <stdint.h>

typedef float c2 __attribute__((ext_vector_type(2)));
typedef float f4 __attribute__((ext_vector_type(4)));
typedef unsigned u2 __attribute__((ext_vector_type(2)));
typedef unsigned u4 __attribute__((ext_vector_type(4)));
typedef __bf16 bf2 __attribute__((ext_vector_type(2)));
typedef __bf16 bf8 __attribute__((ext_vector_type(8)));

#define DEV __device__ __forceinline__
#define SCHED_FENCE() __builtin_amdgcn_sched_barrier(0)

constexpr int S_ = 2048, D_ = 640, F_ = 1536, DF_ = 321;
constexpr float LN_EPS = 1e-5f;

constexpr int XB_MS = 81920 + 512;
constexpr int H_TS = 49;
constexpr int W1_TS = 21;
constexpr size_t OFF_XB = 0;
constexpr size_t OFF_H = OFF_XB + 32ull * XB_MS;
constexpr size_t OFF_W1P = OFF_H + 128ull * H_TS * 1024;
constexpr size_t OFF_W2P = OFF_W1P + 96ull * W1_TS * 1024;
constexpr size_t OFF_W2C = OFF_W2P + 40ull * H_TS * 1024;
constexpr size_t OFF_WT = OFF_W2C + 40ull * H_TS * 1024;
constexpr size_t OFF_X3 = OFF_WT + 160ull * 4 * 256 * 16;
constexpr size_t OFF_RS0 = OFF_X3 + 2048ull * 640 * 2;
constexpr size_t OFF_SSQ = OFF_RS0 + 2048 * 4;

#define TSTAMP(K, P) do {} while (0)
DEV c2 add_mi(c2 a, c2 b) {
  c2 d; asm("v_pk_add_f32 %0, %1, %2 op_sel:[0,1] op_sel_hi:[1,0] neg_hi:[0,1]" : "=v"(d) : "v"(a), "v"(b)); return d;
}
DEV c2 add_pi(c2 a, c2 b) {
  c2 d; asm("v_pk_add_f32 %0, %1, %2 op_sel:[0,1] op_sel_hi:[1,0] neg_lo:[0,1]" : "=v"(d) : "v"(a), "v"(b)); return d;
}
DEV c2 cmul(c2 a, c2 w) {
  c2 t, d;
  asm("v_pk_mul_f32 %0, %1, %2 op_sel:[0,0] op_sel_hi:[0,1]" : "=v"(t) : "v"(a), "v"(w));
  asm("v_pk_fma_f32 %0, %1, %2, %3 op_sel:[1,1,0] op_sel_hi:[1,0,1] neg_lo:[0,1,0]" : "=v"(d) : "v"(a), "v"(w), "v"(t));
  return d;
}
DEV c2 cmulc(c2 a, c2 w) {
  c2 t, d;
  asm("v_pk_mul_f32 %0, %1, %2 op_sel:[0,0] op_sel_hi:[0,1] neg_hi:[0,1]" : "=v"(t) : "v"(a), "v"(w));
  asm("v_pk_fma_f32 %0, %1, %2, %3 op_sel:[1,1,0] op_sel_hi:[1,0,1]" : "=v"(d) : "v"(a), "v"(w), "v"(t));
  return d;
}
template <int SGN> DEV c2 twmul(c2 a, c2 w) { return SGN < 0 ? cmul(a, w) : cmulc(a, w); }
template <int SGN> DEV c2 add_rot(c2 a, c2 b) { return SGN < 0 ? add_mi(a, b) : add_pi(a, b); }
template <int SGN> DEV c2 sub_rot(c2 a, c2 b) { return SGN < 0 ? add_pi(a, b) : add_mi(a, b); }
DEV c2 twiddle_rev(float rev) {
  c2 t = c2{__builtin_amdgcn_cosf(rev), -__builtin_amdgcn_sinf(rev)};
  asm("s_nop 1" : "+v"(t));
  return t;
}
template <int N> DEV void tw_powers(c2* t, c2 w) {
  t[0] = w;
#pragma unroll
  for (int k = 2; k <= N; k++) {
    if ((k & 1) == 0) t[k - 1] = cmul(t[k / 2 - 1], t[k / 2 - 1]);
    else {
      int hi = k >= 8 ? 8 : (k >= 4 ? 4 : 2);
      t[k - 1] = cmul(t[hi - 1], t[k - hi - 1]);
    }
  }
}

template <int SGN> DEV void dft4(c2& a, c2& b, c2& c, c2& d) {
  c2 s0 = a + c, s1 = a - c, s2 = b + d, s3 = b - d;
  a = s0 + s2; c = s0 - s2;
  b = add_rot<SGN>(s1, s3); d = sub_rot<SGN>(s1, s3);
}
template <int SGN> DEV c2 rot1(c2 a) { return add_rot<SGN>(a, a) * 0.70710678118654752f; }
template <int SGN> DEV c2 rot3(c2 a) { return sub_rot<SGN>(a, a) * -0.70710678118654752f; }

template <int SGN> DEV void dft8(c2* v) {
  c2 e0 = v[0], e1 = v[2], e2 = v[4], e3 = v[6], o0 = v[1], o1 = v[3], o2 = v[5], o3 = v[7];
  dft4<SGN>(e0, e1, e2, e3);
  dft4<SGN>(o0, o1, o2, o3);
  o1 = rot1<SGN>(o1); o3 = rot3<SGN>(o3);
  v[0] = e0 + o0; v[4] = e0 - o0;
  v[1] = e1 + o1; v[5] = e1 - o1;
  v[2] = add_rot<SGN>(e2, o2); v[6] = sub_rot<SGN>(e2, o2);
  v[3] = e3 + o3; v[7] = e3 - o3;
}
template <int SGN> DEV void dft16(c2* v) {
  const c2 w1 = c2{0.92387953251128674f, -0.38268343236508977f};
  const c2 w3 = c2{0.38268343236508977f, -0.92387953251128674f};
  c2 y[4][4];
#pragma unroll
  for (int i = 0; i < 4; i++) {
    c2 a = v[i], b = v[i + 4], c = v[i + 8], d = v[i + 12];
    dft4<SGN>(a, b, c, d);
    y[i][0] = a; y[i][1] = b; y[i][2] = c; y[i][3] = d;
  }
  y[1][1] = twmul<SGN>(y[1][1], w1);
  y[1][2] = rot1<SGN>(y[1][2]);
  y[1][3] = twmul<SGN>(y[1][3], w3);
  y[2][1] = rot1<SGN>(y[2][1]);
  y[2][3] = rot3<SGN>(y[2][3]);
  y[3][1] = twmul<SGN>(y[3][1], w3);
  y[3][2] = rot3<SGN>(y[3][2]);
  y[3][3] = -twmul<SGN>(y[3][3], w1);
#pragma unroll
  for (int q = 0; q < 4; q++) {
    c2 a = y[0][q], b = y[1][q], c = y[2][q], d = y[3][q];
    if (q == 2) {
      c2 s0 = add_rot<SGN>(a, c), s1 = sub_rot<SGN>(a, c), s2 = b + d, s3 = b - d;
      a = s0 + s2; c = s0 - s2;
      b = add_rot<SGN>(s1, s3); d = sub_rot<SGN>(s1, s3);
    } else {
      dft4<SGN>(a, b, c, d);
    }
    v[q] = a; v[q + 4] = b; v[q + 8] = c; v[q + 12] = d;
  }
}
template <int SGN> DEV void dft5(c2* x) {
  const float c1 = 0.30901699437494742f, c2_ = -0.80901699437494742f;
  const float s1 = 0.95105651629515357f, s2 = 0.58778525229247313f;
  c2 t1 = x[1] + x[4], t2 = x[2] + x[3], t3 = x[1] - x[4], t4 = x[2] - x[3];
  c2 a1 = x[0] + c1 * t1 + c2_ * t2, a2 = x[0] + c2_ * t1 + c1 * t2;
  c2 b1 = s1 * t3 + s2 * t4, b2 = s2 * t3 - s1 * t4;
  x[0] = x[0] + t1 + t2;
  x[1] = add_rot<SGN>(a1, b1); x[4] = sub_rot<SGN>(a1, b1);
  x[2] = add_rot<SGN>(a2, b2); x[3] = sub_rot<SGN>(a2, b2);
}

DEV void lds_fence() { asm volatile("" ::: "memory"); __builtin_amdgcn_wave_barrier(); asm volatile("" ::: "memory"); }
DEV float bf_lo(unsigned u) { return __builtin_bit_cast(float, u << 16); }
DEV float bf_hi(unsigned u) { return __builtin_bit_cast(float, u & 0xffff0000u); }
DEV unsigned pack_bf(float a, float b) { return __builtin_bit_cast(unsigned, __builtin_convertvector(c2{a, b}, bf2)); }

constexpr int SCR_C2 = 400;
struct Tw320 { c2 a[4]; c2 b[7]; c2 u[5]; };
template <int NU> DEV void tw320_init(Tw320& tw, int lane) {
  c2 u0 = twiddle_rev((float)lane * (1.0f / 640.0f));
  tw_powers<4>(tw.a, cmul(u0, u0));
  tw_powers<7>(tw.b, twiddle_rev((float)(lane & 7) * (1.0f / 64.0f)));
  tw.u[0] = u0;
  if (NU > 1) tw.u[1] = cmul(u0, c2{0.80901699437494742f, -0.58778525229247313f});
  if (NU > 2) tw.u[2] = cmul(u0, c2{0.30901699437494742f, -0.95105651629515357f});
  if (NU > 3) tw.u[3] = cmul(u0, c2{-0.30901699437494742f, -0.95105651629515357f});
  if (NU > 4) tw.u[4] = cmul(u0, c2{-0.80901699437494742f, -0.58778525229247313f});
}
template <int SGN> DEV void fft320(c2* z, const Tw320& tw, c2* scr, int lane, c2* o) {
  dft5<SGN>(z);
#pragma unroll
  for (int q = 1; q < 5; q++) z[q] = twmul<SGN>(z[q], tw.a[q - 1]);
  lds_fence();
#pragma unroll
  for (int q = 0; q < 5; q++) scr[q * 72 + lane] = z[q];
  lds_fence();
  int u = lane < 40 ? lane : 39;
  int q = u >> 3, a = u & 7;
#pragma unroll
  for (int b = 0; b < 8; b++) o[b] = scr[q * 72 + a + 8 * b];
  dft8<SGN>(o);
#pragma unroll
  for (int c = 1; c < 8; c++) o[c] = twmul<SGN>(o[c], tw.b[c - 1]);
  lds_fence();
  if (lane < 40) {
#pragma unroll
    for (int c = 0; c < 8; c++) scr[(q * 8 + c) * 10 + a] = o[c];
  }
  lds_fence();
#pragma unroll
  for (int i = 0; i < 4; i++) {
    f4 t = *(const f4*)&scr[u * 10 + 2 * i];
    o[2 * i] = t.xy; o[2 * i + 1] = t.zw;
  }
  dft8<SGN>(o);
  lds_fence();
}

constexpr int TS = 648;
DEV float rfft640_row(c2* z, const Tw320& tw, c2* scr, int lane, float* trow) {
  c2 o[8];
  fft320<-1>(z, tw, scr, lane, o);
  if (lane < 40) {
    int q = lane >> 3, c = lane & 7;
#pragma unroll
    for (int e = 0; e < 8; e++) scr[q + 5 * c + 40 * e] = o[e];
  }
  lds_fence();
  const float sc = 0.5f * 0.05590169943749474f;
  float ssq = 0.f;
#pragma unroll
  for (int i = 0; i < 3; i++) {
    int k = lane + 64 * i;
    if (k <= 160) {
      c2 A = scr[k], B = scr[k == 0 ? 0 : 320 - k];
      c2 Bc = c2{B.x, -B.y};
      c2 E = (A + Bc) * sc, Od = (A - Bc) * sc;
      c2 wo = cmul(Od, tw.u[i]);
      c2 X1 = add_mi(E, wo), X2 = add_pi(E, wo);
      X2.y = -X2.y;
      if (k == 0) {
        const float r = 0.70710678118654752f;
        float ny = X2.x * r;
        *(c2*)&trow[0] = c2{X1.x * r, ny};
        ssq += ny * ny;
      } else {
        *(c2*)&trow[2 * k] = X1;
        ssq += X1.x * X1.x + X1.y * X1.y;
        if (k != 160) {
          *(c2*)&trow[2 * (320 - k)] = X2;
          ssq += X2.x * X2.x + X2.y * X2.y;
        }
      }
    }
  }
  lds_fence();
  return ssq;
}

DEV float wave_sum(float v) {
#pragma unroll
  for (int o = 32; o >= 1; o >>= 1) v += __shfl_xor(v, o);
  return v;
}

DEV int frag_f(int T, int g, int j) {
  int G = T / 3, r = T - 3 * G;
  int ta = r == 0 ? 0 : (r == 1 ? 3 : 2), tb = r == 0 ? 1 : (r == 1 ? 4 : 5);
  return 96 * G + 16 * (j < 4 ? ta : tb) + 4 * g + (j & 3);
}

__global__ __launch_bounds__(512) void k_prep(const uint16_t* __restrict__ x, const uint16_t* __restrict__ wr,
                                              const uint16_t* __restrict__ wi, const uint16_t* __restrict__ w1,
                                              char* __restrict__ ws) {
  __shared__ __attribute__((aligned(16))) float tile[8 * TS];
  __shared__ __attribute__((aligned(16))) c2 scr_all[8 * SCR_C2];
  const int tid = threadIdx.x, lane = tid & 63, wv = tid >> 6;
  const int b = blockIdx.x;
  c2* scr = scr_all + wv * SCR_C2;
  TSTAMP(0, 0);

  if (b >= 448) {
    int bb = b - 448;
    int unit = (bb & 7) * 16 + (bb >> 3);
    int u = unit & 3, bbit = (unit >> 2) & 1, kc = unit >> 3;
    unsigned* t32 = (unsigned*)tile;
    unsigned short re[2][6], im[2][6];
#pragma unroll
    for (int hh = 0; hh < 2; hh++) {
      int ks = kc + 16 * (2 * u + hh + 8 * bbit) + 256 * wv;
      const uint16_t* pr = wr + (size_t)ks * DF_;
      const uint16_t* pi = wi + (size_t)ks * DF_;
#pragma unroll
      for (int i = 0; i < 6; i++) {
        int c = lane + 64 * i;
        c = c < DF_ ? c : DF_ - 1;
        re[hh][i] = pr[c]; im[hh][i] = pi[c];
      }
    }
#pragma unroll
    for (int hh = 0; hh < 2; hh++)
#pragma unroll
      for (int i = 0; i < 6; i++) {
        int c = lane + 64 * i;
        if (c < DF_) t32[(hh * 8 + wv) * 324 + c] = (unsigned)re[hh][i] | ((unsigned)im[hh][i] << 16);
      }
    __syncthreads();
    u4* WT = (u4*)(ws + OFF_WT);
    for (int n = tid; n < 1280; n += 512) {
      int col = n & 1, hh = (n >> 1) & 1, ih = (n >> 2) & 1, q4 = n >> 3;
      int cc = 2 * q4 + col;
      if (cc == 0) cc = 320;
      u4 v;
#pragma unroll
      for (int e = 0; e < 4; e++) v[e] = t32[(hh * 8 + ih * 4 + e) * 324 + cc];
      int tau = (2 * u + hh) + 8 * kc;
      WT[(size_t)(q4 * 4 + 2 * bbit + ih) * 256 + 2 * tau + col] = v;
    }
    TSTAMP(0, 7);
    return;
  }

  Tw320 tw;
  c2 z[5];
  if (b < 256) {
    const int oct = (b & 7) * 32 + (b >> 3);
    const unsigned* row = (const unsigned*)(x + (size_t)(8 * oct + wv) * D_);
    unsigned raw[5];
#pragma unroll
    for (int j = 0; j < 5; j++) raw[j] = row[lane + 64 * j];
    if (tid < 8) ((double*)(ws + OFF_SSQ))[8 * oct + tid] = 0.0;
    tw320_init<3>(tw, lane);
#pragma unroll
    for (int j = 0; j < 5; j++) z[j] = c2{bf_lo(raw[j]), bf_hi(raw[j])};
    TSTAMP(0, 1);
    float ssq = rfft640_row(z, tw, scr, lane, tile + wv * TS);
    ssq = wave_sum(ssq);
    TSTAMP(0, 2);
    if (lane == 0) ((float*)(ws + OFF_RS0))[8 * oct + wv] = (1.0f / 2048.0f) / sqrtf(ssq * (1.0f / 640.0f) + LN_EPS);
    __syncthreads();
    char* XB = ws + OFF_XB + (size_t)(oct >> 3) * XB_MS;
    const int moff = (oct & 7) * 64;
    for (int n = tid; n < 640; n += 512) {
      int rp = n & 3, q4 = n >> 2;
      f4 a = *(const f4*)&tile[(2 * rp) * TS + 4 * q4], c = *(const f4*)&tile[(2 * rp + 1) * TS + 4 * q4];
      u4 o = u4{pack_bf(a.x, a.y), pack_bf(a.z, a.w), pack_bf(c.x, c.y), pack_bf(c.z, c.w)};
      *(u4*)(XB + q4 * 512 + ((moff + rp * 16) ^ (((q4 >> 1) & 1) << 7))) = o;
    }
    TSTAMP(0, 7);
    return;
  }
  {
    int bb = b - 256;
    int fg = (bb & 7) * 24 + (bb >> 3);
    int f0 = fg * 8;
    unsigned short* t16 = (unsigned short*)tile;
    u4 v0 = *(const u4*)(w1 + (size_t)tid * F_ + f0);
    u4 v1 = *(const u4*)(w1 + (size_t)(512 + (tid & 127)) * F_ + f0);
    tw320_init<3>(tw, lane);
#pragma unroll
    for (int j = 0; j < 4; j++) {
      t16[(2 * j) * TS + tid] = (unsigned short)(v0[j] & 0xffff);
      t16[(2 * j + 1) * TS + tid] = (unsigned short)(v0[j] >> 16);
    }
    if (tid < 128) {
#pragma unroll
      for (int j = 0; j < 4; j++) {
        t16[(2 * j) * TS + 512 + tid] = (unsigned short)(v1[j] & 0xffff);
        t16[(2 * j + 1) * TS + 512 + tid] = (unsigned short)(v1[j] >> 16);
      }
    }
    __syncthreads();
    const unsigned* r32 = (const unsigned*)(t16 + wv * TS);
#pragma unroll
    for (int j = 0; j < 5; j++) { unsigned u = r32[lane + 64 * j]; z[j] = c2{bf_lo(u), bf_hi(u)}; }
    __syncthreads();
    TSTAMP(0, 1);
    rfft640_row(z, tw, scr, lane, tile + wv * TS);
    TSTAMP(0, 2);
    __syncthreads();
    u4* W1P = (u4*)(ws + OFF_W1P);
    for (int n = tid; n < 640; n += 512) {
      int fl = n & 7, o8 = n >> 3;
      const float* p = &tile[fl * TS + 8 * o8];
      f4 a = *(const f4*)p, c = *(const f4*)(p + 4);
      if (o8 == 0) a.x = 0.f;
      u4 o = u4{pack_bf(a.x, a.y), pack_bf(a.z, a.w), pack_bf(c.x, c.y), pack_bf(c.z, c.w)};
      int f = f0 + fl, ks = o8 >> 2, g = o8 & 3;
      W1P[((size_t)(f >> 4) * W1_TS + ks) * 64 + g * 16 + (f & 15)] = o;
    }
    TSTAMP(0, 7);
  }
}

constexpr int P1 = 136;
constexpr int LDS_CF = 84 * 1024;
template <bool FIRST>
__global__ __launch_bounds__(256, 1) void k_colfilter(const uint16_t* __restrict__ w2, char* __restrict__ ws) {
  extern __shared__ __attribute__((aligned(16))) char smem[];
  const int tid = threadIdx.x;
  const int xcd = blockIdx.x & 7, slot = blockIdx.x >> 3;
  constexpr int KID = FIRST ? 1 : 4;
  (void)KID;
  TSTAMP(KID, 0);
  if (slot >= 20) {
    const int lane = tid & 63, wv = tid >> 6;
    const int i = (slot - 20) * 8 + xcd;
    const int T = i >> 1, g0 = (i & 1) * 2;
    if (FIRST) {
      float* tile = (float*)smem;
      c2* scr = (c2*)(smem + 16 * TS * 4) + wv * SCR_C2;
      unsigned raw[4][5];
#pragma unroll
      for (int rr = 0; rr < 4; rr++) {
        int r = wv * 4 + rr;
        const unsigned* row = (const unsigned*)(w2 + (size_t)frag_f(T, g0 + (r >> 3), r & 7) * D_);
#pragma unroll
        for (int j = 0; j < 5; j++) raw[rr][j] = row[lane + 64 * j];
      }
      Tw320 tw;
      tw320_init<3>(tw, lane);
#pragma unroll
      for (int rr = 0; rr < 4; rr++) {
        c2 z[5];
#pragma unroll
        for (int j = 0; j < 5; j++) z[j] = c2{bf_lo(raw[rr][j]), bf_hi(raw[rr][j])};
        rfft640_row(z, tw, scr, lane, tile + (wv * 4 + rr) * TS);
      }
      __syncthreads();
      u4* W2P = (u4*)(ws + OFF_W2P);
#pragma unroll
      for (int it = 0; it < 5; it++) {
        int n = tid + 256 * it;
        int cl = n & 15, gg = (n >> 4) & 1, ct = n >> 5;
        const float* p = tile + gg * 8 * TS + ct * 16 + cl;
        u4 o;
#pragma unroll
        for (int j = 0; j < 4; j++) o[j] = pack_bf(p[(2 * j) * TS], p[(2 * j + 1) * TS]);
        W2P[((size_t)ct * H_TS + T) * 64 + (g0 + gg) * 16 + cl] = o;
      }
    } else {
      u4* W2C = (u4*)(ws + OFF_W2C);
      unsigned short v[5][8];
#pragma unroll
      for (int it = 0; it < 5; it++) {
        int n = tid + 256 * it;
        int cl = n & 15, gg = (n >> 4) & 1, ct = n >> 5;
#pragma unroll
        for (int j = 0; j < 8; j++) v[it][j] = w2[(size_t)frag_f(T, g0 + gg, j) * D_ + ct * 16 + cl];
      }
#pragma unroll
      for (int it = 0; it < 5; it++) {
        int n = tid + 256 * it;
        int cl = n & 15, gg = (n >> 4) & 1, ct = n >> 5;
        u4 o;
#pragma unroll
        for (int j = 0; j < 4; j++) o[j] = (unsigned)v[it][2 * j] | ((unsigned)v[it][2 * j + 1] << 16);
        W2C[((size_t)ct * H_TS + T) * 64 + (g0 + gg) * 16 + cl] = o;
      }
    }
    TSTAMP(KID, 7);
    return;
  }
  c2* ex = (c2*)smem;
  const int col = tid & 1, tau = tid >> 1;
  const int q4 = xcd * 20 + slot;
  unsigned* XB = (unsigned*)(ws + OFF_XB) + (size_t)(tau >> 6) * (XB_MS / 4) + q4 * 128 + ((((tau & 63) << 1) + col) ^ (((q4 >> 1) & 1) << 5));
  unsigned xr[16];
  c2 v[16];
  float rs[16];
  u4 wt[4];
#pragma unroll
  for (int r = 0; r < 16; r++) xr[r] = XB[(size_t)r * (XB_MS / 2)];
  if (FIRST) {
    const float* RS = (const float*)(ws + OFF_RS0);
#pragma unroll
    for (int r = 0; r < 16; r++) rs[r] = RS[tau + 128 * r];
  } else {
    const double* SQ = (const double*)(ws + OFF_SSQ);
#pragma unroll
    for (int r = 0; r < 16; r++) rs[r] = (float)SQ[tau + 128 * r];
  }
  const u4* WT = (const u4*)(ws + OFF_WT) + (size_t)q4 * 1024;
#pragma unroll
  for (int i = 0; i < 4; i++) wt[i] = WT[i * 256 + tid];
  SCHED_FENCE();

  const int lo = tau & 7, kc = tau >> 3;
  c2 tw1[15], tw2[15];
  tw_powers<15>(tw1, twiddle_rev((float)tau * (1.0f / 2048.0f)));
  tw_powers<15>(tw2, twiddle_rev((float)lo * (1.0f / 128.0f)));
  SCHED_FENCE();
  if (!FIRST) {
#pragma unroll
    for (int r = 0; r < 16; r++) rs[r] = (1.0f / 2048.0f) * __builtin_amdgcn_rsqf(rs[r] * (1.0f / 640.0f) + LN_EPS);
  }
  const bool special = (q4 == 0) && (col == 0);
#pragma unroll
  for (int r = 0; r < 16; r++) {
    unsigned u = special ? (xr[r] >> 16) : xr[r];
    v[r] = c2{bf_lo(u), bf_hi(u)} * rs[r];
  }
  TSTAMP(KID, 1);
  dft16<-1>(v);
#pragma unroll
  for (int k = 1; k < 16; k++) v[k] = cmul(v[k], tw1[k - 1]);
#pragma unroll
  for (int k = 0; k < 16; k++) ex[(k * P1 + tau) * 2 + col] = v[k];
  __syncthreads();
#pragma unroll
  for (int sb = 0; sb < 16; sb++) v[sb] = ex[(kc * P1 + sb * 8 + lo) * 2 + col];
  dft16<-1>(v);
#pragma unroll
  for (int k = 1; k < 16; k++) v[k] = cmul(v[k], tw2[k - 1]);
  lds_fence();
#pragma unroll
  for (int kb = 0; kb < 16; kb++) ex[(kc * P1 + kb * 8 + (lo ^ (kb & 7))) * 2 + col] = v[kb];
  lds_fence();
#pragma unroll
  for (int bb = 0; bb < 2; bb++)
#pragma unroll
    for (int sa = 0; sa < 8; sa++) v[bb * 8 + sa] = ex[(kc * P1 + (lo + 8 * bb) * 8 + (sa ^ lo)) * 2 + col];
  dft8<-1>(v);
  dft8<-1>(v + 8);
  TSTAMP(KID, 2);
#pragma unroll
  for (int r = 0; r < 16; r++) {
    unsigned w = wt[r >> 2][r & 3];
    v[r] = cmul(v[r], c2{bf_lo(w), bf_hi(w)});
  }
  dft8<1>(v);
  dft8<1>(v + 8);
  lds_fence();
#pragma unroll
  for (int bb = 0; bb < 2; bb++)
#pragma unroll
    for (int sa = 0; sa < 8; sa++) ex[(kc * P1 + (lo + 8 * bb) * 8 + (sa ^ lo)) * 2 + col] = v[bb * 8 + sa];
  lds_fence();
#pragma unroll
  for (int kb = 0; kb < 16; kb++) v[kb] = ex[(kc * P1 + kb * 8 + (lo ^ (kb & 7))) * 2 + col];
#pragma unroll
  for (int k = 1; k < 16; k++) v[k] = cmulc(v[k], tw2[k - 1]);
  dft16<1>(v);
  lds_fence();
#pragma unroll
  for (int sb = 0; sb < 16; sb++) ex[(kc * P1 + sb * 8 + lo) * 2 + col] = v[sb];
  __syncthreads();
#pragma unroll
  for (int k = 0; k < 16; k++) v[k] = ex[(k * P1 + tau) * 2 + col];
#pragma unroll
  for (int k = 1; k < 16; k++) v[k] = cmulc(v[k], tw1[k - 1]);
  dft16<1>(v);
  TSTAMP(KID, 3);
#pragma unroll
  for (int r = 0; r < 16; r++) {
    c2 y = special ? c2{0.f, v[r].x} : v[r];
    c2 o = c2{bf_lo(xr[r]), bf_hi(xr[r])} + y;
    XB[(size_t)r * (XB_MS / 2)] = pack_bf(o.x, o.y);
  }
  TSTAMP(KID, 7);
}

constexpr int G1_A_BYTES = 81920;
constexpr int G1_WSCR = 2 * 328 * 4 + 2 * SCR_C2 * 8;
#define G1_PF 7
#define G2_PF 4
constexpr int LDS_G1 = G1_A_BYTES + 256;
constexpr int LDS_G1I = LDS_G1 + 4 * G1_WSCR;
DEV float dot2(unsigned a, float c) {
  bf2 q = __builtin_bit_cast(bf2, a);
  return __builtin_amdgcn_fdot2_f32_bf16(q, q, c, false);
}
template <bool WITH_INV>
__global__ __launch_bounds__(256, 1) void k_gemm1(char* __restrict__ ws) {
  extern __shared__ __attribute__((aligned(16))) char smem[];
  const int tid = threadIdx.x, lane = tid & 63;
  const int wv = __builtin_amdgcn_readfirstlane(tid >> 6);
  const int b = blockIdx.x;
  const int xcd = b & 7, slot = b >> 3;
  const int mblk = (xcd >> 1) * 8 + (slot & 7), fs = (xcd & 1) * 4 + (slot >> 3);
  constexpr int KID = WITH_INV ? 5 : 2;
  (void)KID;
  asm volatile("" ::: "a127");
  {
    const char* Ag = ws + OFF_XB + (size_t)mblk * XB_MS + wv * 20480;
    unsigned lds0 = (unsigned)(uintptr_t)(__attribute__((address_space(3))) char*)smem + wv * 20480;
    unsigned voff = lane * 16;
#pragma unroll
    for (int i = 0; i < 5; i++) {
      unsigned keep;
      asm volatile(
          "s_mov_b32 %0, m0\n\ts_mov_b32 m0, %3\n\ts_nop 0\n\t"
          "global_load_lds_dwordx4 %1, %2\n\t"
          "global_load_lds_dwordx4 %1, %2 offset:1024\n\t"
          "global_load_lds_dwordx4 %1, %2 offset:2048\n\t"
          "global_load_lds_dwordx4 %1, %2 offset:3072\n\t"
          "s_mov_b32 m0, %0"
          : "=&s"(keep) : "v"(voff), "s"(Ag + i * 4096), "s"(lds0 + i * 4096) : "memory");
    }
  }
  const bf8* Wg = (const bf8*)(ws + OFF_W1P) + (size_t)(fs * 12 + wv * 3) * W1_TS * 64 + lane;
  constexpr int PF = G1_PF;
  bf8 wf[20][3];
#define G1_LOADW(S) do { _Pragma("unroll") for (int t_ = 0; t_ < 3; t_++) wf[S][t_] = Wg[(t_ * W1_TS + (S)) * 64]; SCHED_FENCE(); } while (0)
#pragma unroll
  for (int s = 0; s < PF; s++) G1_LOADW(s);
  Tw320 tw;
  if (WITH_INV) tw320_init<5>(tw, lane);
  SCHED_FENCE();
  asm volatile("s_waitcnt vmcnt(%0)\n\ts_barrier" :: "n"(3 * PF) : "memory");
  const int g = lane >> 4, r = lane & 15;
  const int swz = (g & 1) << 7;
  const char* abase[4];
#pragma unroll
  for (int m = 0; m < 4; m++) abase[m] = smem + (2 * g) * 512 + (((16 * ((m + wv) & 3) + r) * 8) ^ swz);
  constexpr int NPRE = WITH_INV ? 20 : PF;
  if (WITH_INV) {
    unsigned* buf = (unsigned*)(smem + LDS_G1 + wv * G1_WSCR);
    c2* scr = (c2*)(buf + 2 * 328);
    const int m0 = fs * 8 + 2 * wv;
#pragma unroll
    for (int i = 0; i < 3; i++) {
      int q = lane + 64 * i;
      if (q < 160) {
        u4 t = *(const u4*)(smem + q * 512 + ((m0 * 8) ^ (((q >> 1) & 1) << 7)));
        *(u2*)&buf[2 * q] = t.xy;
        *(u2*)&buf[328 + 2 * q] = t.zw;
      }
    }
    lds_fence();
    SCHED_FENCE();
    G1_LOADW(PF); G1_LOADW(PF + 1);
    const float sc = 0.5f * 17.88854381999832f / 320.0f;
    const float r2 = 1.41421356237309505f;
    c2 z[2][5], o[2][8];
#pragma unroll
    for (int rr = 0; rr < 2; rr++) {
      const unsigned* X = buf + rr * 328;
#pragma unroll
      for (int j = 0; j < 5; j++) {
        int k = lane + 64 * j;
        unsigned ua = X[k], ub = X[k == 0 ? 0 : 320 - k];
        c2 A = c2{bf_lo(ua), bf_hi(ua)}, Bc = c2{bf_lo(ub), -bf_hi(ub)};
        if (j == 0 && k == 0) { Bc = c2{A.y * r2, 0.f}; A = c2{A.x * r2, 0.f}; }
        c2 E = (A + Bc) * sc, Od = (A - Bc) * sc;
        z[rr][j] = add_pi(E, cmulc(Od, tw.u[j]));
      }
    }
    SCHED_FENCE();
    G1_LOADW(PF + 2); G1_LOADW(PF + 3);
    const int u = lane < 40 ? lane : 39;
    const int q = u >> 3, a = u & 7;
#pragma unroll
    for (int rr = 0; rr < 2; rr++) {
      dft5<1>(z[rr]);
#pragma unroll
      for (int i = 1; i < 5; i++) z[rr][i] = cmulc(z[rr][i], tw.a[i - 1]);
#pragma unroll
      for (int i = 0; i < 5; i++) scr[rr * SCR_C2 + i * 72 + lane] = z[rr][i];
    }
    lds_fence();
    SCHED_FENCE();
    G1_LOADW(PF + 4); G1_LOADW(PF + 5);
#pragma unroll
    for (int rr = 0; rr < 2; rr++)
#pragma unroll
      for (int i = 0; i < 8; i++) o[rr][i] = scr[rr * SCR_C2 + q * 72 + a + 8 * i];
    SCHED_FENCE();
    G1_LOADW(PF + 6); G1_LOADW(PF + 7);
#pragma unroll
    for (int rr = 0; rr < 2; rr++) {
      dft8<1>(o[rr]);
#pragma unroll
      for (int c = 1; c < 8; c++) o[rr][c] = cmulc(o[rr][c], tw.b[c - 1]);
    }
    lds_fence();
    if (lane < 40) {
#pragma unroll
      for (int rr = 0; rr < 2; rr++)
#pragma unroll
        for (int c = 0; c < 8; c++) scr[rr * SCR_C2 + (q * 8 + c) * 10 + a] = o[rr][c];
    }
    lds_fence();
    SCHED_FENCE();
    G1_LOADW(PF + 8); G1_LOADW(PF + 9);
#pragma unroll
    for (int rr = 0; rr < 2; rr++)
#pragma unroll
      for (int i = 0; i < 4; i++) {
        f4 t = *(const f4*)&scr[rr * SCR_C2 + u * 10 + 2 * i];
        o[rr][2 * i] = t.xy; o[rr][2 * i + 1] = t.zw;
      }
    SCHED_FENCE();
#pragma unroll
    for (int s = PF + 10; s < 20; s++) G1_LOADW(s);
    unsigned pk[2][8];
#pragma unroll
    for (int rr = 0; rr < 2; rr++) {
      dft8<1>(o[rr]);
#pragma unroll
      for (int e = 0; e < 8; e++) pk[rr][e] = pack_bf(o[rr][e].x, o[rr][e].y);
    }
    if (lane < 40) {
      unsigned* out = (unsigned*)(ws + OFF_X3) + (size_t)(mblk * 64 + m0) * 320 + q + 5 * a;
#pragma unroll
      for (int rr = 0; rr < 2; rr++)
#pragma unroll
        for (int e = 0; e < 8; e++) out[rr * 320 + 40 * e] = pk[rr][e];
    }
  }
  SCHED_FENCE();
  f4 acc[3][4];
#pragma unroll
  for (int t = 0; t < 3; t++)
#pragma unroll
    for (int m = 0; m < 4; m++) acc[t][m] = f4{0.f, 0.f, 0.f, 0.f};
  u4 bfr[2][4];
#pragma unroll
  for (int m = 0; m < 4; m++) {
    u2 lo = *(const u2*)(abase[m]), hi = *(const u2*)(abase[m] + 512);
    bfr[0][m] = u4{lo.x, lo.y, hi.x, hi.y};
  }
  float sq[4] = {0.f, 0.f, 0.f, 0.f};
#pragma unroll
  for (int ks = 0; ks < 20; ks++) {
    if (ks + 1 < 20) {
#pragma unroll
      for (int m = 0; m < 4; m++) {
        u2 lo = *(const u2*)(abase[m] + (ks + 1) * 4096), hi = *(const u2*)(abase[m] + (ks + 1) * 4096 + 512);
        bfr[(ks + 1) & 1][m] = u4{lo.x, lo.y, hi.x, hi.y};
      }
    }
    SCHED_FENCE();
#pragma unroll
    for (int t = 0; t < 3; t++) {
#pragma unroll
      for (int m = 0; m < 4; m++)
        acc[t][m] = __builtin_amdgcn_mfma_f32_16x16x32_bf16(wf[ks][t], __builtin_bit_cast(bf8, bfr[ks & 1][m]), acc[t][m], 0, 0, 0);
      if (t == 0) {
        unsigned e0 = bfr[ks & 1][0].x;
        if (ks == 0 && g == 0) e0 &= 0xffff0000u;
        sq[0] = dot2(e0, sq[0]); sq[1] = dot2(bfr[ks & 1][0].y, sq[1]);
      }
      if (t == 1) { sq[2] = dot2(bfr[ks & 1][0].z, sq[2]); sq[3] = dot2(bfr[ks & 1][0].w, sq[3]); }
      if (ks + NPRE < 20) wf[ks + NPRE][t] = Wg[(t * W1_TS + ks + NPRE) * 64];
      SCHED_FENCE();
    }
  }
  {
    float s = (sq[0] + sq[1]) + (sq[2] + sq[3]);
    s += __shfl_xor(s, 16);
    s += __shfl_xor(s, 32);
    if (g == 0) ((float*)(smem + G1_A_BYTES))[wv * 16 + r] = __builtin_amdgcn_rsqf(s * (1.0f / 640.0f) + LN_EPS);
  }
  asm volatile("s_waitcnt lgkmcnt(0)\n\ts_barrier" ::: "memory");
  TSTAMP(KID, 3);
  char* Hb = ws + OFF_H;
  const int G = fs * 2 + (wv >> 1);
  const int Tfull = 3 * G + (wv & 1), Thalf = 3 * G + 2;
#pragma unroll
  for (int m = 0; m < 4; m++) {
    const int mg = (m + wv) & 3;
    const float rsig = ((const float*)(smem + G1_A_BYTES))[mg * 16 + r];
    const float rl = rsig * -1.44269504088896341f;
    unsigned pk[3][2];
#pragma unroll
    for (int t = 0; t < 3; t++) {
      float hv[4];
#pragma unroll
      for (int e = 0; e < 4; e++) {
        float a = acc[t][m][e];
        hv[e] = (a * rsig) * __builtin_amdgcn_rcpf(1.0f + __builtin_amdgcn_exp2f(a * rl));
      }
      pk[t][0] = pack_bf(hv[0], hv[1]);
      pk[t][1] = pack_bf(hv[2], hv[3]);
    }
    size_t mt = (size_t)mblk * 4 + mg;
    *(u4*)(Hb + ((mt * H_TS + Tfull) * 64 + lane) * 16) = u4{pk[0][0], pk[0][1], pk[1][0], pk[1][1]};
    *(u2*)(Hb + ((mt * H_TS + Thalf) * 64 + lane) * 16 + (wv & 1) * 8) = u2{pk[2][0], pk[2][1]};
  }
  TSTAMP(KID, 7);
}

template <bool FINAL>
__global__ __launch_bounds__(256, 1) void k_gemm2(char* __restrict__ ws, __hip_bfloat16* __restrict__ out) {
  __shared__ __attribute__((aligned(16))) f4 red[4 * 3 * 5 * 64];
  const int tid = threadIdx.x, lane = tid & 63;
  const int wv = __builtin_amdgcn_readfirstlane(tid >> 6);
  const int b = blockIdx.x;
  const int xcd = b & 7, slot = b >> 3;
  const int mblk = (xcd >> 1) * 8 + (slot & 7), cs = (xcd & 1) * 4 + (slot >> 3);
  const int g = lane >> 4, r = lane & 15;
  constexpr int KID = FINAL ? 6 : 3;
  (void)KID;
  asm volatile("" ::: "a127");
  const bf8* Hg = (const bf8*)(ws + OFF_H) + ((size_t)mblk * 4 * H_TS + wv * 12) * 64 + lane;
  const bf8* Hm[4];
#pragma unroll
  for (int m = 0; m < 4; m++) Hm[m] = Hg + ((m + wv) & 3) * H_TS * 64;
  const bf8* Wg = (const bf8*)(ws + (FINAL ? OFF_W2C : OFF_W2P)) + ((size_t)cs * 5 * H_TS + wv * 12) * 64 + lane;
  constexpr int PF = G2_PF;
  bf8 hf[PF + 1][4], wf[PF][5];
#pragma unroll
  for (int s = 0; s < PF; s++) {
#pragma unroll
    for (int m = 0; m < 4; m++) hf[s][m] = Hm[m][s * 64];
#pragma unroll
    for (int c = 0; c < 5; c++) wf[s][c] = Wg[(c * H_TS + s) * 64];
    SCHED_FENCE();
  }
  const int mrow = mblk * 64 + wv * 16 + r;
  u2 res[5];
  char* XB = ws + OFF_XB + (size_t)mblk * XB_MS + (cs * 20 + g) * 512 + (((wv * 16 + r) * 8) ^ ((g >> 1) << 7));
#pragma unroll
  for (int c = 0; c < 5; c++) {
    if (FINAL) res[c] = *(const u2*)(ws + OFF_X3 + ((size_t)mrow * 640 + (cs * 5 + c) * 16 + 4 * g) * 2);
    else res[c] = *(const u2*)(XB + c * 2048);
  }
  SCHED_FENCE();
  f4 acc[5][4];
#pragma unroll
  for (int c = 0; c < 5; c++)
#pragma unroll
    for (int m = 0; m < 4; m++) acc[c][m] = f4{0.f, 0.f, 0.f, 0.f};
#pragma unroll
  for (int s = 0; s < 12; s++) {
#pragma unroll
    for (int c = 0; c < 5; c++) {
#pragma unroll
      for (int m = 0; m < 4; m++)
        acc[c][m] = __builtin_amdgcn_mfma_f32_16x16x32_bf16(wf[s % PF][c], hf[s % (PF + 1)][m], acc[c][m], 0, 0, 0);
      if (s + PF < 12) {
        if (c < 4) hf[(s + PF) % (PF + 1)][c] = Hm[c][(s + PF) * 64];
        wf[s % PF][c] = Wg[(c * H_TS + s + PF) * 64];
      }
      SCHED_FENCE();
    }
  }
  TSTAMP(KID, 3);
#pragma unroll
  for (int m = 1; m < 4; m++)
#pragma unroll
    for (int c = 0; c < 5; c++) red[((((m + wv) & 3) * 3 + m - 1) * 5 + c) * 64 + lane] = acc[c][m];
  __syncthreads();
  f4 own[5];
#pragma unroll
  for (int c = 0; c < 5; c++) own[c] = acc[c][0];
#pragma unroll
  for (int i = 0; i < 3; i++)
#pragma unroll
    for (int c = 0; c < 5; c++) own[c] += red[((wv * 3 + i) * 5 + c) * 64 + lane];
  float s = 0.f;
#pragma unroll
  for (int c = 0; c < 5; c++) {
    f4 o = f4{bf_lo(res[c].x), bf_hi(res[c].x), bf_lo(res[c].y), bf_hi(res[c].y)} + own[c];
    u2 pk = u2{pack_bf(o.x, o.y), pack_bf(o.z, o.w)};
    if (FINAL) {
      *(u2*)((uint16_t*)out + (size_t)mrow * 640 + (cs * 5 + c) * 16 + 4 * g) = pk;
    } else {
      *(u2*)(XB + c * 2048) = pk;
      unsigned p0 = (cs == 0 && c == 0 && g == 0) ? (pk.x & 0xffff0000u) : pk.x;
      s = dot2(p0, s);
      s = dot2(pk.y, s);
    }
  }
  if (!FINAL) {
    s += __shfl_xor(s, 16);
    s += __shfl_xor(s, 32);
    if (g == 0) unsafeAtomicAdd((double*)(ws + OFF_SSQ) + mrow, (double)s);
  }
  TSTAMP(KID, 7);
}

extern "C" void kernel_launch(void* const* d_in, const int* in_sizes, int n_in, void* d_out, int out_size, void* d_ws,
                              size_t ws_size, hipStream_t stream) {
  const uint16_t* x = (const uint16_t*)d_in[0];
  const uint16_t* wr = (const uint16_t*)d_in[1];
  const uint16_t* wi = (const uint16_t*)d_in[2];
  const uint16_t* w1 = (const uint16_t*)d_in[4];
  const uint16_t* w2 = (const uint16_t*)d_in[5];
  char* ws = (char*)d_ws;
  __hip_bfloat16* out = (__hip_bfloat16*)d_out;
  (void)hipFuncSetAttribute((const void*)k_colfilter<true>, hipFuncAttributeMaxDynamicSharedMemorySize, LDS_CF);
  (void)hipFuncSetAttribute((const void*)k_colfilter<false>, hipFuncAttributeMaxDynamicSharedMemorySize, LDS_CF);
  (void)hipFuncSetAttribute((const void*)k_gemm1<false>, hipFuncAttributeMaxDynamicSharedMemorySize, LDS_G1);
  (void)hipFuncSetAttribute((const void*)k_gemm1<true>, hipFuncAttributeMaxDynamicSharedMemorySize, LDS_G1I);
#define NK 7
  k_prep<<<576, 512, 0, stream>>>(x, wr, wi, w1, ws);
  if (NK > 1) k_colfilter<true><<<256, 256, LDS_CF, stream>>>(w2, ws);
  if (NK > 2) k_gemm1<false><<<256, 256, LDS_G1, stream>>>(ws);
  if (NK > 3) k_gemm2<false><<<256, 256, 0, stream>>>(ws, out);
  if (NK > 4) k_colfilter<false><<<256, 256, LDS_CF, stream>>>(w2, ws);
  if (NK > 5) k_gemm1<true><<<256, 256, LDS_G1I, stream>>>(ws);
  if (NK > 6) k_gemm2<true><<<256, 256, 0, stream>>>(ws, out);
}
```

```cpp
#pragma clang diagnostic ignored "-Warray-bounds"
#include <hip/hip_runtime.h>
#include <hip/hip_bf16.h>
#include <stdint.h>

typedef float c2 __attribute__((ext_vector_type(2)));
typedef float f4 __attribute__((ext_vector_type(4)));
typedef unsigned u2 __attribute__((ext_vector_type(2)));
typedef unsigned u4 __attribute__((ext_vector_type(4)));
typedef __bf16 bf2 __attribute__((ext_vector_type(2)));
typedef __bf16 bf8 __attribute__((ext_vector_type(8)));

#define DEV __device__ __forceinline__
#define SCHED_FENCE() __builtin_amdgcn_sched_barrier(0)

constexpr int S_ = 2048, D_ = 640, F_ = 1536, DF_ = 321;
constexpr float LN_EPS = 1e-5f;

constexpr int XB_MS = 81920 + 512;
constexpr int H_TS = 49;
constexpr int W1_TS = 21;
constexpr size_t OFF_XB = 0;
constexpr size_t OFF_H = OFF_XB + 32ull * XB_MS;
constexpr size_t OFF_W1P = OFF_H + 128ull * H_TS * 1024;
constexpr size_t OFF_W2P = OFF_W1P + 96ull * W1_TS * 1024;
constexpr size_t OFF_W2C = OFF_W2P + 40ull * H_TS * 1024;
constexpr size_t OFF_WT = OFF_W2C + 40ull * H_TS * 1024;
constexpr size_t OFF_X3 = OFF_WT + 160ull * 4 * 256 * 16;
constexpr size_t OFF_RS0 = OFF_X3 + 2048ull * 640 * 2;
constexpr size_t OFF_SSQ = OFF_RS0 + 2048 * 4;

#define TSTAMP(K, P) do {} while (0)
#define PT_DECL do {} while (0)
#define PT(i) do {} while (0)
#define PT_FLUSH(K) do {} while (0)
#define ST_AUX 16
#define AUX_X3 16
#define AUX_G2 16
#define AUX_K0 16
#define AUX_W1 16
#define AUX_CF 16
typedef __amdgpu_buffer_rsrc_t rsrc_t;
DEV rsrc_t make_rsrc(const void* p, unsigned bytes) { return __builtin_amdgcn_make_buffer_rsrc((void*)p, 0, bytes, 0x00020000); }
DEV bf8 bload(rsrc_t rs, unsigned voff, int OFF) {
  return __builtin_bit_cast(bf8, __builtin_amdgcn_raw_buffer_load_b128(rs, voff + (OFF & 4095), OFF & ~4095, 0));
}
DEV c2 add_mi(c2 a, c2 b) {
  c2 d; asm("v_pk_add_f32 %0, %1, %2 op_sel:[0,1] op_sel_hi:[1,0] neg_hi:[0,1]" : "=v"(d) : "v"(a), "v"(b)); return d;
}
DEV c2 add_pi(c2 a, c2 b) {
  c2 d; asm("v_pk_add_f32 %0, %1, %2 op_sel:[0,1] op_sel_hi:[1,0] neg_lo:[0,1]" : "=v"(d) : "v"(a), "v"(b)); return d;
}
DEV c2 cmul(c2 a, c2 w) {
  c2 t, d;
  asm("v_pk_mul_f32 %0, %1, %2 op_sel:[0,0] op_sel_hi:[0,1]" : "=v"(t) : "v"(a), "v"(w));
  asm("v_pk_fma_f32 %0, %1, %2, %3 op_sel:[1,1,0] op_sel_hi:[1,0,1] neg_lo:[0,1,0]" : "=v"(d) : "v"(a), "v"(w), "v"(t));
  return d;
}
DEV c2 cmulc(c2 a, c2 w) {
  c2 t, d;
  asm("v_pk_mul_f32 %0, %1, %2 op_sel:[0,0] op_sel_hi:[0,1] neg_hi:[0,1]" : "=v"(t) : "v"(a), "v"(w));
  asm("v_pk_fma_f32 %0, %1, %2, %3 op_sel:[1,1,0] op_sel_hi:[1,0,1]" : "=v"(d) : "v"(a), "v"(w), "v"(t));
  return d;
}
template <int SGN> DEV c2 twmul(c2 a, c2 w) { return SGN < 0 ? cmul(a, w) : cmulc(a, w); }
template <int SGN> DEV c2 add_rot(c2 a, c2 b) { return SGN < 0 ? add_mi(a, b) : add_pi(a, b); }
template <int SGN> DEV c2 sub_rot(c2 a, c2 b) { return SGN < 0 ? add_pi(a, b) : add_mi(a, b); }
DEV c2 twiddle_rev(float rev) {
  c2 t = c2{__builtin_amdgcn_cosf(rev), -__builtin_amdgcn_sinf(rev)};
  asm("s_nop 1" : "+v"(t));
  return t;
}
template <int N> DEV void tw_powers(c2* t, c2 w) {
  t[0] = w;
#pragma unroll
  for (int k = 2; k <= N; k++) {
    if ((k & 1) == 0) t[k - 1] = cmul(t[k / 2 - 1], t[k / 2 - 1]);
    else {
      int hi = k >= 8 ? 8 : (k >= 4 ? 4 : 2);
      t[k - 1] = cmul(t[hi - 1], t[k - hi - 1]);
    }
  }
}

template <int SGN> DEV void dft4(c2& a, c2& b, c2& c, c2& d) {
  c2 s0 = a + c, s1 = a - c, s2 = b + d, s3 = b - d;
  a = s0 + s2; c = s0 - s2;
  b = add_rot<SGN>(s1, s3); d = sub_rot<SGN>(s1, s3);
}
template <int SGN> DEV c2 rot1(c2 a) { return add_rot<SGN>(a, a) * 0.70710678118654752f; }
template <int SGN> DEV c2 rot3(c2 a) { return sub_rot<SGN>(a, a) * -0.70710678118654752f; }

template <int SGN> DEV void dft8(c2* v) {
  c2 e0 = v[0], e1 = v[2], e2 = v[4], e3 = v[6], o0 = v[1], o1 = v[3], o2 = v[5], o3 = v[7];
  dft4<SGN>(e0, e1, e2, e3);
  dft4<SGN>(o0, o1, o2, o3);
  o1 = rot1<SGN>(o1); o3 = rot3<SGN>(o3);
  v[0] = e0 + o0; v[4] = e0 - o0;
  v[1] = e1 + o1; v[5] = e1 - o1;
  v[2] = add_rot<SGN>(e2, o2); v[6] = sub_rot<SGN>(e2, o2);
  v[3] = e3 + o3; v[7] = e3 - o3;
}
template <int SGN> DEV void dft16(c2* v) {
  const c2 w1 = c2{0.92387953251128674f, -0.38268343236508977f};
  const c2 w3 = c2{0.38268343236508977f, -0.92387953251128674f};
  c2 y[4][4];
#pragma unroll
  for (int i = 0; i < 4; i++) {
    c2 a = v[i], b = v[i + 4], c = v[i + 8], d = v[i + 12];
    dft4<SGN>(a, b, c, d);
    y[i][0] = a; y[i][1] = b; y[i][2] = c; y[i][3] = d;
  }
  y[1][1] = twmul<SGN>(y[1][1], w1);
  y[1][2] = rot1<SGN>(y[1][2]);
  y[1][3] = twmul<SGN>(y[1][3], w3);
  y[2][1] = rot1<SGN>(y[2][1]);
  y[2][3] = rot3<SGN>(y[2][3]);
  y[3][1] = twmul<SGN>(y[3][1], w3);
  y[3][2] = rot3<SGN>(y[3][2]);
  y[3][3] = -twmul<SGN>(y[3][3], w1);
#pragma unroll
  for (int q = 0; q < 4; q++) {
    c2 a = y[0][q], b = y[1][q], c = y[2][q], d = y[3][q];
    if (q == 2) {
      c2 s0 = add_rot<SGN>(a, c), s1 = sub_rot<SGN>(a, c), s2 = b + d, s3 = b - d;
      a = s0 + s2; c = s0 - s2;
      b = add_rot<SGN>(s1, s3); d = sub_rot<SGN>(s1, s3);
    } else {
      dft4<SGN>(a, b, c, d);
    }
    v[q] = a; v[q + 4] = b; v[q + 8] = c; v[q + 12] = d;
  }
}
template <int SGN> DEV void twmulp(c2& a0, c2 w0, c2& a1, c2 w1) {
  c2 d0, d1;
  if (SGN < 0)
    asm("v_pk_mul_f32 %0, %2, %4 op_sel:[0,0] op_sel_hi:[0,1]\n\t"
        "v_pk_mul_f32 %1, %3, %5 op_sel:[0,0] op_sel_hi:[0,1]\n\t"
        "v_pk_fma_f32 %0, %2, %4, %0 op_sel:[1,1,0] op_sel_hi:[1,0,1] neg_lo:[0,1,0]\n\t"
        "v_pk_fma_f32 %1, %3, %5, %1 op_sel:[1,1,0] op_sel_hi:[1,0,1] neg_lo:[0,1,0]"
        : "=&v"(d0), "=&v"(d1) : "v"(a0), "v"(a1), "v"(w0), "v"(w1));
  else
    asm("v_pk_mul_f32 %0, %2, %4 op_sel:[0,0] op_sel_hi:[0,1] neg_hi:[0,1]\n\t"
        "v_pk_mul_f32 %1, %3, %5 op_sel:[0,0] op_sel_hi:[0,1] neg_hi:[0,1]\n\t"
        "v_pk_fma_f32 %0, %2, %4, %0 op_sel:[1,1,0] op_sel_hi:[1,0,1]\n\t"
        "v_pk_fma_f32 %1, %3, %5, %1 op_sel:[1,1,0] op_sel_hi:[1,0,1]"
        : "=&v"(d0), "=&v"(d1) : "v"(a0), "v"(a1), "v"(w0), "v"(w1));
  a0 = d0; a1 = d1;
}
template <int SGN, typename PRE, typename POST> DEV void dft16s(c2* v, PRE pre, POST post) {
  const c2 w1 = c2{0.92387953251128674f, -0.38268343236508977f};
  const c2 w3 = c2{0.38268343236508977f, -0.92387953251128674f};
  const c2 w9 = c2{-0.92387953251128674f, 0.38268343236508977f};
  c2 y[4][4];
#pragma unroll
  for (int i = 0; i < 4; i++) {
    pre(i);
    c2 a = v[i], b = v[i + 4], c = v[i + 8], d = v[i + 12];
    dft4<SGN>(a, b, c, d);
    y[i][0] = a; y[i][1] = b; y[i][2] = c; y[i][3] = d;
    if (i == 1) { twmulp<SGN>(y[1][1], w1, y[1][3], w3); y[1][2] = rot1<SGN>(y[1][2]); }
    if (i == 2) { y[2][1] = rot1<SGN>(y[2][1]); y[2][3] = rot3<SGN>(y[2][3]); }
    if (i == 3) { twmulp<SGN>(y[3][1], w3, y[3][3], w9); y[3][2] = rot3<SGN>(y[3][2]); }
    SCHED_FENCE();
  }
#pragma unroll
  for (int q = 0; q < 4; q++) {
    c2 a = y[0][q], b = y[1][q], c = y[2][q], d = y[3][q];
    if (q == 2) {
      c2 s0 = add_rot<SGN>(a, c), s1 = sub_rot<SGN>(a, c), s2 = b + d, s3 = b - d;
      a = s0 + s2; c = s0 - s2;
      b = add_rot<SGN>(s1, s3); d = sub_rot<SGN>(s1, s3);
    } else {
      dft4<SGN>(a, b, c, d);
    }
    v[q] = a; v[q + 4] = b; v[q + 8] = c; v[q + 12] = d;
    post(q);
    SCHED_FENCE();
  }
}
template <int SGN> DEV void dft5(c2* x) {
  const float c1 = 0.30901699437494742f, c2_ = -0.80901699437494742f;
  const float s1 = 0.95105651629515357f, s2 = 0.58778525229247313f;
  c2 t1 = x[1] + x[4], t2 = x[2] + x[3], t3 = x[1] - x[4], t4 = x[2] - x[3];
  c2 a1 = x[0] + c1 * t1 + c2_ * t2, a2 = x[0] + c2_ * t1 + c1 * t2;
  c2 b1 = s1 * t3 + s2 * t4, b2 = s2 * t3 - s1 * t4;
  x[0] = x[0] + t1 + t2;
  x[1] = add_rot<SGN>(a1, b1); x[4] = sub_rot<SGN>(a1, b1);
  x[2] = add_rot<SGN>(a2, b2); x[3] = sub_rot<SGN>(a2, b2);
}

DEV void lds_fence() { asm volatile("" ::: "memory"); __builtin_amdgcn_wave_barrier(); asm volatile("" ::: "memory"); }
DEV float bf_lo(unsigned u) { return __builtin_bit_cast(float, u << 16); }
DEV float bf_hi(unsigned u) { return __builtin_bit_cast(float, u & 0xffff0000u); }
DEV unsigned pack_bf(float a, float b) { return __builtin_bit_cast(unsigned, __builtin_convertvector(c2{a, b}, bf2)); }

template <int SGN> DEV void twmul2(c2& a0, c2& a1, c2 w) {
  c2 d0, d1;
  if (SGN < 0)
    asm("v_pk_mul_f32 %0, %2, %4 op_sel:[0,0] op_sel_hi:[0,1]\n\t"
        "v_pk_mul_f32 %1, %3, %4 op_sel:[0,0] op_sel_hi:[0,1]\n\t"
        "v_pk_fma_f32 %0, %2, %4, %0 op_sel:[1,1,0] op_sel_hi:[1,0,1] neg_lo:[0,1,0]\n\t"
        "v_pk_fma_f32 %1, %3, %4, %1 op_sel:[1,1,0] op_sel_hi:[1,0,1] neg_lo:[0,1,0]"
        : "=&v"(d0), "=&v"(d1) : "v"(a0), "v"(a1), "v"(w));
  else
    asm("v_pk_mul_f32 %0, %2, %4 op_sel:[0,0] op_sel_hi:[0,1] neg_hi:[0,1]\n\t"
        "v_pk_mul_f32 %1, %3, %4 op_sel:[0,0] op_sel_hi:[0,1] neg_hi:[0,1]\n\t"
        "v_pk_fma_f32 %0, %2, %4, %0 op_sel:[1,1,0] op_sel_hi:[1,0,1]\n\t"
        "v_pk_fma_f32 %1, %3, %4, %1 op_sel:[1,1,0] op_sel_hi:[1,0,1]"
        : "=&v"(d0), "=&v"(d1) : "v"(a0), "v"(a1), "v"(w));
  a0 = d0; a1 = d1;
}
DEV void tangle2(c2& d0, c2& d1, c2 A0, c2 B0, c2 A1, c2 B1, c2 w) {
  c2 e0, e1, o0, o1;
  asm("v_pk_add_f32 %0, %4, %5 neg_hi:[0,1]\n\t"
      "v_pk_add_f32 %1, %6, %7 neg_hi:[0,1]\n\t"
      "v_pk_add_f32 %2, %4, %5 neg_lo:[0,1]\n\t"
      "v_pk_add_f32 %3, %6, %7 neg_lo:[0,1]\n\t"
      "v_pk_fma_f32 %0, %2, %8, %0 op_sel:[0,0,0] op_sel_hi:[0,1,1]\n\t"
      "v_pk_fma_f32 %1, %3, %8, %1 op_sel:[0,0,0] op_sel_hi:[0,1,1]\n\t"
      "v_pk_fma_f32 %0, %2, %8, %0 op_sel:[1,1,0] op_sel_hi:[1,0,1] neg_lo:[0,1,0]\n\t"
      "v_pk_fma_f32 %1, %3, %8, %1 op_sel:[1,1,0] op_sel_hi:[1,0,1] neg_lo:[0,1,0]"
      : "=&v"(e0), "=&v"(e1), "=&v"(o0), "=&v"(o1) : "v"(A0), "v"(B0), "v"(A1), "v"(B1), "v"(w));
  d0 = e0; d1 = e1;
}

constexpr int SCR_F4 = 361;
struct Tw320 { c2 a[4]; c2 b[7]; };
DEV void tw320_init(Tw320& tw, int lane, c2 u0, float scale) {
  tw_powers<4>(tw.a, cmul(u0, u0));
  tw_powers<7>(tw.b, twiddle_rev((float)(lane & 7) * (1.0f / 64.0f)));
#pragma unroll
  for (int i = 0; i < 7; i++) tw.b[i] = tw.b[i] * scale;
}
#define FFT_HOOK_NONE(i)
template <int SGN, typename HOOK> DEV void fft320x2(c2 (*z)[5], const Tw320& tw, float scale, f4* scr, int lane, c2 (*o)[8], HOOK hook) {
  const int u = lane < 40 ? lane : 39;
  const int q = u >> 3, a = u & 7;
  dft5<SGN>(z[0]); dft5<SGN>(z[1]);
#pragma unroll
  for (int i = 1; i < 5; i++) twmul2<SGN>(z[0][i], z[1][i], tw.a[i - 1]);
  lds_fence();
#pragma unroll
  for (int i = 0; i < 5; i++) scr[i * 72 + lane] = f4{z[0][i].x, z[0][i].y, z[1][i].x, z[1][i].y};
  lds_fence();
#pragma unroll
  for (int b = 0; b < 8; b++) { f4 t = scr[q * 72 + a + 8 * b]; o[0][b] = t.xy; o[1][b] = t.zw; }
  SCHED_FENCE();
  hook(0);
  dft8<SGN>(o[0]); dft8<SGN>(o[1]);
  hook(1);
  o[0][0] = o[0][0] * scale; o[1][0] = o[1][0] * scale;
#pragma unroll
  for (int c = 1; c < 8; c++) twmul2<SGN>(o[0][c], o[1][c], tw.b[c - 1]);
  lds_fence();
#pragma unroll
  for (int c = 0; c < 8; c++) scr[(q * 8 + c) * 9 + a] = f4{o[0][c].x, o[0][c].y, o[1][c].x, o[1][c].y};
  lds_fence();
#pragma unroll
  for (int i = 0; i < 8; i++) { f4 t = scr[u * 9 + i]; o[0][i] = t.xy; o[1][i] = t.zw; }
  SCHED_FENCE();
  hook(2);
  dft8<SGN>(o[0]);
  hook(3);
  dft8<SGN>(o[1]);
  lds_fence();
}
constexpr float FWD_SC = 0.5f * 0.05590169943749474f;
DEV void rfft2_rows(const unsigned* r0, const unsigned* r1, const Tw320& tw, f4* scr, int lane, c2 (*o)[8]) {
  c2 z[2][5];
#pragma unroll
  for (int j = 0; j < 5; j++) { z[0][j] = c2{bf_lo(r0[j]), bf_hi(r0[j])}; z[1][j] = c2{bf_lo(r1[j]), bf_hi(r1[j])}; }
  fft320x2<-1>(z, tw, FWD_SC, scr, lane, o, [](int) {});
  const int u = lane < 40 ? lane : 39;
  f4* p = scr + (u >> 3) + 5 * (u & 7);
#pragma unroll
  for (int e = 0; e < 8; e++) p[40 * e] = f4{o[0][e].x, o[0][e].y, o[1][e].x, o[1][e].y};
}
DEV void untangle2(c2& x0, c2& x1, f4 A, f4 B, c2 w, bool k_is_0) {
  tangle2(x0, x1, A.xy, B.xy, A.zw, B.zw, w);
  if (k_is_0) {
    const float r = 1.41421356237309505f;
    x0 = c2{(A.x + A.y) * r, (A.x - A.y) * r};
    x1 = c2{(A.z + A.w) * r, (A.z - A.w) * r};
  }
}
DEV c2 mul_mi(c2 w) { return c2{w.y, -w.x}; }

template <int CTRL> DEV float dpp_add(float v) {
  return v + __builtin_bit_cast(float, __builtin_amdgcn_update_dpp(0, __builtin_bit_cast(int, v), CTRL, 0xf, 0xf, true));
}
DEV float wave_sum63(float v) {
  v = dpp_add<0x111>(v);
  v = dpp_add<0x112>(v);
  v = dpp_add<0x114>(v);
  v = dpp_add<0x118>(v);
  v = v + __builtin_bit_cast(float, __builtin_amdgcn_update_dpp(0, __builtin_bit_cast(int, v), 0x142, 0xa, 0xf, true));
  v = v + __builtin_bit_cast(float, __builtin_amdgcn_update_dpp(0, __builtin_bit_cast(int, v), 0x143, 0xc, 0xf, true));
  return v;
}

#define CODE_ADDR(A, SYM) asm("s_getpc_b64 vcc\n\ts_add_u32 vcc_lo, vcc_lo, " SYM "@rel32@lo+4\n\ts_addc_u32 vcc_hi, vcc_hi, " SYM "@rel32@hi+12\n\ts_mov_b64 %0, vcc" \
                              : "=s"(A) :: "vcc", "scc")
#define CODE_PF(V, SYM, N) do { const char *b_, *e_; CODE_ADDR(b_, SYM); CODE_ADDR(e_, N); \
    if ((long)(threadIdx.x * 64 + 4) <= e_ - b_) V = *(const __attribute__((address_space(1))) unsigned*)(b_ + threadIdx.x * 64); } while (0)
#define SYM_K1 "_Z11k_colfilterILb1EEvPKtPc"
#define SYM_K4 "_Z11k_colfilterILb0EEvPKtPc"
#define SYM_K2 "_Z7k_gemm1ILb0EEvPc"
#define SYM_K5 "_Z7k_gemm1ILb1EEvPc"
#define SYM_K3 "_Z7k_gemm2ILb0EEvPcP14__hip_bfloat16"
#define SYM_K6 "_Z7k_gemm2ILb1EEvPcP14__hip_bfloat16"
#define CODE_PF_K1(V) CODE_PF(V, SYM_K1, SYM_K4)
#define CODE_PF_K4(V) CODE_PF(V, SYM_K4, SYM_K2)
#define CODE_PF_K2(V) CODE_PF(V, SYM_K2, SYM_K5)
#define CODE_PF_K5(V) CODE_PF(V, SYM_K5, SYM_K3)
#define CODE_PF_K3(V) CODE_PF(V, SYM_K3, SYM_K6)
#define CODE_PF_K6(V) CODE_PF(V, SYM_K6, "k6_code_end")

DEV int frag_f(int T, int g, int j) {
  int G = T / 3, r = T - 3 * G;
  int ta = r == 0 ? 0 : (r == 1 ? 3 : 2), tb = r == 0 ? 1 : (r == 1 ? 4 : 5);
  return 96 * G + 16 * (j < 4 ? ta : tb) + 4 * g + (j & 3);
}

constexpr int K0_X = 128, K0_W1 = 96, K0_WT = 128;
template <bool is_x>
DEV void prep_rows(const uint16_t* __restrict__ x, const uint16_t* __restrict__ w1, char* __restrict__ ws, f4* scr_all, u2* stage, int grp) {
  const int tid = threadIdx.x, lane = tid & 63, wv = tid >> 6;
  f4* scr = scr_all + wv * SCR_F4;
  unsigned r0[5], r1[5];
  u4 s0[2], s1[2];
  if (is_x) {
    const unsigned* row = (const unsigned*)(x + (size_t)(16 * grp + 2 * wv) * D_);
#pragma unroll
    for (int j = 0; j < 5; j++) { r0[j] = row[lane + 64 * j]; r1[j] = row[320 + lane + 64 * j]; }
    if (tid < 16) ((double*)(ws + OFF_SSQ))[16 * grp + tid] = 0.0;
  } else {
#pragma unroll
    for (int it = 0; it < 2; it++) {
      int n = it == 0 ? tid : 512 + (tid & 127);
      const uint16_t* p = w1 + (size_t)(n >> 1) * (2 * F_) + grp * 16 + (n & 1) * 8;
      s0[it] = *(const u4*)p; s1[it] = *(const u4*)(p + F_);
    }
  }
  SCHED_FENCE();
  Tw320 tw;
  tw320_init(tw, lane, twiddle_rev((float)lane * (1.0f / 640.0f)), FWD_SC);
  c2 wo[3][2];
  wo[0][0] = mul_mi(twiddle_rev((float)(tid >> 3) * (1.0f / 320.0f)));
  wo[1][0] = cmul(wo[0][0], c2{0.30901699437494742f, -0.95105651629515357f});
  wo[2][0] = cmul(wo[0][0], c2{-0.80901699437494742f, -0.58778525229247313f});
#pragma unroll
  for (int it = 0; it < 3; it++) wo[it][1] = cmul(wo[it][0], c2{0.99995180895932817f, -0.00981731933714962f});
  SCHED_FENCE();
  if (!is_x) {
#pragma unroll
    for (int it = 0; it < 2; it++) {
      int n = tid + 512 * it;
      if (it == 0 || tid < 128) {
#pragma unroll
        for (int i = 0; i < 4; i++)
          stage[((n & 1) * 4 + i) * 324 + (n >> 1)] = u2{__builtin_amdgcn_perm(s1[it][i], s0[it][i], 0x05040100u), __builtin_amdgcn_perm(s1[it][i], s0[it][i], 0x07060302u)};
      }
    }
    __syncthreads();
#pragma unroll
    for (int j = 0; j < 5; j++) { u2 t = stage[wv * 324 + lane + 64 * j]; r0[j] = t.x; r1[j] = t.y; }
  }
  TSTAMP(0, 1);
  c2 o[2][8];
  rfft2_rows(r0, r1, tw, scr, lane, o);
  if (is_x) {
    float ss[2];
#pragma unroll
    for (int rr = 0; rr < 2; rr++) {
      c2 t = o[rr][0];
      if (lane == 0) t = c2{(t.x - t.y) * 0.70710678118654752f, 0.f};
      c2 acc = t * t;
#pragma unroll
      for (int e = 1; e < 8; e++) acc += o[rr][e] * o[rr][e];
      ss[rr] = wave_sum63(lane < 40 ? acc.x + acc.y : 0.f);
    }
    if (lane == 63)
      *(c2*)(ws + OFF_RS0 + (size_t)(16 * grp + 2 * wv) * 4) =
          c2{__builtin_amdgcn_rsqf(ss[0] * (1.0f / 160.0f) + LN_EPS), __builtin_amdgcn_rsqf(ss[1] * (1.0f / 160.0f) + LN_EPS)} * (1.0f / 2048.0f);
  }
  TSTAMP(0, 2);
  __syncthreads();
  const int rp = tid & 7;
  const f4* S = scr_all + rp * SCR_F4;
#pragma unroll
  for (int it = 0; it < 3; it++) {
    if (it == 2 && tid >= 256) break;
    const int q4 = (tid >> 3) + 64 * it;
    f4 A0 = S[2 * q4], A1 = S[2 * q4 + 1], B0 = S[(it == 0 && q4 == 0) ? 0 : 320 - 2 * q4], B1 = S[319 - 2 * q4];
    c2 x00, x10, x01, x11;
    untangle2(x00, x10, A0, B0, wo[it][0], it == 0 && q4 == 0);
    untangle2(x01, x11, A1, B1, wo[it][1], false);
    if (is_x) {
      u4 v = u4{pack_bf(x00.x, x00.y), pack_bf(x01.x, x01.y), pack_bf(x10.x, x10.y), pack_bf(x11.x, x11.y)};
      __builtin_amdgcn_raw_buffer_store_b128(v, make_rsrc(ws + OFF_XB + (size_t)(grp >> 2) * XB_MS, XB_MS), q4 * 512 + (((grp & 3) * 128 + rp * 16) ^ (((q4 >> 1) & 1) << 7)), 0, AUX_K0);
    } else {
      if (it == 0 && q4 == 0) { x00.x = 0.f; x10.x = 0.f; }
      const rsrc_t Wr = make_rsrc(ws + OFF_W1P + (size_t)grp * W1_TS * 1024, W1_TS * 1024);
      const unsigned vo = ((q4 >> 1) * 16 + 2 * rp) * 16 + (q4 & 1) * 8;
      __builtin_amdgcn_raw_buffer_store_b64(u2{pack_bf(x00.x, x00.y), pack_bf(x01.x, x01.y)}, Wr, vo, 0, AUX_W1);
      __builtin_amdgcn_raw_buffer_store_b64(u2{pack_bf(x10.x, x10.y), pack_bf(x11.x, x11.y)}, Wr, vo + 16, 0, AUX_W1);
    }
  }
  TSTAMP(0, 7);
}

__global__ __launch_bounds__(512) void k_prep(const uint16_t* __restrict__ x, const uint16_t* __restrict__ wr,
                                              const uint16_t* __restrict__ wi, const uint16_t* __restrict__ w1,
                                              char* __restrict__ ws) {
  __shared__ __attribute__((aligned(16))) f4 scr_all[8 * SCR_F4];
  __shared__ __attribute__((aligned(16))) u2 stage[8 * 324];
  const int tid = threadIdx.x, lane = tid & 63, wv = tid >> 6;
  const int b = blockIdx.x;
  TSTAMP(0, 0);

  if (b >= K0_X + K0_W1) {
    int bb = b - (K0_X + K0_W1);
    int unit = (bb & 7) * 16 + (bb >> 3);
    int u = unit & 3, bbit = (unit >> 2) & 1, kc = unit >> 3;
    unsigned* t32 = (unsigned*)scr_all;
    unsigned cpf = 0;
    if ((bb >> 3) == 0) CODE_PF_K1(cpf);
    if ((bb >> 3) == 1) CODE_PF_K2(cpf);
    if ((bb >> 3) == 2) CODE_PF_K3(cpf);
    if ((bb >> 3) == 3) CODE_PF_K4(cpf);
    if ((bb >> 3) == 4) CODE_PF_K5(cpf);
    if ((bb >> 3) == 5) CODE_PF_K6(cpf);
    unsigned short re[2][6], im[2][6];
#pragma unroll
    for (int hh = 0; hh < 2; hh++) {
      int ks = kc + 16 * (2 * u + hh + 8 * bbit) + 256 * wv;
      const uint16_t* pr = wr + (size_t)ks * DF_;
      const uint16_t* pi = wi + (size_t)ks * DF_;
#pragma unroll
      for (int i = 0; i < 6; i++) {
        int c = lane + 64 * i;
        c = c < DF_ ? c : DF_ - 1;
        re[hh][i] = pr[c]; im[hh][i] = pi[c];
      }
    }
#pragma unroll
    for (int hh = 0; hh < 2; hh++)
#pragma unroll
      for (int i = 0; i < 6; i++) {
        int c = lane + 64 * i;
        if (c < DF_) t32[(hh * 8 + wv) * 324 + c] = (unsigned)re[hh][i] | ((unsigned)im[hh][i] << 16);
      }
    __syncthreads();
    const rsrc_t WT = make_rsrc(ws + OFF_WT, 160 * 4 * 256 * 16);
    for (int n = tid; n < 1280; n += 512) {
      int col = n & 1, hh = (n >> 1) & 1, ih = (n >> 2) & 1, q4 = n >> 3;
      int cc = 2 * q4 + col;
      if (cc == 0) cc = 320;
      u4 v;
#pragma unroll
      for (int e = 0; e < 4; e++) v[e] = t32[(hh * 8 + ih * 4 + e) * 324 + cc];
      int tau = (2 * u + hh) + 8 * kc;
      __builtin_amdgcn_raw_buffer_store_b128(v, WT, ((q4 * 4 + 2 * bbit + ih) * 256 + 2 * tau + col) * 16, 0, AUX_K0);
    }
    asm volatile("" :: "v"(cpf));
    TSTAMP(0, 7);
    return;
  }

  if (b < K0_X) prep_rows<true>(x, w1, ws, scr_all, stage, (b & 7) * 16 + (b >> 3));
  else prep_rows<false>(x, w1, ws, scr_all, stage, ((b - K0_X) & 7) * 12 + ((b - K0_X) >> 3));
}

constexpr int P1 = 152;
constexpr int LDS_CF = 84 * 1024;
template <bool FIRST>
__global__ __launch_bounds__(256, 1) void k_colfilter(const uint16_t* __restrict__ w2, char* __restrict__ ws) {
  extern __shared__ __attribute__((aligned(16))) char smem[];
  const int tid = threadIdx.x;
  const int xcd = blockIdx.x & 7, slot = blockIdx.x >> 3;
  constexpr int KID = FIRST ? 1 : 4;
  (void)KID;
  if (slot >= 20) {
    TSTAMP(KID, 0);
    const int lane = tid & 63, wv = tid >> 6;
    const int i = (slot - 20) * 8 + xcd;
    const int T = i >> 1, g0 = (i & 1) * 2;
    unsigned pf[6], cpf = 0;
    if (FIRST) { if (slot == 30) CODE_PF_K3(cpf); if (slot == 31) CODE_PF_K4(cpf); }
    else { if (slot == 30) CODE_PF_K5(cpf); if (slot == 31) CODE_PF_K6(cpf); }
    const char* pfbase = ws + OFF_W1P + (size_t)(xcd & 1) * 48 * W1_TS * 1024 + tid * 64;
#define L2_PREFETCH() do { SCHED_FENCE(); _Pragma("unroll") for (int j = 0; j < 6; j++) { int blk = (slot - 20) + 12 * j; blk = blk < 63 ? blk : 62; \
      pf[j] = *(const unsigned*)(pfbase + blk * 16384); } SCHED_FENCE(); } while (0)
    if (FIRST) {
      f4* scr_all = (f4*)smem;
      unsigned raw[4][5];
#pragma unroll
      for (int rr = 0; rr < 4; rr++) {
        int r = wv * 4 + rr;
        const unsigned* row = (const unsigned*)(w2 + (size_t)frag_f(T, g0 + (r >> 3), r & 7) * D_);
#pragma unroll
        for (int j = 0; j < 5; j++) raw[rr][j] = row[lane + 64 * j];
      }
      L2_PREFETCH();
      Tw320 tw;
      tw320_init(tw, lane, twiddle_rev((float)lane * (1.0f / 640.0f)), FWD_SC);
      const int k0 = (tid & 7) + 8 * (tid >> 4), gg = (tid >> 3) & 1;
      c2 wo[3];
      wo[0] = mul_mi(twiddle_rev((float)k0 * (1.0f / 640.0f)));
      wo[1] = cmul(wo[0], c2{0.30901699437494742f, -0.95105651629515357f});
      wo[2] = cmul(wo[0], c2{-0.80901699437494742f, -0.58778525229247313f});
      SCHED_FENCE();
      c2 o[2][8];
      rfft2_rows(raw[0], raw[1], tw, scr_all + (wv * 2) * SCR_F4, lane, o);
      rfft2_rows(raw[2], raw[3], tw, scr_all + (wv * 2 + 1) * SCR_F4, lane, o);
      __syncthreads();
      const f4* S = scr_all + gg * 4 * SCR_F4;
#pragma unroll
      for (int it = 0; it < 3; it++) {
        if (it == 2 && tid >= 128) break;
        const int k = k0 + 128 * it;
        const bool z = it == 0 && k == 0;
        f4 A[4], B[4];
#pragma unroll
        for (int jp = 0; jp < 4; jp++) { A[jp] = S[jp * SCR_F4 + k]; B[jp] = S[jp * SCR_F4 + (z ? 0 : 320 - k)]; }
        u4 re, im;
#pragma unroll
        for (int jp = 0; jp < 4; jp++) {
          c2 x0, x1;
          untangle2(x0, x1, A[jp], B[jp], wo[it], z);
          re[jp] = pack_bf(x0.x, x1.x); im[jp] = pack_bf(x0.y, x1.y);
        }
        const rsrc_t Wr = make_rsrc(ws + OFF_W2P, 40 * H_TS * 1024);
        const unsigned vo = (((k >> 3) * H_TS + T) * 64 + (g0 + gg) * 16 + ((2 * k) & 15)) * 16;
        __builtin_amdgcn_raw_buffer_store_b128(re, Wr, vo, 0, AUX_W1);
        __builtin_amdgcn_raw_buffer_store_b128(im, Wr, vo + 16, 0, AUX_W1);
      }
    } else {
      const rsrc_t W2C = make_rsrc(ws + OFF_W2C, 40 * H_TS * 1024);
      unsigned short v[5][8];
#pragma unroll
      for (int it = 0; it < 5; it++) {
        int n = tid + 256 * it;
        int cl = n & 15, gg = (n >> 4) & 1, ct = n >> 5;
#pragma unroll
        for (int j = 0; j < 8; j++) v[it][j] = w2[(size_t)frag_f(T, g0 + gg, j) * D_ + ct * 16 + cl];
      }
      L2_PREFETCH();
#pragma unroll
      for (int it = 0; it < 5; it++) {
        int n = tid + 256 * it;
        int cl = n & 15, gg = (n >> 4) & 1, ct = n >> 5;
        u4 o;
#pragma unroll
        for (int j = 0; j < 4; j++) o[j] = (unsigned)v[it][2 * j] | ((unsigned)v[it][2 * j + 1] << 16);
        __builtin_amdgcn_raw_buffer_store_b128(o, W2C, ((ct * H_TS + T) * 64 + (g0 + gg) * 16 + cl) * 16, 0, AUX_K0);
      }
    }
    asm volatile("" :: "v"(pf[0]), "v"(pf[1]), "v"(pf[2]), "v"(pf[3]), "v"(pf[4]), "v"(pf[5]), "v"(cpf));
    TSTAMP(KID, 7);
    return;
  }
  typedef __attribute__((address_space(3))) volatile c2* ldsp;
  const int col = tid & 1, tau = tid >> 1;
  const int q4 = xcd * 20 + slot;
  const int lo = tau & 7, kc = tau >> 3;
  PT_DECL; PT(0);
  const rsrc_t XBr = make_rsrc(ws + OFF_XB + q4 * 512, 32 * XB_MS);
  const unsigned xoff = (tau >> 6) * XB_MS + ((((tau & 63) << 1) + col) ^ (((q4 >> 1) & 1) << 5)) * 4;
  unsigned xr[16];
  c2 v[16];
  float rs[16];
  u2 sq[16];
  u4 wt[4];
#pragma unroll
  for (int r = 0; r < 16; r++) xr[r] = __builtin_amdgcn_raw_buffer_load_b32(XBr, xoff, r * (2 * XB_MS), 0);
  {
    const rsrc_t Sr = make_rsrc(ws + OFF_RS0, 2048 * 12);
#pragma unroll
    for (int r = 0; r < 16; r++) {
      if (FIRST) rs[r] = __builtin_bit_cast(float, __builtin_amdgcn_raw_buffer_load_b32(Sr, tau * 4 + (r & 7) * 512, (r >> 3) * 4096, 0));
      else sq[r] = __builtin_amdgcn_raw_buffer_load_b64(Sr, tau * 8 + (r & 3) * 1024, 8192 + (r >> 2) * 4096, 0);
    }
    const rsrc_t Wr = make_rsrc(ws + OFF_WT + (size_t)q4 * 16384, 16384);
#pragma unroll
    for (int i = 0; i < 4; i++) wt[i] = __builtin_amdgcn_raw_buffer_load_b128(Wr, tid * 16, i * 4096, 0);
  }
  SCHED_FENCE();
  c2 tw1[15], tw2[15];
  tw_powers<15>(tw1, twiddle_rev((float)tau * (1.0f / 2048.0f)));
  tw_powers<15>(tw2, twiddle_rev((float)lo * (1.0f / 128.0f)));
  const bool special = (q4 == 0) && (col == 0);
  const unsigned sel_lo = special ? 0x07060c0cu : 0x05040c0cu, sel_hi = special ? 0x0c0c0c0cu : 0x07060c0cu;
  ldsp e1 = (ldsp)(uintptr_t)(unsigned)(uintptr_t)(__attribute__((address_space(3))) char*)smem + tid;
  ldsp e2 = (ldsp)(uintptr_t)(unsigned)(uintptr_t)(__attribute__((address_space(3))) char*)smem + (kc * P1 + lo) * 2 + col;
  ldsp e3 = (ldsp)(uintptr_t)(unsigned)(uintptr_t)(__attribute__((address_space(3))) char*)smem + (kc * P1 + lo * 9) * 2 + col;
  SCHED_FENCE();
  c2 xv[16];
  dft16s<-1>(v,
    [&](int i) {
#pragma unroll
      for (int j = 0; j < 4; j++) {
        const int r = i + 4 * j;
        if (!FIRST) {
          u2 t = sq[r];
          rs[r] = __builtin_amdgcn_rsqf((float)__builtin_bit_cast(double, t) * (2048.0f * 2048.0f / 640.0f) + LN_EPS * 2048.0f * 2048.0f);
        }
        xv[r] = c2{__builtin_bit_cast(float, __builtin_amdgcn_perm(xr[r], 0u, sel_lo)), __builtin_bit_cast(float, __builtin_amdgcn_perm(xr[r], 0u, sel_hi))};
        v[r] = xv[r] * rs[r];
      }
      if (i == 0) PT(1);
    },
    [&](int q) {
      if (q == 0) v[4] = cmul(v[4], tw1[3]); else twmulp<-1>(v[q], tw1[q - 1], v[q + 4], tw1[q + 3]);
      e1[q * (2 * P1)] = v[q]; e1[(q + 4) * (2 * P1)] = v[q + 4];
      twmulp<-1>(v[q + 8], tw1[q + 7], v[q + 12], tw1[q + 11]);
      e1[(q + 8) * (2 * P1)] = v[q + 8]; e1[(q + 12) * (2 * P1)] = v[q + 12];
    });
  __syncthreads();
#pragma unroll
  for (int i = 0; i < 4; i++)
#pragma unroll
    for (int j = 0; j < 4; j++) v[i + 4 * j] = e2[(i + 4 * j) * 16];
  SCHED_FENCE();
  c2 wf[16];
#pragma unroll
  for (int r = 0; r < 16; r++) { unsigned w = wt[r >> 2][r & 3]; wf[r] = c2{bf_lo(w), bf_hi(w)}; }
  SCHED_FENCE();
  dft16s<-1>(v, [&](int) {},
    [&](int q) {
      if (q == 0) v[4] = cmul(v[4], tw2[3]); else twmulp<-1>(v[q], tw2[q - 1], v[q + 4], tw2[q + 3]);
      e2[q * 18] = v[q]; e2[(q + 4) * 18] = v[q + 4];
      twmulp<-1>(v[q + 8], tw2[q + 7], v[q + 12], tw2[q + 11]);
      e2[(q + 8) * 18] = v[q + 8]; e2[(q + 12) * 18] = v[q + 12];
    });
#pragma unroll
  for (int bb = 0; bb < 2; bb++)
#pragma unroll
    for (int sa = 0; sa < 8; sa++) v[bb * 8 + sa] = e3[(bb * 72 + sa) * 2];
  SCHED_FENCE();
  PT(2);
#pragma unroll
  for (int bb = 0; bb < 2; bb++) {
    dft8<-1>(v + 8 * bb);
#pragma unroll
    for (int r = 0; r < 8; r += 2) twmulp<-1>(v[8 * bb + r], wf[8 * bb + r], v[8 * bb + r + 1], wf[8 * bb + r + 1]);
    SCHED_FENCE();
  }
  dft8<1>(v);
  SCHED_FENCE();
  {
    c2* u = v + 8;
    c2 a0 = u[0], a1 = u[2], a2 = u[4], a3 = u[6], o0 = u[1], o1 = u[3], o2 = u[5], o3 = u[7];
    e3[0] = v[0]; e3[2] = v[1];
    dft4<1>(a0, a1, a2, a3);
    SCHED_FENCE();
    e3[4] = v[2]; e3[6] = v[3];
    dft4<1>(o0, o1, o2, o3);
    SCHED_FENCE();
    e3[8] = v[4]; e3[10] = v[5];
    o1 = rot1<1>(o1); o3 = rot3<1>(o3);
    u[0] = a0 + o0; u[4] = a0 - o0;
    u[2] = add_rot<1>(a2, o2); u[6] = sub_rot<1>(a2, o2);
    SCHED_FENCE();
    e3[12] = v[6]; e3[14] = v[7];
    u[1] = a1 + o1; u[5] = a1 - o1;
    u[3] = a3 + o3; u[7] = a3 - o3;
    SCHED_FENCE();
#pragma unroll
    for (int sa = 0; sa < 8; sa++) e3[(72 + sa) * 2] = u[sa];
  }
#pragma unroll
  for (int i = 0; i < 4; i++)
#pragma unroll
    for (int j = 0; j < 4; j++) v[i + 4 * j] = e2[(i + 4 * j) * 18];
  SCHED_FENCE();
  dft16s<1>(v,
    [&](int i) {
      if (i == 0) v[4] = cmulc(v[4], tw2[3]); else twmulp<1>(v[i], tw2[i - 1], v[i + 4], tw2[i + 3]);
      twmulp<1>(v[i + 8], tw2[i + 7], v[i + 12], tw2[i + 11]);
    },
    [&](int q) {
#pragma unroll
      for (int j = 0; j < 4; j++) e2[(q + 4 * j) * 16] = v[q + 4 * j];
    });
  __syncthreads();
#pragma unroll
  for (int i = 0; i < 4; i++)
#pragma unroll
    for (int j = 0; j < 4; j++) v[i + 4 * j] = e1[(i + 4 * j) * (2 * P1)];
  SCHED_FENCE();
  const unsigned sel_o = special ? 0x05040100u : 0x07060504u;
  PT(3);
  dft16s<1>(v,
    [&](int i) {
      if (i == 0) v[4] = cmulc(v[4], tw1[3]); else twmulp<1>(v[i], tw1[i - 1], v[i + 4], tw1[i + 3]);
      twmulp<1>(v[i + 8], tw1[i + 7], v[i + 12], tw1[i + 11]);
    },
    [&](int q) {
#pragma unroll
      for (int j = 0; j < 4; j++) {
        const int r = q + 4 * j;
        c2 o = xv[r] + v[r];
        __builtin_amdgcn_raw_buffer_store_b32(__builtin_amdgcn_perm(pack_bf(o.x, o.y), xr[r], sel_o), XBr, xoff, r * (2 * XB_MS), AUX_CF);
      }
    });
  PT(7); PT_FLUSH(KID);
}

constexpr int G1_A_BYTES = 81920;
constexpr int G1_WSCR = SCR_F4 * 16;
#define G1_PF 4
#define G1_PFI 5
#define G1_VOL volatile
#define G1_LGKM 0xc87f
#define G1_LS 4
#define G1_NP1 42
#define G2_PF 3
#define G2_ND 12
#define G2_WS 2
#define G2_BD 4
template <int I> struct ic { static constexpr int value = I; };
template <int N, int I = 0, typename F> DEV void static_for(F f) { if constexpr (I < N) { f(ic<I>{}); static_for<N, I + 1>(f); } }
constexpr int LDS_G1 = G1_A_BYTES + 512;
constexpr int LDS_G1I = LDS_G1 + 4 * G1_WSCR;
DEV float dot2(unsigned a, float c) {
  bf2 q = __builtin_bit_cast(bf2, a);
  return __builtin_amdgcn_fdot2_f32_bf16(q, q, c, false);
}
template <bool WITH_INV>
__global__ __launch_bounds__(256, 1) void k_gemm1(char* __restrict__ ws) {
  extern __shared__ __attribute__((aligned(16))) char smem[];
  const int tid = threadIdx.x, lane = tid & 63;
  const int wv = __builtin_amdgcn_readfirstlane(tid >> 6);
  const int b = blockIdx.x;
  const int xcd = b & 7, slot = b >> 3;
  const int mblk = (xcd >> 1) * 8 + (slot & 7), fs = (xcd & 1) * 4 + (slot >> 3);
  constexpr int KID = WITH_INV ? 5 : 2;
  (void)KID;
  asm volatile("" ::: "a191");
  PT_DECL; PT(0);
  const char* Ag = ws + OFF_XB + (size_t)mblk * XB_MS;
  const unsigned lds0 = (unsigned)(uintptr_t)(__attribute__((address_space(3))) char*)smem + wv * 20480;
  const unsigned l16 = lane * 16;
#define G1_DMA(I) do { unsigned keep_; asm volatile( \
          "s_mov_b32 %0, m0\n\ts_mov_b32 m0, %3\n\ts_nop 0\n\t" \
          "global_load_lds_dwordx4 %1, %2\n\t" \
          "global_load_lds_dwordx4 %1, %2 offset:1024\n\t" \
          "global_load_lds_dwordx4 %1, %2 offset:2048\n\t" \
          "global_load_lds_dwordx4 %1, %2 offset:3072\n\t" \
          "s_mov_b32 m0, %0" \
          : "=&s"(keep_) : "v"(l16), "s"(Ag + wv * 20480 + (I) * 4096), "s"(lds0 + (I) * 4096) : "memory"); SCHED_FENCE(); } while (0)
  const rsrc_t Wg = make_rsrc(ws + OFF_W1P + (size_t)(fs * 12 + wv * 3) * W1_TS * 1024, 3 * W1_TS * 1024);
  constexpr int PF = WITH_INV ? G1_PFI : G1_PF;
  constexpr int NL = 3 * PF;
  bf8 wf[60];
#define G1_LOAD1(I) do { if ((I) < 60) wf[(I) < 60 ? (I) : 0] = bload(Wg, l16, ((I) / 20 * W1_TS + (I) % 20) * 1024); } while (0)
#define G1_LOADW(S) do { if ((S) < PF) { G1_LOAD1(3 * (S)); G1_LOAD1(3 * (S) + 1); G1_LOAD1(3 * (S) + 2); SCHED_FENCE(); } } while (0)
  unsigned pk3[2][8];
  const int m0 = fs * 8 + 2 * wv;
  const int iu = lane < 40 ? lane : 39;
  const int iq = iu >> 3, ia = iu & 7;
  if (!WITH_INV) {
    G1_DMA(0); G1_DMA(1); G1_DMA(2); G1_DMA(3); G1_DMA(4);
#pragma unroll
    for (int s = 0; s < PF; s++) G1_LOADW(s);
    PT(6);
  } else {
    f4* scr = (f4*)(smem + LDS_G1 + wv * G1_WSCR);
    u2* buf = (u2*)scr;
    u4 rw[3];
#pragma unroll
    for (int i = 0; i < 3; i++) {
      int q = lane + 64 * i;
      if (i == 2) q = q < 160 ? q : 159;
      unsigned vo = q * 512 + ((m0 * 8) ^ (((q >> 1) & 1) << 7));
      asm volatile("global_load_dwordx4 %0, %1, %2" : "=v"(rw[i]) : "v"(vo), "s"(Ag) : "memory");
    }
    SCHED_FENCE();
    G1_DMA(0); G1_DMA(1);
    const float sc = 0.5f * 17.88854381999832f / 320.0f;
    const float r2 = 1.41421356237309505f;
    Tw320 tw;
    c2 wu[5];
    {
      c2 u0 = twiddle_rev((float)lane * (1.0f / 640.0f));
      tw320_init(tw, lane, u0, sc);
      wu[0] = c2{u0.y, u0.x};
      wu[1] = cmul(wu[0], c2{0.80901699437494742f, 0.58778525229247313f});
      wu[2] = cmul(wu[0], c2{0.30901699437494742f, 0.95105651629515357f});
      wu[3] = cmul(wu[0], c2{-0.30901699437494742f, 0.95105651629515357f});
      wu[4] = cmul(wu[0], c2{-0.80901699437494742f, 0.58778525229247313f});
    }
    SCHED_FENCE();
    G1_DMA(2); G1_DMA(3); G1_DMA(4);
    PT(4);
    asm volatile("s_waitcnt vmcnt(20)" : "+v"(rw[0]), "+v"(rw[1]), "+v"(rw[2]) :: "memory");
    PT(5);
#pragma unroll
    for (int i = 0; i < 3; i++) {
      int q = lane + 64 * i;
      if (i == 2) q = q < 160 ? q : 159;
      *(u4*)&buf[2 * q] = u4{rw[i].x, rw[i].z, rw[i].y, rw[i].w};
    }
    lds_fence();
    c2 z[2][5], o[2][8];
    u2 ua[5], ub[5];
#pragma unroll
    for (int j = 0; j < 5; j++) {
      int k = lane + 64 * j;
      ua[j] = buf[k]; ub[j] = buf[(j == 0 && k == 0) ? 0 : 320 - k];
    }
    SCHED_FENCE();
    G1_LOADW(0); G1_LOADW(1);
#pragma unroll
    for (int j = 0; j < 5; j++) {
      c2 A0 = c2{bf_lo(ua[j].x), bf_hi(ua[j].x)}, B0 = c2{bf_lo(ub[j].x), bf_hi(ub[j].x)};
      c2 A1 = c2{bf_lo(ua[j].y), bf_hi(ua[j].y)}, B1 = c2{bf_lo(ub[j].y), bf_hi(ub[j].y)};
      if (j == 0 && lane == 0) {
        B0 = c2{A0.y * r2, 0.f}; A0 = c2{A0.x * r2, 0.f};
        B1 = c2{A1.y * r2, 0.f}; A1 = c2{A1.x * r2, 0.f};
      }
      tangle2(z[0][j], z[1][j], A0, B0, A1, B1, wu[j]);
    }
    fft320x2<1>(z, tw, sc, scr, lane, o, [&](int h) {
      if (h == 0) { G1_LOADW(2); G1_LOADW(3); }
      if (h == 2) { G1_LOADW(4); G1_LOADW(5); }
    });
    PT(6);
#pragma unroll
    for (int rr = 0; rr < 2; rr++)
#pragma unroll
      for (int e = 0; e < 8; e++) pk3[rr][e] = pack_bf(o[rr][e].x, o[rr][e].y);
    SCHED_FENCE();
#pragma unroll
    for (int s = 6; s < PF; s++) G1_LOADW(s);
  }
  const rsrc_t Hr = make_rsrc(ws + OFF_H + (size_t)mblk * 4 * H_TS * 1024, 4 * H_TS * 1024);
  const int G = fs * 2 + (wv >> 1);
  int hfull[4], hhalf[4];
#pragma unroll
  for (int m = 0; m < 4; m++) {
    hfull[m] = (((m + wv) & 3) * H_TS + 3 * G + (wv & 1)) * 1024; hhalf[m] = (((m + wv) & 3) * H_TS + 3 * G + 2) * 1024;
    asm("" : "+s"(hfull[m]), "+s"(hhalf[m]));
  }
  SCHED_FENCE();
  asm volatile("s_waitcnt vmcnt(%0)\n\ts_barrier" :: "n"(3 * PF) : "memory");
  if (WITH_INV) {
    const rsrc_t Xr = make_rsrc(ws + OFF_X3 + (size_t)(mblk * 64 + m0) * 1280, 2560);
    const unsigned vo = (iq + 5 * ia) * 4;
#pragma unroll
    for (int rr = 0; rr < 2; rr++)
#pragma unroll
      for (int e = 0; e < 8; e++) __builtin_amdgcn_raw_buffer_store_b32(pk3[rr][e], Xr, vo + (rr * 320 + 40 * e) * 4, 0, AUX_X3);
    SCHED_FENCE();
  }
  PT(1);
  const int g = lane >> 4, r = lane & 15;
  const int swz = (g & 1) << 7;
  typedef __attribute__((address_space(3))) const char* ldsc;
  typedef __attribute__((address_space(3))) G1_VOL const u2* ldsu2;
  ldsc abase[4], abase2[4];
#pragma unroll
  for (int m = 0; m < 4; m++) {
    unsigned o = (2 * g) * 512 + (((16 * ((m + wv) & 3) + r) * 8) ^ swz), o2 = o + 65536;
    asm("" : "+v"(o2));
    abase[m] = (ldsc)smem + o; abase2[m] = (ldsc)smem + o2;
  }
  SCHED_FENCE();
  f4 acc[3][4];
#pragma unroll
  for (int t = 0; t < 3; t++)
#pragma unroll
    for (int m = 0; m < 4; m++) acc[t][m] = f4{0.f, 0.f, 0.f, 0.f};
  u4 af[20][4];
#define G1_READA(KS, M) do { ldsc ap_ = (KS) < 16 ? abase[M] + (KS) * 4096 : abase2[M] + ((KS) - 16) * 4096; \
    u2 lo_ = *(ldsu2)ap_, hi_ = *(ldsu2)(ap_ + 512); af[KS][M] = u4{lo_.x, lo_.y, hi_.x, hi_.y}; } while (0)
#define G1_MFMA(ACC, W, B) ACC = __builtin_amdgcn_mfma_f32_16x16x32_bf16(W, __builtin_bit_cast(bf8, B), ACC, 0, 0, 0)
  G1_READA(0, 0); G1_READA(1, 0); G1_READA(0, 1); G1_READA(0, 2); G1_READA(0, 3);
  G1_READA(2, 0); G1_READA(3, 0); G1_READA(1, 1); G1_READA(1, 2); G1_READA(1, 3);
  typedef __attribute__((address_space(3))) volatile float* ldsf;
  ldsf stw = (ldsf)((ldsc)smem + G1_A_BYTES) + (g == (r >> 2) ? wv * 16 + r : 64 + (lane & 31));
  ldsf str = (ldsf)((ldsc)smem + G1_A_BYTES) + r;
  float rl[4], irs[4], e0, e1;
  unsigned pk[3][4][2];
  SCHED_FENCE();
  f4 gram = f4{0.f, 0.f, 0.f, 0.f};
  constexpr int NP1 = G1_NP1;
  static_for<20>([&](auto kI) {
    constexpr int ks = kI.value;
    constexpr int l0 = NL + (NP1 - NL) * ks / 20, l1 = NL + (NP1 - NL) * (ks + 1) / 20;
    constexpr int infl = (ks == 0 ? 10 : ks <= 8 ? 10 : ks == 12 ? 7 : ks == 14 ? 10 : ks < 19 ? 6 : 0);
    __builtin_amdgcn_s_waitcnt(0xc07f | ((infl & 15) << 8));
    SCHED_FENCE();
    if (ks < 10) {
      u4 ga = af[2 * ks][0];
      if (ks == 0 && g == 0) ga.x &= 0xffff0000u;
      G1_MFMA(gram, __builtin_bit_cast(bf8, ga), af[2 * ks][0]);
      if (ks < 8) G1_READA(ks < 8 ? 2 * ks + 4 : 0, 0);
      SCHED_FENCE();
    }
    static_for<4>([&](auto mI) {
      constexpr int m = mI.value;
      G1_MFMA(acc[0][m], wf[ks], af[ks][m]);
      if (m == 0 && ks < 8) G1_READA(ks < 8 ? 2 * ks + 5 : 0, 0);
      if (m > 0 && ks + 2 < 20) G1_READA(ks + 2 < 20 ? ks + 2 : 0, m);
      if (m == 0 && l0 < l1) G1_LOAD1(l0);
      if (m == 2 && l0 + 1 < l1) G1_LOAD1(l0 + 1);
      if (m == 0 && ks == 11) {
        float sv = (r & 2) ? ((r & 1) ? gram.w : gram.z) : ((r & 1) ? gram.y : gram.x);
        *stw = sv * (1.0f / 640.0f) + LN_EPS;
      }
      if (m == 0 && ks == 13) {
        asm volatile("s_barrier" ::: "memory");
#pragma unroll
        for (int q = 0; q < 4; q++) irs[q] = str[((q + wv) & 3) * 16];
      }
      if (m == 0 && ks >= 15 && ks < 19) {
        constexpr int q = (ks - 15) & 3;
        float rs = __builtin_amdgcn_rsqf(irs[q]);
        asm("s_nop 0\n\tv_mul_f32 %0, %0, %2\n\tv_mul_f32 %1, 0xbfb8aa3b, %2" : "+v"(irs[q]), "=v"(rl[q]) : "v"(rs));
      }
      SCHED_FENCE();
      if (m == 1 && ks < 10) {
        G1_MFMA(gram, __builtin_bit_cast(bf8, af[2 * ks + 1][0]), af[2 * ks + 1][0]);
        SCHED_FENCE();
      }
    });
  });
  PT(2);
  const unsigned l16h = l16 + (wv & 1) * 8;
  float a0, a1;
  auto silu_op = [&](auto cI, auto oI) {
    constexpr int c = cI.value, o = oI.value, t = c >> 3, m = (c >> 1) & 3, e = 2 * (c & 1);
    if (o == 0) { a0 = acc[t][m][e]; a1 = acc[t][m][e + 1]; asm("" : "+v"(a0), "+v"(a1)); }
    if (o == 1) asm("v_mul_f32 %0, %2, %4\n\tv_mul_f32 %1, %3, %4" : "=&v"(e0), "=&v"(e1) : "v"(a0), "v"(a1), "v"(rl[m]));
    if (o == 2) asm("v_exp_f32 %0, %0" : "+v"(e0));
    if (o == 3) asm("v_exp_f32 %0, %0" : "+v"(e1));
    if (o == 4) asm("v_fma_f32 %0, %0, %2, %2\n\tv_fma_f32 %1, %1, %2, %2" : "+v"(e0), "+v"(e1) : "v"(irs[m]));
    if (o == 5) asm("v_fma_f32 %0, %0, %2, %2\n\tv_fma_f32 %1, %1, %2, %2\n\tv_rcp_f32 %0, %0" : "+v"(e0), "+v"(e1) : "v"(irs[m]));
    if (o == 6) asm("v_rcp_f32 %0, %0" : "+v"(e0));
    if (o == 7) asm("v_rcp_f32 %0, %0" : "+v"(e1));
    if (o == 8) asm("v_rcp_f32 %1, %1\n\tv_mul_f32 %0, %0, %2" : "+v"(e0), "+v"(e1) : "v"(a0));
    if (o == 9) asm("v_mul_f32 %0, %0, %2\n\tv_mul_f32 %1, %1, %3" : "+v"(e0), "+v"(e1) : "v"(a0), "v"(a1));
    if (o == 10) asm("v_cvt_pk_bf16_f32 %0, %1, %2" : "=v"(pk[t][m][c & 1]) : "v"(e0), "v"(e1));
    if (o == 11) asm("v_mul_f32 %2, %2, %3\n\tv_cvt_pk_bf16_f32 %0, %1, %2" : "=v"(pk[t][m][c & 1]), "+v"(e0), "+v"(e1) : "v"(a1));
    if (o == 10 || o == 11) {
      if (t == 1 && (c & 1))
        __builtin_amdgcn_raw_buffer_store_b128(u4{pk[0][m][0], pk[0][m][1], pk[1][m][0], pk[1][m][1]}, Hr, l16, hfull[m], ST_AUX);
      if (t == 2 && (c & 1))
        __builtin_amdgcn_raw_buffer_store_b64(u2{pk[2][m][0], pk[2][m][1]}, Hr, l16h, hhalf[m], ST_AUX);
    }
    SCHED_FENCE();
  };
#define SOP(C, O) silu_op(ic<(C)>{}, ic<(O)>{})
  static_for<160>([&](auto jI) {
    constexpr int j = jI.value;
    constexpr int t = j < 80 ? 1 : 2;
    constexpr int ks = j < 40 ? j >> 2 : j < 80 ? 10 + ((j - 40) % 20) / 2 : ((j - 80) % 40) >> 1;
    constexpr int m = j < 40 ? j & 3 : j < 80 ? ((j - 40) / 20) * 2 + (j & 1) : ((j - 80) / 40) * 2 + (j & 1);
    G1_MFMA(acc[t][m], wf[20 * t + ks], af[ks][m]);
    SCHED_FENCE();
    constexpr int c = j >> 3, o = j & 7;
    if (j == 0) SOP(0, 0);
    if (o == 0) SOP(c, 1);
    if (o == 1) SOP(c, 2);
    if (o == 2) SOP(c, 3);
    if (o == 3) SOP(c, 4);
    if (o == 4) SOP(c, 6);
    if (o == 5) SOP(c, 7);
    if (o == 6) SOP(c, 9);
    if (o == 7) { SOP(c, 10); if (c < 19) SOP(c + 1, 0); }
    if (j % G1_LS == 3) G1_LOAD1(NP1 + j / G1_LS);
    if (j == 80) PT(3);
    SCHED_FENCE();
  });
  PT(4 + (WITH_INV ? 3 : 0));
  {
    float x[8];
#pragma unroll
    for (int i = 0; i < 8; i++) x[i] = acc[2][2 + (i >> 2)][i & 3] * rl[2 + (i >> 2)];
#pragma unroll
    for (int i = 0; i < 8; i++) x[i] = __builtin_amdgcn_exp2f(x[i]);
#pragma unroll
    for (int i = 0; i < 8; i++) x[i] = __builtin_amdgcn_rcpf(__builtin_fmaf(x[i], irs[2 + (i >> 2)], irs[2 + (i >> 2)])) * acc[2][2 + (i >> 2)][i & 3];
#pragma unroll
    for (int m = 2; m < 4; m++)
      __builtin_amdgcn_raw_buffer_store_b64(u2{pack_bf(x[4 * m - 8], x[4 * m - 7]), pack_bf(x[4 * m - 6], x[4 * m - 5])}, Hr, l16h, hhalf[m], ST_AUX);
  }
  PT(7); PT_FLUSH(KID);
}

template <bool FINAL>
__global__ __launch_bounds__(256, 1) void k_gemm2(char* __restrict__ ws, __hip_bfloat16* __restrict__ out) {
  __shared__ __attribute__((aligned(16))) f4 red[4 * 3 * 5 * 64];
  const int tid = threadIdx.x, lane = tid & 63;
  const int wv = __builtin_amdgcn_readfirstlane(tid >> 6);
  const int b = blockIdx.x;
  const int xcd = b & 7, slot = b >> 3;
  const int mblk = (xcd >> 1) * 8 + (slot & 7), cs = (xcd & 1) * 4 + (slot >> 3);
  const int g = lane >> 4, r = lane & 15;
  constexpr int KID = FINAL ? 6 : 3;
  (void)KID;
  asm volatile("" ::: "a191");
  PT_DECL; PT(0);
  const unsigned l16 = lane * 16;
  const rsrc_t Hr = make_rsrc(ws + OFF_H + ((size_t)mblk * 4 * H_TS + wv * 12) * 1024, (4 * H_TS - wv * 12) * 1024);
  const rsrc_t Wr = make_rsrc(ws + (FINAL ? OFF_W2C : OFF_W2P) + ((size_t)cs * 5 * H_TS + wv * 12) * 1024, (5 * H_TS - wv * 12) * 1024);
  int hoff[4];
#pragma unroll
  for (int m = 0; m < 4; m++) hoff[m] = ((m + wv) & 3) * H_TS * 1024;
  constexpr int PF = G2_PF, ND = G2_ND, S0 = 12 - ND;
  bf8 hf[12][4], wf[12][5];
#define G2_LH(S, M) hf[S][M] = __builtin_bit_cast(bf8, __builtin_amdgcn_raw_buffer_load_b128(Hr, l16 + ((S) & 3) * 1024, hoff[M] + ((S) >> 2) * 4096, 0))
#define G2_LW(S, C) wf[S][C] = bload(Wr, l16, ((C) * H_TS + (S)) * 1024)
#define G2_LOAD(S, J) do { constexpr int s_ = (S), j_ = (J); \
    if (s_ < 12) { constexpr int q_ = s_ < 12 ? s_ : 0; \
      if (j_ == 0) { G2_LH(q_, 1); G2_LW(q_, 0); } if (j_ == 1) { G2_LH(q_, 2); G2_LW(q_, 1); } if (j_ == 2) { G2_LH(q_, 3); G2_LW(q_, 2); } \
      if (j_ == 3) { G2_LW(q_, 3); if (q_ < S0) G2_LH(q_, 0); } if (j_ == 4) G2_LW(q_, 4); } \
    else { constexpr int per_ = (ND + PF - 1) / PF, i_ = (s_ - 12) * per_ + j_; \
      if (j_ < per_ && i_ < ND) G2_LH(S0 + (i_ < ND ? i_ : 0), 0); \
      if (j_ == 4) { _Pragma("unroll") for (int e_ = 5; e_ < per_; e_++) if ((s_ - 12) * per_ + e_ < ND) G2_LH(S0 + ((s_ - 12) * per_ + e_) % ND, 0); } } \
    } while (0)
  const int mrow = mblk * 64 + wv * 16 + r;
  u2 res[5];
  {
    const rsrc_t Rr = FINAL ? make_rsrc(ws + OFF_X3, 2048 * 1280) : make_rsrc(ws + OFF_XB + (size_t)mblk * XB_MS, XB_MS);
    const unsigned ro = FINAL ? (mrow * 640 + 4 * g) * 2 : g * 512 + (((wv * 16 + r) * 8) ^ ((g >> 1) << 7));
    static_for<PF>([&](auto sI) {
      constexpr int s = sI.value;
      G2_LOAD(s, 0); G2_LOAD(s, 1); G2_LOAD(s, 2); G2_LOAD(s, 3); G2_LOAD(s, 4);
      SCHED_FENCE();
      if (s == 0) {
#pragma unroll
        for (int c = 0; c < 5; c++)
          res[c] = FINAL ? __builtin_amdgcn_raw_buffer_load_b64(Rr, ro + c * 32, cs * 160, 0)
                         : __builtin_amdgcn_raw_buffer_load_b64(Rr, ro + (c & 1) * 2048, cs * 10240 + (c >> 1) * 4096, 0);
        SCHED_FENCE();
      }
    });
  }
  PT(6);
  f4 acc[5][4];
#pragma unroll
  for (int c = 0; c < 5; c++)
#pragma unroll
    for (int m = 0; m < 4; m++) acc[c][m] = f4{0.f, 0.f, 0.f, 0.f};
#define G2_MFMA(S, C, M) acc[C][M] = __builtin_amdgcn_mfma_f32_16x16x32_bf16(wf[S][C], hf[S][M], acc[C][M], 0, 0, 0)
  f4* rw[3];
#pragma unroll
  for (int m = 1; m < 4; m++) rw[m - 1] = red + (((m + wv) & 3) * 3 + m - 1) * 5 * 64 + lane;
#define G2_WRITE(I) do { if ((I) >= 0 && (I) < 15) rw[(I) / 5][((I) % 5) * 64] = acc[(I) % 5][1 + (I) / 5]; } while (0)
  static_for<11>([&](auto sI) {
    constexpr int s = sI.value;
    static_for<5>([&](auto cI) {
      constexpr int c = cI.value;
      G2_MFMA(s, c, 1);
      if (s == 0 && c == 0) PT(2);
      G2_MFMA(s, c, 2); G2_MFMA(s, c, 3);
      if (s < S0) G2_MFMA(s, c, 0);
      G2_LOAD(s + PF, c);
      SCHED_FENCE();
    });
    if (s == 0) PT(1);
  });
  constexpr int NM = 15 + (S0 == 12 ? 1 : ND) * 5, W0 = 7, NB = W0 + 15 * G2_WS + G2_BD;
  f4 part[15];
  static_for<NM>([&](auto iI) {
    constexpr int i = iI.value;
    if (i < 15) G2_MFMA(11, i % 5, 1 + i / 5);
    else G2_MFMA((S0 == 12 ? 11 : S0) + (i - 15) / 5, i % 5, 0);
    if (i < 5) G2_LOAD(11 + PF, i);
    if (i >= W0 && (i - W0) % G2_WS == 0) G2_WRITE((i - W0) / G2_WS);
    if (i == 15) PT(3);
    if (i == (NB < NM ? NB : NM - 1)) {
#pragma unroll
      for (int k = (NM - 1 - W0) / G2_WS + 1; k < 15; k++) G2_WRITE(k);
      asm volatile("s_waitcnt lgkmcnt(0)\n\ts_barrier" ::: "memory");
      PT(4);
#pragma unroll
      for (int k = 0; k < 15; k++) part[k] = red[(wv * 15 + k) * 64 + lane];
    }
    SCHED_FENCE();
  });
  PT(5);
  f4 own[5];
#pragma unroll
  for (int c = 0; c < 5; c++) own[c] = (part[c] + part[5 + c]) + (part[10 + c] + acc[c][0]);
  float s = 0.f;
#pragma unroll
  for (int c = 0; c < 5; c++) {
    f4 o = f4{bf_lo(res[c].x), bf_hi(res[c].x), bf_lo(res[c].y), bf_hi(res[c].y)} + own[c];
    u2 pk = u2{pack_bf(o.x, o.y), pack_bf(o.z, o.w)};
    if (FINAL) {
      __builtin_amdgcn_raw_buffer_store_b64(pk, make_rsrc(out, 2048 * 1280), (mrow * 640 + 4 * g) * 2 + c * 32, cs * 160, AUX_G2);
    } else {
      __builtin_amdgcn_raw_buffer_store_b64(pk, make_rsrc(ws + OFF_XB + (size_t)mblk * XB_MS, XB_MS), g * 512 + (((wv * 16 + r) * 8) ^ ((g >> 1) << 7)) + (c & 1) * 2048, cs * 10240 + (c >> 1) * 4096, AUX_G2);
      unsigned p0 = (cs == 0 && c == 0 && g == 0) ? (pk.x & 0xffff0000u) : pk.x;
      s = dot2(p0, s);
      s = dot2(pk.y, s);
    }
  }
  if (!FINAL) {
    s += __shfl_xor(s, 16);
    s += __shfl_xor(s, 32);
    if (g == 0) unsafeAtomicAdd((double*)(ws + OFF_SSQ) + mrow, (double)s);
  }
  if (FINAL) asm volatile("k6_code_end:");
  PT(7); PT_FLUSH(KID);
}

extern "C" void kernel_launch(void* const* d_in, const int* in_sizes, int n_in, void* d_out, int out_size, void* d_ws,
                              size_t ws_size, hipStream_t stream) {
  const uint16_t* x = (const uint16_t*)d_in[0];
  const uint16_t* wr = (const uint16_t*)d_in[1];
  const uint16_t* wi = (const uint16_t*)d_in[2];
  const uint16_t* w1 = (const uint16_t*)d_in[4];
  const uint16_t* w2 = (const uint16_t*)d_in[5];
  char* ws = (char*)d_ws;
  __hip_bfloat16* out = (__hip_bfloat16*)d_out;
  (void)hipFuncSetAttribute((const void*)k_colfilter<true>, hipFuncAttributeMaxDynamicSharedMemorySize, LDS_CF);
  (void)hipFuncSetAttribute((const void*)k_colfilter<false>, hipFuncAttributeMaxDynamicSharedMemorySize, LDS_CF);
  (void)hipFuncSetAttribute((const void*)k_gemm1<false>, hipFuncAttributeMaxDynamicSharedMemorySize, LDS_G1);
  (void)hipFuncSetAttribute((const void*)k_gemm1<true>, hipFuncAttributeMaxDynamicSharedMemorySize, LDS_G1I);
#define NK 7
  k_prep<<<K0_X + K0_W1 + K0_WT, 512, 0, stream>>>(x, wr, wi, w1, ws);
  if (NK > 1) k_colfilter<true><<<256, 256, LDS_CF, stream>>>(w2, ws);
  if (NK > 2) k_gemm1<false><<<256, 256, LDS_G1, stream>>>(ws);
  if (NK > 3) k_gemm2<false><<<256, 256, 0, stream>>>(ws, out);
  if (NK > 4) k_colfilter<false><<<256, 256, LDS_CF, stream>>>(w2, ws);
  if (NK > 5) k_gemm1<true><<<256, 256, LDS_G1I, stream>>>(ws);
  if (NK > 6) k_gemm2<true><<<256, 256, 0, stream>>>(ws, out);
}
```

```cpp
#pragma clang diagnostic ignored "-Warray-bounds"
#include <hip/hip_runtime.h>
#include <hip/hip_bf16.h>
#include <stdint.h>

typedef float c2 __attribute__((ext_vector_type(2)));
typedef float f4 __attribute__((ext_vector_type(4)));
typedef unsigned u2 __attribute__((ext_vector_type(2)));
typedef unsigned u4 __attribute__((ext_vector_type(4)));
typedef __bf16 bf2 __attribute__((ext_vector_type(2)));
typedef __bf16 bf8 __attribute__((ext_vector_type(8)));

#define DEV __device__ __forceinline__
#define SCHED_FENCE() __builtin_amdgcn_sched_barrier(0)

constexpr int S_ = 2048, D_ = 640, F_ = 1536, DF_ = 321;
constexpr float LN_EPS = 1e-5f;

constexpr int XB_MS = 81920 + 512;
constexpr int H_TS = 49;
constexpr int W1_TS = 21;
constexpr size_t OFF_XB = 0;
constexpr size_t OFF_H = OFF_XB + 32ull * XB_MS;
constexpr size_t OFF_W1P = OFF_H + 128ull * H_TS * 1024;
constexpr size_t OFF_W2P = OFF_W1P + 96ull * W1_TS * 1024;
constexpr size_t OFF_W2C = OFF_W2P + 40ull * H_TS * 1024;
constexpr size_t OFF_WT = OFF_W2C + 40ull * H_TS * 1024;
constexpr size_t OFF_X3 = OFF_WT + 160ull * 4 * 256 * 16;
constexpr size_t OFF_RS0 = OFF_X3 + 2048ull * 640 * 2;
constexpr size_t OFF_SSQ = OFF_RS0 + 2048 * 4;

#define TSTAMP(K, P) do {} while (0)
#define PT_DECL do {} while (0)
#define PT(i) do {} while (0)
#define PT_FLUSH(K) do {} while (0)
#define ST_AUX 16
#define AUX_X3 16
#define AUX_G2 16
#define AUX_K0 16
#define AUX_W1 16
#define AUX_CF 16
typedef __amdgpu_buffer_rsrc_t rsrc_t;
DEV rsrc_t make_rsrc(const void* p, unsigned bytes) { return __builtin_amdgcn_make_buffer_rsrc((void*)p, 0, bytes, 0x00020000); }
DEV bf8 bload(rsrc_t rs, unsigned voff, int OFF) {
  return __builtin_bit_cast(bf8, __builtin_amdgcn_raw_buffer_load_b128(rs, voff + (OFF & 4095), OFF & ~4095, 0));
}
DEV c2 add_mi(c2 a, c2 b) {
  c2 d; asm("v_pk_add_f32 %0, %1, %2 op_sel:[0,1] op_sel_hi:[1,0] neg_hi:[0,1]" : "=v"(d) : "v"(a), "v"(b)); return d;
}
DEV c2 add_pi(c2 a, c2 b) {
  c2 d; asm("v_pk_add_f32 %0, %1, %2 op_sel:[0,1] op_sel_hi:[1,0] neg_lo:[0,1]" : "=v"(d) : "v"(a), "v"(b)); return d;
}
DEV c2 cmul(c2 a, c2 w) {
  c2 t, d;
  asm("v_pk_mul_f32 %0, %1, %2 op_sel:[0,0] op_sel_hi:[0,1]" : "=v"(t) : "v"(a), "v"(w));
  asm("v_pk_fma_f32 %0, %1, %2, %3 op_sel:[1,1,0] op_sel_hi:[1,0,1] neg_lo:[0,1,0]" : "=v"(d) : "v"(a), "v"(w), "v"(t));
  return d;
}
DEV c2 cmulc(c2 a, c2 w) {
  c2 t, d;
  asm("v_pk_mul_f32 %0, %1, %2 op_sel:[0,0] op_sel_hi:[0,1] neg_hi:[0,1]" : "=v"(t) : "v"(a), "v"(w));
  asm("v_pk_fma_f32 %0, %1, %2, %3 op_sel:[1,1,0] op_sel_hi:[1,0,1]" : "=v"(d) : "v"(a), "v"(w), "v"(t));
  return d;
}
template <int SGN> DEV c2 twmul(c2 a, c2 w) { return SGN < 0 ? cmul(a, w) : cmulc(a, w); }
template <int SGN> DEV c2 add_rot(c2 a, c2 b) { return SGN < 0 ? add_mi(a, b) : add_pi(a, b); }
template <int SGN> DEV c2 sub_rot(c2 a, c2 b) { return SGN < 0 ? add_pi(a, b) : add_mi(a, b); }
DEV c2 twiddle_rev(float rev) {
  c2 t = c2{__builtin_amdgcn_cosf(rev), -__builtin_amdgcn_sinf(rev)};
  asm("s_nop 1" : "+v"(t));
  return t;
}
template <int N> DEV void tw_powers(c2* t, c2 w) {
  t[0] = w;
#pragma unroll
  for (int k = 2; k <= N; k++) {
    if ((k & 1) == 0) t[k - 1] = cmul(t[k / 2 - 1], t[k / 2 - 1]);
    else {
      int hi = k >= 8 ? 8 : (k >= 4 ? 4 : 2);
      t[k - 1] = cmul(t[hi - 1], t[k - hi - 1]);
    }
  }
}

template <int SGN> DEV void dft4(c2& a, c2& b, c2& c, c2& d) {
  c2 s0 = a + c, s1 = a - c, s2 = b + d, s3 = b - d;
  a = s0 + s2; c = s0 - s2;
  b = add_rot<SGN>(s1, s3); d = sub_rot<SGN>(s1, s3);
}
template <int SGN> DEV c2 rot1(c2 a) { return add_rot<SGN>(a, a) * 0.70710678118654752f; }
template <int SGN> DEV c2 rot3(c2 a) { return sub_rot<SGN>(a, a) * -0.70710678118654752f; }

template <int SGN> DEV void dft8(c2* v) {
  c2 e0 = v[0], e1 = v[2], e2 = v[4], e3 = v[6], o0 = v[1], o1 = v[3], o2 = v[5], o3 = v[7];
  dft4<SGN>(e0, e1, e2, e3);
  dft4<SGN>(o0, o1, o2, o3);
  o1 = rot1<SGN>(o1); o3 = rot3<SGN>(o3);
  v[0] = e0 + o0; v[4] = e0 - o0;
  v[1] = e1 + o1; v[5] = e1 - o1;
  v[2] = add_rot<SGN>(e2, o2); v[6] = sub_rot<SGN>(e2, o2);
  v[3] = e3 + o3; v[7] = e3 - o3;
}
template <int SGN> DEV void dft16(c2* v) {
  const c2 w1 = c2{0.92387953251128674f, -0.38268343236508977f};
  const c2 w3 = c2{0.38268343236508977f, -0.92387953251128674f};
  c2 y[4][4];
#pragma unroll
  for (int i = 0; i < 4; i++) {
    c2 a = v[i], b = v[i + 4], c = v[i + 8], d = v[i + 12];
    dft4<SGN>(a, b, c, d);
    y[i][0] = a; y[i][1] = b; y[i][2] = c; y[i][3] = d;
  }
  y[1][1] = twmul<SGN>(y[1][1], w1);
  y[1][2] = rot1<SGN>(y[1][2]);
  y[1][3] = twmul<SGN>(y[1][3], w3);
  y[2][1] = rot1<SGN>(y[2][1]);
  y[2][3] = rot3<SGN>(y[2][3]);
  y[3][1] = twmul<SGN>(y[3][1], w3);
  y[3][2] = rot3<SGN>(y[3][2]);
  y[3][3] = -twmul<SGN>(y[3][3], w1);
#pragma unroll
  for (int q = 0; q < 4; q++) {
    c2 a = y[0][q], b = y[1][q], c = y[2][q], d = y[3][q];
    if (q == 2) {
      c2 s0 = add_rot<SGN>(a, c), s1 = sub_rot<SGN>(a, c), s2 = b + d, s3 = b - d;
      a = s0 + s2; c = s0 - s2;
      b = add_rot<SGN>(s1, s3); d = sub_rot<SGN>(s1, s3);
    } else {
      dft4<SGN>(a, b, c, d);
    }
    v[q] = a; v[q + 4] = b; v[q + 8] = c; v[q + 12] = d;
  }
}
template <int SGN> DEV void twmulp(c2& a0, c2 w0, c2& a1, c2 w1) {
  c2 d0, d1;
  if (SGN < 0)
    asm("v_pk_mul_f32 %0, %2, %4 op_sel:[0,0] op_sel_hi:[0,1]\n\t"
        "v_pk_mul_f32 %1, %3, %5 op_sel:[0,0] op_sel_hi:[0,1]\n\t"
        "v_pk_fma_f32 %0, %2, %4, %0 op_sel:[1,1,0] op_sel_hi:[1,0,1] neg_lo:[0,1,0]\n\t"
        "v_pk_fma_f32 %1, %3, %5, %1 op_sel:[1,1,0] op_sel_hi:[1,0,1] neg_lo:[0,1,0]"
        : "=&v"(d0), "=&v"(d1) : "v"(a0), "v"(a1), "v"(w0), "v"(w1));
  else
    asm("v_pk_mul_f32 %0, %2, %4 op_sel:[0,0] op_sel_hi:[0,1] neg_hi:[0,1]\n\t"
        "v_pk_mul_f32 %1, %3, %5 op_sel:[0,0] op_sel_hi:[0,1] neg_hi:[0,1]\n\t"
        "v_pk_fma_f32 %0, %2, %4, %0 op_sel:[1,1,0] op_sel_hi:[1,0,1]\n\t"
        "v_pk_fma_f32 %1, %3, %5, %1 op_sel:[1,1,0] op_sel_hi:[1,0,1]"
        : "=&v"(d0), "=&v"(d1) : "v"(a0), "v"(a1), "v"(w0), "v"(w1));
  a0 = d0; a1 = d1;
}
template <int SGN, typename PRE, typename POST> DEV void dft16s(c2* v, PRE pre, POST post) {
  const c2 w1 = c2{0.92387953251128674f, -0.38268343236508977f};
  const c2 w3 = c2{0.38268343236508977f, -0.92387953251128674f};
  const c2 w9 = c2{-0.92387953251128674f, 0.38268343236508977f};
  c2 y[4][4];
#pragma unroll
  for (int i = 0; i < 4; i++) {
    pre(i);
    c2 a = v[i], b = v[i + 4], c = v[i + 8], d = v[i + 12];
    dft4<SGN>(a, b, c, d);
    y[i][0] = a; y[i][1] = b; y[i][2] = c; y[i][3] = d;
    if (i == 1) { twmulp<SGN>(y[1][1], w1, y[1][3], w3); y[1][2] = rot1<SGN>(y[1][2]); }
    if (i == 2) { y[2][1] = rot1<SGN>(y[2][1]); y[2][3] = rot3<SGN>(y[2][3]); }
    if (i == 3) { twmulp<SGN>(y[3][1], w3, y[3][3], w9); y[3][2] = rot3<SGN>(y[3][2]); }
    SCHED_FENCE();
  }
#pragma unroll
  for (int q = 0; q < 4; q++) {
    c2 a = y[0][q], b = y[1][q], c = y[2][q], d = y[3][q];
    if (q == 2) {
      c2 s0 = add_rot<SGN>(a, c), s1 = sub_rot<SGN>(a, c), s2 = b + d, s3 = b - d;
      a = s0 + s2; c = s0 - s2;
      b = add_rot<SGN>(s1, s3); d = sub_rot<SGN>(s1, s3);
    } else {
      dft4<SGN>(a, b, c, d);
    }
    v[q] = a; v[q + 4] = b; v[q + 8] = c; v[q + 12] = d;
    post(q);
    SCHED_FENCE();
  }
}
template <int SGN> DEV void dft5(c2* x) {
  const float c1 = 0.30901699437494742f, c2_ = -0.80901699437494742f;
  const float s1 = 0.95105651629515357f, s2 = 0.58778525229247313f;
  c2 t1 = x[1] + x[4], t2 = x[2] + x[3], t3 = x[1] - x[4], t4 = x[2] - x[3];
  c2 a1 = x[0] + c1 * t1 + c2_ * t2, a2 = x[0] + c2_ * t1 + c1 * t2;
  c2 b1 = s1 * t3 + s2 * t4, b2 = s2 * t3 - s1 * t4;
  x[0] = x[0] + t1 + t2;
  x[1] = add_rot<SGN>(a1, b1); x[4] = sub_rot<SGN>(a1, b1);
  x[2] = add_rot<SGN>(a2, b2); x[3] = sub_rot<SGN>(a2, b2);
}

DEV void lds_fence() { asm volatile("" ::: "memory"); __builtin_amdgcn_wave_barrier(); asm volatile("" ::: "memory"); }
DEV float bf_lo(unsigned u) { return __builtin_bit_cast(float, u << 16); }
DEV float bf_hi(unsigned u) { return __builtin_bit_cast(float, u & 0xffff0000u); }
DEV unsigned pack_bf(float a, float b) { return __builtin_bit_cast(unsigned, __builtin_convertvector(c2{a, b}, bf2)); }

template <int SGN> DEV void twmul2(c2& a0, c2& a1, c2 w) {
  c2 d0, d1;
  if (SGN < 0)
    asm("v_pk_mul_f32 %0, %2, %4 op_sel:[0,0] op_sel_hi:[0,1]\n\t"
        "v_pk_mul_f32 %1, %3, %4 op_sel:[0,0] op_sel_hi:[0,1]\n\t"
        "v_pk_fma_f32 %0, %2, %4, %0 op_sel:[1,1,0] op_sel_hi:[1,0,1] neg_lo:[0,1,0]\n\t"
        "v_pk_fma_f32 %1, %3, %4, %1 op_sel:[1,1,0] op_sel_hi:[1,0,1] neg_lo:[0,1,0]"
        : "=&v"(d0), "=&v"(d1) : "v"(a0), "v"(a1), "v"(w));
  else
    asm("v_pk_mul_f32 %0, %2, %4 op_sel:[0,0] op_sel_hi:[0,1] neg_hi:[0,1]\n\t"
        "v_pk_mul_f32 %1, %3, %4 op_sel:[0,0] op_sel_hi:[0,1] neg_hi:[0,1]\n\t"
        "v_pk_fma_f32 %0, %2, %4, %0 op_sel:[1,1,0] op_sel_hi:[1,0,1]\n\t"
        "v_pk_fma_f32 %1, %3, %4, %1 op_sel:[1,1,0] op_sel_hi:[1,0,1]"
        : "=&v"(d0), "=&v"(d1) : "v"(a0), "v"(a1), "v"(w));
  a0 = d0; a1 = d1;
}
DEV void tangle2(c2& d0, c2& d1, c2 A0, c2 B0, c2 A1, c2 B1, c2 w) {
  c2 e0, e1, o0, o1;
  asm("v_pk_add_f32 %0, %4, %5 neg_hi:[0,1]\n\t"
      "v_pk_add_f32 %1, %6, %7 neg_hi:[0,1]\n\t"
      "v_pk_add_f32 %2, %4, %5 neg_lo:[0,1]\n\t"
      "v_pk_add_f32 %3, %6, %7 neg_lo:[0,1]\n\t"
      "v_pk_fma_f32 %0, %2, %8, %0 op_sel:[0,0,0] op_sel_hi:[0,1,1]\n\t"
      "v_pk_fma_f32 %1, %3, %8, %1 op_sel:[0,0,0] op_sel_hi:[0,1,1]\n\t"
      "v_pk_fma_f32 %0, %2, %8, %0 op_sel:[1,1,0] op_sel_hi:[1,0,1] neg_lo:[0,1,0]\n\t"
      "v_pk_fma_f32 %1, %3, %8, %1 op_sel:[1,1,0] op_sel_hi:[1,0,1] neg_lo:[0,1,0]"
      : "=&v"(e0), "=&v"(e1), "=&v"(o0), "=&v"(o1) : "v"(A0), "v"(B0), "v"(A1), "v"(B1), "v"(w));
  d0 = e0; d1 = e1;
}

constexpr int SCR_F4 = 361;
struct Tw320 { c2 a[4]; c2 b[7]; };
DEV void tw320_init(Tw320& tw, int lane, c2 u0, float scale) {
  tw_powers<4>(tw.a, cmul(u0, u0));
  tw_powers<7>(tw.b, twiddle_rev((float)(lane & 7) * (1.0f / 64.0f)));
#pragma unroll
  for (int i = 0; i < 7; i++) tw.b[i] = tw.b[i] * scale;
}
#define FFT_HOOK_NONE(i)
template <int SGN, typename HOOK> DEV void fft320x2(c2 (*z)[5], const Tw320& tw, float scale, f4* scr, int lane, c2 (*o)[8], HOOK hook) {
  const int u = lane < 40 ? lane : 39;
  const int q = u >> 3, a = u & 7;
  dft5<SGN>(z[0]); dft5<SGN>(z[1]);
#pragma unroll
  for (int i = 1; i < 5; i++) twmul2<SGN>(z[0][i], z[1][i], tw.a[i - 1]);
  lds_fence();
#pragma unroll
  for (int i = 0; i < 5; i++) scr[i * 72 + lane] = f4{z[0][i].x, z[0][i].y, z[1][i].x, z[1][i].y};
  lds_fence();
#pragma unroll
  for (int b = 0; b < 8; b++) { f4 t = scr[q * 72 + a + 8 * b]; o[0][b] = t.xy; o[1][b] = t.zw; }
  SCHED_FENCE();
  hook(0);
  dft8<SGN>(o[0]); dft8<SGN>(o[1]);
  hook(1);
  o[0][0] = o[0][0] * scale; o[1][0] = o[1][0] * scale;
#pragma unroll
  for (int c = 1; c < 8; c++) twmul2<SGN>(o[0][c], o[1][c], tw.b[c - 1]);
  lds_fence();
#pragma unroll
  for (int c = 0; c < 8; c++) scr[(q * 8 + c) * 9 + a] = f4{o[0][c].x, o[0][c].y, o[1][c].x, o[1][c].y};
  lds_fence();
#pragma unroll
  for (int i = 0; i < 8; i++) { f4 t = scr[u * 9 + i]; o[0][i] = t.xy; o[1][i] = t.zw; }
  SCHED_FENCE();
  hook(2);
  dft8<SGN>(o[0]);
  hook(3);
  dft8<SGN>(o[1]);
  lds_fence();
}
constexpr float FWD_SC = 0.5f * 0.05590169943749474f;
DEV void rfft2_rows(const unsigned* r0, const unsigned* r1, const Tw320& tw, f4* scr, int lane, c2 (*o)[8]) {
  c2 z[2][5];
#pragma unroll
  for (int j = 0; j < 5; j++) { z[0][j] = c2{bf_lo(r0[j]), bf_hi(r0[j])}; z[1][j] = c2{bf_lo(r1[j]), bf_hi(r1[j])}; }
  fft320x2<-1>(z, tw, FWD_SC, scr, lane, o, [](int) {});
  const int u = lane < 40 ? lane : 39;
  f4* p = scr + (u >> 3) + 5 * (u & 7);
#pragma unroll
  for (int e = 0; e < 8; e++) p[40 * e] = f4{o[0][e].x, o[0][e].y, o[1][e].x, o[1][e].y};
}
DEV void untangle2(c2& x0, c2& x1, f4 A, f4 B, c2 w, bool k_is_0) {
  tangle2(x0, x1, A.xy, B.xy, A.zw, B.zw, w);
  if (k_is_0) {
    const float r = 1.41421356237309505f;
    x0 = c2{(A.x + A.y) * r, (A.x - A.y) * r};
    x1 = c2{(A.z + A.w) * r, (A.z - A.w) * r};
  }
}
DEV c2 mul_mi(c2 w) { return c2{w.y, -w.x}; }

template <int CTRL> DEV float dpp_add(float v) {
  return v + __builtin_bit_cast(float, __builtin_amdgcn_update_dpp(0, __builtin_bit_cast(int, v), CTRL, 0xf, 0xf, true));
}
DEV float wave_sum63(float v) {
  v = dpp_add<0x111>(v);
  v = dpp_add<0x112>(v);
  v = dpp_add<0x114>(v);
  v = dpp_add<0x118>(v);
  v = v + __builtin_bit_cast(float, __builtin_amdgcn_update_dpp(0, __builtin_bit_cast(int, v), 0x142, 0xa, 0xf, true));
  v = v + __builtin_bit_cast(float, __builtin_amdgcn_update_dpp(0, __builtin_bit_cast(int, v), 0x143, 0xc, 0xf, true));
  return v;
}

#define CODE_ADDR(A, SYM) asm("s_getpc_b64 vcc\n\ts_add_u32 vcc_lo, vcc_lo, " SYM "@rel32@lo+4\n\ts_addc_u32 vcc_hi, vcc_hi, " SYM "@rel32@hi+12\n\ts_mov_b64 %0, vcc" \
                              : "=s"(A) :: "vcc", "scc")
#define CODE_PF(V, SYM, N) do { const char *b_, *e_; CODE_ADDR(b_, SYM); CODE_ADDR(e_, N); \
    if ((long)(threadIdx.x * 64 + 4) <= e_ - b_) V = *(const __attribute__((address_space(1))) unsigned*)(b_ + threadIdx.x * 64); } while (0)
#define SYM_K1 "_Z11k_colfilterILb1EEvPKtPc"
#define SYM_K4 "_Z11k_colfilterILb0EEvPKtPc"
#define SYM_K2 "_Z7k_gemm1ILb0EEvPc"
#define SYM_K5 "_Z7k_gemm1ILb1EEvPc"
#define SYM_K3 "_Z7k_gemm2ILb0EEvPcP14__hip_bfloat16"
#define SYM_K6 "_Z7k_gemm2ILb1EEvPcP14__hip_bfloat16"
#define CODE_PF_K1(V) CODE_PF(V, SYM_K1, SYM_K4)
#define CODE_PF_K4(V) CODE_PF(V, SYM_K4, SYM_K2)
#define CODE_PF_K2(V) CODE_PF(V, SYM_K2, SYM_K5)
#define CODE_PF_K5(V) CODE_PF(V, SYM_K5, SYM_K3)
#define CODE_PF_K3(V) CODE_PF(V, SYM_K3, SYM_K6)
#define CODE_PF_K6(V) CODE_PF(V, SYM_K6, "k6_code_end")

DEV int frag_f(int T, int g, int j) {
  int G = T / 3, r = T - 3 * G;
  int ta = r == 0 ? 0 : (r == 1 ? 3 : 2), tb = r == 0 ? 1 : (r == 1 ? 4 : 5);
  return 96 * G + 16 * (j < 4 ? ta : tb) + 4 * g + (j & 3);
}

constexpr int K0_X = 128, K0_W1 = 96, K0_WT = 128;
template <bool is_x>
DEV void prep_rows(const uint16_t* __restrict__ x, const uint16_t* __restrict__ w1, char* __restrict__ ws, f4* scr_all, u2* stage, int grp) {
  const int tid = threadIdx.x, lane = tid & 63, wv = tid >> 6;
  f4* scr = scr_all + wv * SCR_F4;
  unsigned r0[5], r1[5];
  u4 s0[2], s1[2];
  if (is_x) {
    const unsigned* row = (const unsigned*)(x + (size_t)(16 * grp + 2 * wv) * D_);
#pragma unroll
    for (int j = 0; j < 5; j++) { r0[j] = row[lane + 64 * j]; r1[j] = row[320 + lane + 64 * j]; }
    if (tid < 16) ((double*)(ws + OFF_SSQ))[16 * grp + tid] = 0.0;
  } else {
#pragma unroll
    for (int it = 0; it < 2; it++) {
      int n = it == 0 ? tid : 512 + (tid & 127);
      const uint16_t* p = w1 + (size_t)(n >> 1) * (2 * F_) + grp * 16 + (n & 1) * 8;
      s0[it] = *(const u4*)p; s1[it] = *(const u4*)(p + F_);
    }
  }
  SCHED_FENCE();
  Tw320 tw;
  tw320_init(tw, lane, twiddle_rev((float)lane * (1.0f / 640.0f)), FWD_SC);
  c2 wo[3][2];
  wo[0][0] = mul_mi(twiddle_rev((float)(tid >> 3) * (1.0f / 320.0f)));
  wo[1][0] = cmul(wo[0][0], c2{0.30901699437494742f, -0.95105651629515357f});
  wo[2][0] = cmul(wo[0][0], c2{-0.80901699437494742f, -0.58778525229247313f});
#pragma unroll
  for (int it = 0; it < 3; it++) wo[it][1] = cmul(wo[it][0], c2{0.99995180895932817f, -0.00981731933714962f});
  SCHED_FENCE();
  if (!is_x) {
#pragma unroll
    for (int it = 0; it < 2; it++) {
      int n = tid + 512 * it;
      if (it == 0 || tid < 128) {
#pragma unroll
        for (int i = 0; i < 4; i++)
          stage[((n & 1) * 4 + i) * 324 + (n >> 1)] = u2{__builtin_amdgcn_perm(s1[it][i], s0[it][i], 0x05040100u), __builtin_amdgcn_perm(s1[it][i], s0[it][i], 0x07060302u)};
      }
    }
    __syncthreads();
#pragma unroll
    for (int j = 0; j < 5; j++) { u2 t = stage[wv * 324 + lane + 64 * j]; r0[j] = t.x; r1[j] = t.y; }
  }
  TSTAMP(0, 1);
  c2 o[2][8];
  rfft2_rows(r0, r1, tw, scr, lane, o);
  if (is_x) {
    float ss[2];
#pragma unroll
    for (int rr = 0; rr < 2; rr++) {
      c2 t = o[rr][0];
      if (lane == 0) t = c2{(t.x - t.y) * 0.70710678118654752f, 0.f};
      c2 acc = t * t;
#pragma unroll
      for (int e = 1; e < 8; e++) acc += o[rr][e] * o[rr][e];
      ss[rr] = wave_sum63(lane < 40 ? acc.x + acc.y : 0.f);
    }
    if (lane == 63)
      *(c2*)(ws + OFF_RS0 + (size_t)(16 * grp + 2 * wv) * 4) =
          c2{__builtin_amdgcn_rsqf(ss[0] * (1.0f / 160.0f) + LN_EPS), __builtin_amdgcn_rsqf(ss[1] * (1.0f / 160.0f) + LN_EPS)} * (1.0f / 2048.0f);
  }
  TSTAMP(0, 2);
  __syncthreads();
  const int rp = tid & 7;
  const f4* S = scr_all + rp * SCR_F4;
#pragma unroll
  for (int it = 0; it < 3; it++) {
    if (it == 2 && tid >= 256) break;
    const int q4 = (tid >> 3) + 64 * it;
    f4 A0 = S[2 * q4], A1 = S[2 * q4 + 1], B0 = S[(it == 0 && q4 == 0) ? 0 : 320 - 2 * q4], B1 = S[319 - 2 * q4];
    c2 x00, x10, x01, x11;
    untangle2(x00, x10, A0, B0, wo[it][0], it == 0 && q4 == 0);
    untangle2(x01, x11, A1, B1, wo[it][1], false);
    if (is_x) {
      u4 v = u4{pack_bf(x00.x, x00.y), pack_bf(x01.x, x01.y), pack_bf(x10.x, x10.y), pack_bf(x11.x, x11.y)};
      __builtin_amdgcn_raw_buffer_store_b128(v, make_rsrc(ws + OFF_XB + (size_t)(grp >> 2) * XB_MS, XB_MS), q4 * 512 + (((grp & 3) * 128 + rp * 16) ^ (((q4 >> 1) & 1) << 7)), 0, AUX_K0);
    } else {
      if (it == 0 && q4 == 0) { x00.x = 0.f; x10.x = 0.f; }
      const rsrc_t Wr = make_rsrc(ws + OFF_W1P + (size_t)grp * W1_TS * 1024, W1_TS * 1024);
      const unsigned vo = ((q4 >> 1) * 16 + 2 * rp) * 16 + (q4 & 1) * 8;
      __builtin_amdgcn_raw_buffer_store_b64(u2{pack_bf(x00.x, x00.y), pack_bf(x01.x, x01.y)}, Wr, vo, 0, AUX_W1);
      __builtin_amdgcn_raw_buffer_store_b64(u2{pack_bf(x10.x, x10.y), pack_bf(x11.x, x11.y)}, Wr, vo + 16, 0, AUX_W1);
    }
  }
  TSTAMP(0, 7);
}

__global__ __launch_bounds__(512) void k_prep(const uint16_t* __restrict__ x, const uint16_t* __restrict__ wr,
                                              const uint16_t* __restrict__ wi, const uint16_t* __restrict__ w1,
                                              const uint16_t* __restrict__ w2, const char* outp, char* __restrict__ ws) {
  __shared__ __attribute__((aligned(16))) f4 scr_all[8 * SCR_F4];
  __shared__ __attribute__((aligned(16))) u2 stage[8 * 324];
  const int tid = threadIdx.x, lane = tid & 63, wv = tid >> 6;
  const int b = blockIdx.x;
  TSTAMP(0, 0);
  unsigned spf = 0;
  if ((b & 63) < 8) CODE_PF(spf, "_Z6k_prepPKtS0_S0_S0_S0_PKcPc", SYM_K1);

  if (b >= K0_X + K0_W1) {
    int bb = b - (K0_X + K0_W1);
    int unit = (bb & 7) * 16 + (bb >> 3);
    int u = unit & 3, bbit = (unit >> 2) & 1, kc = unit >> 3;
    unsigned* t32 = (unsigned*)scr_all;
    unsigned cpf = 0;
    if ((bb >> 3) == 0) CODE_PF_K1(cpf);
    if ((bb >> 3) == 1) CODE_PF_K2(cpf);
    if ((bb >> 3) == 2) CODE_PF_K3(cpf);
    if ((bb >> 3) == 3) CODE_PF_K4(cpf);
    if ((bb >> 3) == 4) CODE_PF_K5(cpf);
    if ((bb >> 3) == 5) CODE_PF_K6(cpf);
    if ((bb >> 3) == 6) { CODE_PF(cpf, SYM_K1 ".kd", SYM_K6 ".kd"); }
    if ((bb >> 3) == 7 && tid == 0) { const char* a_; CODE_ADDR(a_, SYM_K6 ".kd"); cpf = *(const __attribute__((address_space(1))) unsigned*)a_; }
    if ((bb >> 3) == 8 && tid * 65536u < OFF_RS0) cpf = *(const unsigned*)(ws + tid * 65536u);
    if ((bb >> 3) == 9 && tid < 40) cpf = *(const unsigned*)(outp + tid * 65536u);
    if ((bb >> 3) == 10 && tid < 30) cpf = *(const unsigned*)((const char*)w2 + tid * 65536u);
    unsigned short re[2][6], im[2][6];
#pragma unroll
    for (int hh = 0; hh < 2; hh++) {
      int ks = kc + 16 * (2 * u + hh + 8 * bbit) + 256 * wv;
      const uint16_t* pr = wr + (size_t)ks * DF_;
      const uint16_t* pi = wi + (size_t)ks * DF_;
#pragma unroll
      for (int i = 0; i < 6; i++) {
        int c = lane + 64 * i;
        c = c < DF_ ? c : DF_ - 1;
        re[hh][i] = pr[c]; im[hh][i] = pi[c];
      }
    }
#pragma unroll
    for (int hh = 0; hh < 2; hh++)
#pragma unroll
      for (int i = 0; i < 6; i++) {
        int c = lane + 64 * i;
        if (c < DF_) t32[(hh * 8 + wv) * 324 + c] = (unsigned)re[hh][i] | ((unsigned)im[hh][i] << 16);
      }
    __syncthreads();
    const rsrc_t WT = make_rsrc(ws + OFF_WT, 160 * 4 * 256 * 16);
    for (int n = tid; n < 1280; n += 512) {
      int col = n & 1, hh = (n >> 1) & 1, ih = (n >> 2) & 1, q4 = n >> 3;
      int cc = 2 * q4 + col;
      if (cc == 0) cc = 320;
      u4 v;
#pragma unroll
      for (int e = 0; e < 4; e++) v[e] = t32[(hh * 8 + ih * 4 + e) * 324 + cc];
      int tau = (2 * u + hh) + 8 * kc;
      __builtin_amdgcn_raw_buffer_store_b128(v, WT, ((q4 * 4 + 2 * bbit + ih) * 256 + 2 * tau + col) * 16, 0, AUX_K0);
    }
    asm volatile("" :: "v"(cpf), "v"(spf));
    TSTAMP(0, 7);
    return;
  }

  if (b < K0_X) prep_rows<true>(x, w1, ws, scr_all, stage, (b & 7) * 16 + (b >> 3));
  else prep_rows<false>(x, w1, ws, scr_all, stage, ((b - K0_X) & 7) * 12 + ((b - K0_X) >> 3));
  asm volatile("" :: "v"(spf));
}

constexpr int P1 = 152;
constexpr int LDS_CF = 84 * 1024;
template <bool FIRST>
__global__ __launch_bounds__(256, 1) void k_colfilter(const uint16_t* __restrict__ w2, char* __restrict__ ws) {
  extern __shared__ __attribute__((aligned(16))) char smem[];
  const int tid = threadIdx.x;
  const int xcd = blockIdx.x & 7, slot = blockIdx.x >> 3;
  constexpr int KID = FIRST ? 1 : 4;
  (void)KID;
  if (slot >= 20) {
    TSTAMP(KID, 0);
    const int lane = tid & 63, wv = tid >> 6;
    const int i = (slot - 20) * 8 + xcd;
    const int T = i >> 1, g0 = (i & 1) * 2;
    unsigned pf[6], cpf = 0;
    if (FIRST) { if (slot == 30) CODE_PF_K3(cpf); if (slot == 31) CODE_PF_K4(cpf); }
    else { if (slot == 30) CODE_PF_K5(cpf); if (slot == 31) CODE_PF_K6(cpf); }
    const char* pfbase = ws + OFF_W1P + (size_t)(xcd & 1) * 48 * W1_TS * 1024 + tid * 64;
#define L2_PREFETCH() do { SCHED_FENCE(); _Pragma("unroll") for (int j = 0; j < 6; j++) { int blk = (slot - 20) + 12 * j; blk = blk < 63 ? blk : 62; \
      pf[j] = *(const unsigned*)(pfbase + blk * 16384); } SCHED_FENCE(); } while (0)
    if (FIRST) {
      f4* scr_all = (f4*)smem;
      unsigned raw[4][5];
#pragma unroll
      for (int rr = 0; rr < 4; rr++) {
        int r = wv * 4 + rr;
        const unsigned* row = (const unsigned*)(w2 + (size_t)frag_f(T, g0 + (r >> 3), r & 7) * D_);
#pragma unroll
        for (int j = 0; j < 5; j++) raw[rr][j] = row[lane + 64 * j];
      }
      L2_PREFETCH();
      Tw320 tw;
      tw320_init(tw, lane, twiddle_rev((float)lane * (1.0f / 640.0f)), FWD_SC);
      const int k0 = (tid & 7) + 8 * (tid >> 4), gg = (tid >> 3) & 1;
      c2 wo[3];
      wo[0] = mul_mi(twiddle_rev((float)k0 * (1.0f / 640.0f)));
      wo[1] = cmul(wo[0], c2{0.30901699437494742f, -0.95105651629515357f});
      wo[2] = cmul(wo[0], c2{-0.80901699437494742f, -0.58778525229247313f});
      SCHED_FENCE();
      c2 o[2][8];
      rfft2_rows(raw[0], raw[1], tw, scr_all + (wv * 2) * SCR_F4, lane, o);
      rfft2_rows(raw[2], raw[3], tw, scr_all + (wv * 2 + 1) * SCR_F4, lane, o);
      __syncthreads();
      const f4* S = scr_all + gg * 4 * SCR_F4;
#pragma unroll
      for (int it = 0; it < 3; it++) {
        if (it == 2 && tid >= 128) break;
        const int k = k0 + 128 * it;
        const bool z = it == 0 && k == 0;
        f4 A[4], B[4];
#pragma unroll
        for (int jp = 0; jp < 4; jp++) { A[jp] = S[jp * SCR_F4 + k]; B[jp] = S[jp * SCR_F4 + (z ? 0 : 320 - k)]; }
        u4 re, im;
#pragma unroll
        for (int jp = 0; jp < 4; jp++) {
          c2 x0, x1;
          untangle2(x0, x1, A[jp], B[jp], wo[it], z);
          re[jp] = pack_bf(x0.x, x1.x); im[jp] = pack_bf(x0.y, x1.y);
        }
        const rsrc_t Wr = make_rsrc(ws + OFF_W2P, 40 * H_TS * 1024);
        const unsigned vo = (((k >> 3) * H_TS + T) * 64 + (g0 + gg) * 16 + ((2 * k) & 15)) * 16;
        __builtin_amdgcn_raw_buffer_store_b128(re, Wr, vo, 0, AUX_W1);
        __builtin_amdgcn_raw_buffer_store_b128(im, Wr, vo + 16, 0, AUX_W1);
      }
    } else {
      const rsrc_t W2C = make_rsrc(ws + OFF_W2C, 40 * H_TS * 1024);
      unsigned short v[5][8];
#pragma unroll
      for (int it = 0; it < 5; it++) {
        int n = tid + 256 * it;
        int cl = n & 15, gg = (n >> 4) & 1, ct = n >> 5;
#pragma unroll
        for (int j = 0; j < 8; j++) v[it][j] = w2[(size_t)frag_f(T, g0 + gg, j) * D_ + ct * 16 + cl];
      }
      L2_PREFETCH();
#pragma unroll
      for (int it = 0; it < 5; it++) {
        int n = tid + 256 * it;
        int cl = n & 15, gg = (n >> 4) & 1, ct = n >> 5;
        u4 o;
#pragma unroll
        for (int j = 0; j < 4; j++) o[j] = (unsigned)v[it][2 * j] | ((unsigned)v[it][2 * j + 1] << 16);
        __builtin_amdgcn_raw_buffer_store_b128(o, W2C, ((ct * H_TS + T) * 64 + (g0 + gg) * 16 + cl) * 16, 0, AUX_K0);
      }
    }
    asm volatile("" :: "v"(pf[0]), "v"(pf[1]), "v"(pf[2]), "v"(pf[3]), "v"(pf[4]), "v"(pf[5]), "v"(cpf));
    TSTAMP(KID, 7);
    return;
  }
  typedef __attribute__((address_space(3))) volatile c2* ldsp;
  const int col = tid & 1, tau = tid >> 1;
  const int q4 = xcd * 20 + slot;
  const int lo = tau & 7, kc = tau >> 3;
  PT_DECL; PT(0);
  const rsrc_t XBr = make_rsrc(ws + OFF_XB + q4 * 512, 32 * XB_MS);
  const unsigned xoff = (tau >> 6) * XB_MS + ((((tau & 63) << 1) + col) ^ (((q4 >> 1) & 1) << 5)) * 4;
  unsigned xr[16];
  c2 v[16];
  float rs[16];
  u2 sq[16];
  u4 wt[4];
#pragma unroll
  for (int r = 0; r < 16; r++) xr[r] = __builtin_amdgcn_raw_buffer_load_b32(XBr, xoff, r * (2 * XB_MS), 0);
  {
    const rsrc_t Sr = make_rsrc(ws + OFF_RS0, 2048 * 12);
#pragma unroll
    for (int r = 0; r < 16; r++) {
      if (FIRST) rs[r] = __builtin_bit_cast(float, __builtin_amdgcn_raw_buffer_load_b32(Sr, tau * 4 + (r & 7) * 512, (r >> 3) * 4096, 0));
      else sq[r] = __builtin_amdgcn_raw_buffer_load_b64(Sr, tau * 8 + (r & 3) * 1024, 8192 + (r >> 2) * 4096, 0);
    }
    const rsrc_t Wr = make_rsrc(ws + OFF_WT + (size_t)q4 * 16384, 16384);
#pragma unroll
    for (int i = 0; i < 4; i++) wt[i] = __builtin_amdgcn_raw_buffer_load_b128(Wr, tid * 16, i * 4096, 0);
  }
  SCHED_FENCE();
  c2 tw1[15], tw2[15];
  tw_powers<15>(tw1, twiddle_rev((float)tau * (1.0f / 2048.0f)));
  tw_powers<15>(tw2, twiddle_rev((float)lo * (1.0f / 128.0f)));
  const bool special = (q4 == 0) && (col == 0);
  const unsigned sel_lo = special ? 0x07060c0cu : 0x05040c0cu, sel_hi = special ? 0x0c0c0c0cu : 0x07060c0cu;
  ldsp e1 = (ldsp)(uintptr_t)(unsigned)(uintptr_t)(__attribute__((address_space(3))) char*)smem + tid;
  ldsp e2 = (ldsp)(uintptr_t)(unsigned)(uintptr_t)(__attribute__((address_space(3))) char*)smem + (kc * P1 + lo) * 2 + col;
  ldsp e3 = (ldsp)(uintptr_t)(unsigned)(uintptr_t)(__attribute__((address_space(3))) char*)smem + (kc * P1 + lo * 9) * 2 + col;
  SCHED_FENCE();
  c2 xv[16];
  dft16s<-1>(v,
    [&](int i) {
#pragma unroll
      for (int j = 0; j < 4; j++) {
        const int r = i + 4 * j;
        if (!FIRST) {
          u2 t = sq[r];
          rs[r] = __builtin_amdgcn_rsqf((float)__builtin_bit_cast(double, t) * (2048.0f * 2048.0f / 640.0f) + LN_EPS * 2048.0f * 2048.0f);
        }
        xv[r] = c2{__builtin_bit_cast(float, __builtin_amdgcn_perm(xr[r], 0u, sel_lo)), __builtin_bit_cast(float, __builtin_amdgcn_perm(xr[r], 0u, sel_hi))};
        v[r] = xv[r] * rs[r];
      }
      if (i == 0) PT(1);
    },
    [&](int q) {
      if (q == 0) v[4] = cmul(v[4], tw1[3]); else twmulp<-1>(v[q], tw1[q - 1], v[q + 4], tw1[q + 3]);
      e1[q * (2 * P1)] = v[q]; e1[(q + 4) * (2 * P1)] = v[q + 4];
      twmulp<-1>(v[q + 8], tw1[q + 7], v[q + 12], tw1[q + 11]);
      e1[(q + 8) * (2 * P1)] = v[q + 8]; e1[(q + 12) * (2 * P1)] = v[q + 12];
    });
  __syncthreads();
#pragma unroll
  for (int i = 0; i < 4; i++)
#pragma unroll
    for (int j = 0; j < 4; j++) v[i + 4 * j] = e2[(i + 4 * j) * 16];
  SCHED_FENCE();
  c2 wf[16];
#pragma unroll
  for (int r = 0; r < 16; r++) { unsigned w = wt[r >> 2][r & 3]; wf[r] = c2{bf_lo(w), bf_hi(w)}; }
  SCHED_FENCE();
  dft16s<-1>(v, [&](int) {},
    [&](int q) {
      if (q == 0) v[4] = cmul(v[4], tw2[3]); else twmulp<-1>(v[q], tw2[q - 1], v[q + 4], tw2[q + 3]);
      e2[q * 18] = v[q]; e2[(q + 4) * 18] = v[q + 4];
      twmulp<-1>(v[q + 8], tw2[q + 7], v[q + 12], tw2[q + 11]);
      e2[(q + 8) * 18] = v[q + 8]; e2[(q + 12) * 18] = v[q + 12];
    });
#pragma unroll
  for (int bb = 0; bb < 2; bb++)
#pragma unroll
    for (int sa = 0; sa < 8; sa++) v[bb * 8 + sa] = e3[(bb * 72 + sa) * 2];
  SCHED_FENCE();
  PT(2);
#pragma unroll
  for (int bb = 0; bb < 2; bb++) {
    dft8<-1>(v + 8 * bb);
#pragma unroll
    for (int r = 0; r < 8; r += 2) twmulp<-1>(v[8 * bb + r], wf[8 * bb + r], v[8 * bb + r + 1], wf[8 * bb + r + 1]);
    SCHED_FENCE();
  }
  dft8<1>(v);
  SCHED_FENCE();
  {
    c2* u = v + 8;
    c2 a0 = u[0], a1 = u[2], a2 = u[4], a3 = u[6], o0 = u[1], o1 = u[3], o2 = u[5], o3 = u[7];
    e3[0] = v[0]; e3[2] = v[1];
    dft4<1>(a0, a1, a2, a3);
    SCHED_FENCE();
    e3[4] = v[2]; e3[6] = v[3];
    dft4<1>(o0, o1, o2, o3);
    SCHED_FENCE();
    e3[8] = v[4]; e3[10] = v[5];
    o1 = rot1<1>(o1); o3 = rot3<1>(o3);
    u[0] = a0 + o0; u[4] = a0 - o0;
    u[2] = add_rot<1>(a2, o2); u[6] = sub_rot<1>(a2, o2);
    SCHED_FENCE();
    e3[12] = v[6]; e3[14] = v[7];
    u[1] = a1 + o1; u[5] = a1 - o1;
    u[3] = a3 + o3; u[7] = a3 - o3;
    SCHED_FENCE();
#pragma unroll
    for (int sa = 0; sa < 8; sa++) e3[(72 + sa) * 2] = u[sa];
  }
#pragma unroll
  for (int i = 0; i < 4; i++)
#pragma unroll
    for (int j = 0; j < 4; j++) v[i + 4 * j] = e2[(i + 4 * j) * 18];
  SCHED_FENCE();
  dft16s<1>(v,
    [&](int i) {
      if (i == 0) v[4] = cmulc(v[4], tw2[3]); else twmulp<1>(v[i], tw2[i - 1], v[i + 4], tw2[i + 3]);
      twmulp<1>(v[i + 8], tw2[i + 7], v[i + 12], tw2[i + 11]);
    },
    [&](int q) {
#pragma unroll
      for (int j = 0; j < 4; j++) e2[(q + 4 * j) * 16] = v[q + 4 * j];
    });
  __syncthreads();
#pragma unroll
  for (int i = 0; i < 4; i++)
#pragma unroll
    for (int j = 0; j < 4; j++) v[i + 4 * j] = e1[(i + 4 * j) * (2 * P1)];
  SCHED_FENCE();
  const unsigned sel_o = special ? 0x05040100u : 0x07060504u;
  PT(3);
  dft16s<1>(v,
    [&](int i) {
      if (i == 0) v[4] = cmulc(v[4], tw1[3]); else twmulp<1>(v[i], tw1[i - 1], v[i + 4], tw1[i + 3]);
      twmulp<1>(v[i + 8], tw1[i + 7], v[i + 12], tw1[i + 11]);
    },
    [&](int q) {
#pragma unroll
      for (int j = 0; j < 4; j++) {
        const int r = q + 4 * j;
        c2 o = xv[r] + v[r];
        __builtin_amdgcn_raw_buffer_store_b32(__builtin_amdgcn_perm(pack_bf(o.x, o.y), xr[r], sel_o), XBr, xoff, r * (2 * XB_MS), AUX_CF);
      }
    });
  PT(7); PT_FLUSH(KID);
}

constexpr int G1_A_BYTES = 81920;
constexpr int G1_WSCR = SCR_F4 * 16;
#define G1_PF 4
#define G1_PFI 5
#define G1_VOL volatile
#define G1_LGKM 0xc87f
#define G1_LS 4
#define G1_NP1 42
#define G2_PF 3
#define G2_ND 12
#define G2_WS 2
#define G2_BD 4
template <int I> struct ic { static constexpr int value = I; };
template <int N, int I = 0, typename F> DEV void static_for(F f) { if constexpr (I < N) { f(ic<I>{}); static_for<N, I + 1>(f); } }
constexpr int LDS_G1 = G1_A_BYTES + 512;
constexpr int LDS_G1I = LDS_G1 + 4 * G1_WSCR;
DEV float dot2(unsigned a, float c) {
  bf2 q = __builtin_bit_cast(bf2, a);
  return __builtin_amdgcn_fdot2_f32_bf16(q, q, c, false);
}
template <bool WITH_INV>
__global__ __launch_bounds__(256, 1) void k_gemm1(char* __restrict__ ws) {
  extern __shared__ __attribute__((aligned(16))) char smem[];
  const int tid = threadIdx.x, lane = tid & 63;
  const int wv = __builtin_amdgcn_readfirstlane(tid >> 6);
  const int b = blockIdx.x;
  const int xcd = b & 7, slot = b >> 3;
  const int mblk = (xcd >> 1) * 8 + (slot & 7), fs = (xcd & 1) * 4 + (slot >> 3);
  constexpr int KID = WITH_INV ? 5 : 2;
  (void)KID;
  asm volatile("" ::: "a191");
  PT_DECL; PT(0);
  const char* Ag = ws + OFF_XB + (size_t)mblk * XB_MS;
  const unsigned lds0 = (unsigned)(uintptr_t)(__attribute__((address_space(3))) char*)smem + wv * 20480;
  const unsigned l16 = lane * 16;
#define G1_DMA(I) do { unsigned keep_; asm volatile( \
          "s_mov_b32 %0, m0\n\ts_mov_b32 m0, %3\n\ts_nop 0\n\t" \
          "global_load_lds_dwordx4 %1, %2\n\t" \
          "global_load_lds_dwordx4 %1, %2 offset:1024\n\t" \
          "global_load_lds_dwordx4 %1, %2 offset:2048\n\t" \
          "global_load_lds_dwordx4 %1, %2 offset:3072\n\t" \
          "s_mov_b32 m0, %0" \
          : "=&s"(keep_) : "v"(l16), "s"(Ag + wv * 20480 + (I) * 4096), "s"(lds0 + (I) * 4096) : "memory"); SCHED_FENCE(); } while (0)
  const rsrc_t Wg = make_rsrc(ws + OFF_W1P + (size_t)(fs * 12 + wv * 3) * W1_TS * 1024, 3 * W1_TS * 1024);
  constexpr int PF = WITH_INV ? G1_PFI : G1_PF;
  constexpr int NL = 3 * PF;
  bf8 wf[60];
#define G1_LOAD1(I) do { if ((I) < 60) wf[(I) < 60 ? (I) : 0] = bload(Wg, l16, ((I) / 20 * W1_TS + (I) % 20) * 1024); } while (0)
#define G1_LOADW(S) do { if ((S) < PF) { G1_LOAD1(3 * (S)); G1_LOAD1(3 * (S) + 1); G1_LOAD1(3 * (S) + 2); SCHED_FENCE(); } } while (0)
  unsigned pk3[2][8];
  const int m0 = fs * 8 + 2 * wv;
  const int iu = lane < 40 ? lane : 39;
  const int iq = iu >> 3, ia = iu & 7;
  if (!WITH_INV) {
    G1_DMA(0); G1_DMA(1); G1_DMA(2); G1_DMA(3); G1_DMA(4);
#pragma unroll
    for (int s = 0; s < PF; s++) G1_LOADW(s);
    PT(6);
  } else {
    f4* scr = (f4*)(smem + LDS_G1 + wv * G1_WSCR);
    u2* buf = (u2*)scr;
    u4 rw[3];
#pragma unroll
    for (int i = 0; i < 3; i++) {
      int q = lane + 64 * i;
      if (i == 2) q = q < 160 ? q : 159;
      unsigned vo = q * 512 + ((m0 * 8) ^ (((q >> 1) & 1) << 7));
      asm volatile("global_load_dwordx4 %0, %1, %2" : "=v"(rw[i]) : "v"(vo), "s"(Ag) : "memory");
    }
    SCHED_FENCE();
    G1_DMA(0); G1_DMA(1);
    const float sc = 0.5f * 17.88854381999832f / 320.0f;
    const float r2 = 1.41421356237309505f;
    Tw320 tw;
    c2 wu[5];
    {
      c2 u0 = twiddle_rev((float)lane * (1.0f / 640.0f));
      tw320_init(tw, lane, u0, sc);
      wu[0] = c2{u0.y, u0.x};
      wu[1] = cmul(wu[0], c2{0.80901699437494742f, 0.58778525229247313f});
      wu[2] = cmul(wu[0], c2{0.30901699437494742f, 0.95105651629515357f});
      wu[3] = cmul(wu[0], c2{-0.30901699437494742f, 0.95105651629515357f});
      wu[4] = cmul(wu[0], c2{-0.80901699437494742f, 0.58778525229247313f});
    }
    SCHED_FENCE();
    G1_DMA(2); G1_DMA(3); G1_DMA(4);
    PT(4);
    asm volatile("s_waitcnt vmcnt(20)" : "+v"(rw[0]), "+v"(rw[1]), "+v"(rw[2]) :: "memory");
    PT(5);
#pragma unroll
    for (int i = 0; i < 3; i++) {
      int q = lane + 64 * i;
      if (i == 2) q = q < 160 ? q : 159;
      *(u4*)&buf[2 * q] = u4{rw[i].x, rw[i].z, rw[i].y, rw[i].w};
    }
    lds_fence();
    c2 z[2][5], o[2][8];
    u2 ua[5], ub[5];
#pragma unroll
    for (int j = 0; j < 5; j++) {
      int k = lane + 64 * j;
      ua[j] = buf[k]; ub[j] = buf[(j == 0 && k == 0) ? 0 : 320 - k];
    }
    SCHED_FENCE();
    G1_LOADW(0); G1_LOADW(1);
#pragma unroll
    for (int j = 0; j < 5; j++) {
      c2 A0 = c2{bf_lo(ua[j].x), bf_hi(ua[j].x)}, B0 = c2{bf_lo(ub[j].x), bf_hi(ub[j].x)};
      c2 A1 = c2{bf_lo(ua[j].y), bf_hi(ua[j].y)}, B1 = c2{bf_lo(ub[j].y), bf_hi(ub[j].y)};
      if (j == 0 && lane == 0) {
        B0 = c2{A0.y * r2, 0.f}; A0 = c2{A0.x * r2, 0.f};
        B1 = c2{A1.y * r2, 0.f}; A1 = c2{A1.x * r2, 0.f};
      }
      tangle2(z[0][j], z[1][j], A0, B0, A1, B1, wu[j]);
    }
    fft320x2<1>(z, tw, sc, scr, lane, o, [&](int h) {
      if (h == 0) { G1_LOADW(2); G1_LOADW(3); }
      if (h == 2) { G1_LOADW(4); G1_LOADW(5); }
    });
    PT(6);
#pragma unroll
    for (int rr = 0; rr < 2; rr++)
#pragma unroll
      for (int e = 0; e < 8; e++) pk3[rr][e] = pack_bf(o[rr][e].x, o[rr][e].y);
    SCHED_FENCE();
#pragma unroll
    for (int s = 6; s < PF; s++) G1_LOADW(s);
  }
  const rsrc_t Hr = make_rsrc(ws + OFF_H + (size_t)mblk * 4 * H_TS * 1024, 4 * H_TS * 1024);
  const int G = fs * 2 + (wv >> 1);
  int hfull[4], hhalf[4];
#pragma unroll
  for (int m = 0; m < 4; m++) {
    hfull[m] = (((m + wv) & 3) * H_TS + 3 * G + (wv & 1)) * 1024; hhalf[m] = (((m + wv) & 3) * H_TS + 3 * G + 2) * 1024;
    asm("" : "+s"(hfull[m]), "+s"(hhalf[m]));
  }
  SCHED_FENCE();
  asm volatile("s_waitcnt vmcnt(%0)\n\ts_barrier" :: "n"(3 * PF) : "memory");
  if (WITH_INV) {
    const rsrc_t Xr = make_rsrc(ws + OFF_X3 + (size_t)(mblk * 64 + m0) * 1280, 2560);
    const unsigned vo = (iq + 5 * ia) * 4;
#pragma unroll
    for (int rr = 0; rr < 2; rr++)
#pragma unroll
      for (int e = 0; e < 8; e++) __builtin_amdgcn_raw_buffer_store_b32(pk3[rr][e], Xr, vo + (rr * 320 + 40 * e) * 4, 0, AUX_X3);
    SCHED_FENCE();
  }
  PT(1);
  const int g = lane >> 4, r = lane & 15;
  const int swz = (g & 1) << 7;
  typedef __attribute__((address_space(3))) const char* ldsc;
  typedef __attribute__((address_space(3))) G1_VOL const u2* ldsu2;
  ldsc abase[4], abase2[4];
#pragma unroll
  for (int m = 0; m < 4; m++) {
    unsigned o = (2 * g) * 512 + (((16 * ((m + wv) & 3) + r) * 8) ^ swz), o2 = o + 65536;
    asm("" : "+v"(o2));
    abase[m] = (ldsc)smem + o; abase2[m] = (ldsc)smem + o2;
  }
  SCHED_FENCE();
  f4 acc[3][4];
#pragma unroll
  for (int t = 0; t < 3; t++)
#pragma unroll
    for (int m = 0; m < 4; m++) acc[t][m] = f4{0.f, 0.f, 0.f, 0.f};
  u4 af[20][4];
#define G1_READA(KS, M) do { ldsc ap_ = (KS) < 16 ? abase[M] + (KS) * 4096 : abase2[M] + ((KS) - 16) * 4096; \
    u2 lo_ = *(ldsu2)ap_, hi_ = *(ldsu2)(ap_ + 512); af[KS][M] = u4{lo_.x, lo_.y, hi_.x, hi_.y}; } while (0)
#define G1_MFMA(ACC, W, B) ACC = __builtin_amdgcn_mfma_f32_16x16x32_bf16(W, __builtin_bit_cast(bf8, B), ACC, 0, 0, 0)
  G1_READA(0, 0); G1_READA(1, 0); G1_READA(0, 1); G1_READA(0, 2); G1_READA(0, 3);
  G1_READA(2, 0); G1_READA(3, 0); G1_READA(1, 1); G1_READA(1, 2); G1_READA(1, 3);
  typedef __attribute__((address_space(3))) volatile float* ldsf;
  ldsf stw = (ldsf)((ldsc)smem + G1_A_BYTES) + (g == (r >> 2) ? wv * 16 + r : 64 + (lane & 31));
  ldsf str = (ldsf)((ldsc)smem + G1_A_BYTES) + r;
  float rl[4], irs[4], e0, e1;
  unsigned pk[3][4][2];
  SCHED_FENCE();
  f4 gram = f4{0.f, 0.f, 0.f, 0.f};
  constexpr int NP1 = G1_NP1;
  static_for<20>([&](auto kI) {
    constexpr int ks = kI.value;
    constexpr int l0 = NL + (NP1 - NL) * ks / 20, l1 = NL + (NP1 - NL) * (ks + 1) / 20;
    constexpr int infl = (ks == 0 ? 10 : ks <= 8 ? 10 : ks == 12 ? 7 : ks == 14 ? 10 : ks < 19 ? 6 : 0);
    __builtin_amdgcn_s_waitcnt(0xc07f | ((infl & 15) << 8));
    SCHED_FENCE();
    if (ks < 10) {
      u4 ga = af[2 * ks][0];
      if (ks == 0 && g == 0) ga.x &= 0xffff0000u;
      G1_MFMA(gram, __builtin_bit_cast(bf8, ga), af[2 * ks][0]);
      if (ks < 8) G1_READA(ks < 8 ? 2 * ks + 4 : 0, 0);
      SCHED_FENCE();
    }
    static_for<4>([&](auto mI) {
      constexpr int m = mI.value;
      G1_MFMA(acc[0][m], wf[ks], af[ks][m]);
      if (m == 0 && ks < 8) G1_READA(ks < 8 ? 2 * ks + 5 : 0, 0);
      if (m > 0 && ks + 2 < 20) G1_READA(ks + 2 < 20 ? ks + 2 : 0, m);
      if (m == 0 && l0 < l1) G1_LOAD1(l0);
      if (m == 2 && l0 + 1 < l1) G1_LOAD1(l0 + 1);
      if (m == 0 && ks == 11) {
        float sv = (r & 2) ? ((r & 1) ? gram.w : gram.z) : ((r & 1) ? gram.y : gram.x);
        *stw = sv * (1.0f / 640.0f) + LN_EPS;
      }
      if (m == 0 && ks == 13) {
        asm volatile("s_barrier" ::: "memory");
#pragma unroll
        for (int q = 0; q < 4; q++) irs[q] = str[((q + wv) & 3) * 16];
      }
      if (m == 0 && ks >= 15 && ks < 19) {
        constexpr int q = (ks - 15) & 3;
        float rs = __builtin_amdgcn_rsqf(irs[q]);
        asm("s_nop 0\n\tv_mul_f32 %0, %0, %2\n\tv_mul_f32 %1, 0xbfb8aa3b, %2" : "+v"(irs[q]), "=v"(rl[q]) : "v"(rs));
      }
      SCHED_FENCE();
      if (m == 1 && ks < 10) {
        G1_MFMA(gram, __builtin_bit_cast(bf8, af[2 * ks + 1][0]), af[2 * ks + 1][0]);
        SCHED_FENCE();
      }
    });
  });
  PT(2);
  const unsigned l16h = l16 + (wv & 1) * 8;
  float a0, a1;
  auto silu_op = [&](auto cI, auto oI) {
    constexpr int c = cI.value, o = oI.value, t = c >> 3, m = (c >> 1) & 3, e = 2 * (c & 1);
    if (o == 0) { a0 = acc[t][m][e]; a1 = acc[t][m][e + 1]; asm("" : "+v"(a0), "+v"(a1)); }
    if (o == 1) asm("v_mul_f32 %0, %2, %4\n\tv_mul_f32 %1, %3, %4" : "=&v"(e0), "=&v"(e1) : "v"(a0), "v"(a1), "v"(rl[m]));
    if (o == 2) asm("v_exp_f32 %0, %0" : "+v"(e0));
    if (o == 3) asm("v_exp_f32 %0, %0" : "+v"(e1));
    if (o == 4) asm("v_fma_f32 %0, %0, %2, %2\n\tv_fma_f32 %1, %1, %2, %2" : "+v"(e0), "+v"(e1) : "v"(irs[m]));
    if (o == 5) asm("v_fma_f32 %0, %0, %2, %2\n\tv_fma_f32 %1, %1, %2, %2\n\tv_rcp_f32 %0, %0" : "+v"(e0), "+v"(e1) : "v"(irs[m]));
    if (o == 6) asm("v_rcp_f32 %0, %0" : "+v"(e0));
    if (o == 7) asm("v_rcp_f32 %0, %0" : "+v"(e1));
    if (o == 8) asm("v_rcp_f32 %1, %1\n\tv_mul_f32 %0, %0, %2" : "+v"(e0), "+v"(e1) : "v"(a0));
    if (o == 9) asm("v_mul_f32 %0, %0, %2\n\tv_mul_f32 %1, %1, %3" : "+v"(e0), "+v"(e1) : "v"(a0), "v"(a1));
    if (o == 10) asm("v_cvt_pk_bf16_f32 %0, %1, %2" : "=v"(pk[t][m][c & 1]) : "v"(e0), "v"(e1));
    if (o == 11) asm("v_mul_f32 %2, %2, %3\n\tv_cvt_pk_bf16_f32 %0, %1, %2" : "=v"(pk[t][m][c & 1]), "+v"(e0), "+v"(e1) : "v"(a1));
    if (o == 10 || o == 11) {
      if (t == 1 && (c & 1))
        __builtin_amdgcn_raw_buffer_store_b128(u4{pk[0][m][0], pk[0][m][1], pk[1][m][0], pk[1][m][1]}, Hr, l16, hfull[m], ST_AUX);
      if (t == 2 && (c & 1))
        __builtin_amdgcn_raw_buffer_store_b64(u2{pk[2][m][0], pk[2][m][1]}, Hr, l16h, hhalf[m], ST_AUX);
    }
    SCHED_FENCE();
  };
#define SOP(C, O) silu_op(ic<(C)>{}, ic<(O)>{})
  static_for<160>([&](auto jI) {
    constexpr int j = jI.value;
    constexpr int t = j < 80 ? 1 : 2;
    constexpr int ks = j < 40 ? j >> 2 : j < 80 ? 10 + ((j - 40) % 20) / 2 : ((j - 80) % 40) >> 1;
    constexpr int m = j < 40 ? j & 3 : j < 80 ? ((j - 40) / 20) * 2 + (j & 1) : ((j - 80) / 40) * 2 + (j & 1);
    G1_MFMA(acc[t][m], wf[20 * t + ks], af[ks][m]);
    SCHED_FENCE();
    constexpr int c = j >> 3, o = j & 7;
    if (j == 0) SOP(0, 0);
    if (o == 0) SOP(c, 1);
    if (o == 1) SOP(c, 2);
    if (o == 2) SOP(c, 3);
    if (o == 3) SOP(c, 4);
    if (o == 4) SOP(c, 6);
    if (o == 5) SOP(c, 7);
    if (o == 6) SOP(c, 9);
    if (o == 7) { SOP(c, 10); if (c < 19) SOP(c + 1, 0); }
    if (j % G1_LS == 3) G1_LOAD1(NP1 + j / G1_LS);
    if (j == 80) PT(3);
    SCHED_FENCE();
  });
  PT(4 + (WITH_INV ? 3 : 0));
  {
    float x[8];
#pragma unroll
    for (int i = 0; i < 8; i++) x[i] = acc[2][2 + (i >> 2)][i & 3] * rl[2 + (i >> 2)];
#pragma unroll
    for (int i = 0; i < 8; i++) x[i] = __builtin_amdgcn_exp2f(x[i]);
#pragma unroll
    for (int i = 0; i < 8; i++) x[i] = __builtin_amdgcn_rcpf(__builtin_fmaf(x[i], irs[2 + (i >> 2)], irs[2 + (i >> 2)])) * acc[2][2 + (i >> 2)][i & 3];
#pragma unroll
    for (int m = 2; m < 4; m++)
      __builtin_amdgcn_raw_buffer_store_b64(u2{pack_bf(x[4 * m - 8], x[4 * m - 7]), pack_bf(x[4 * m - 6], x[4 * m - 5])}, Hr, l16h, hhalf[m], ST_AUX);
  }
  PT(7); PT_FLUSH(KID);
}

template <bool FINAL>
__global__ __launch_bounds__(256, 1) void k_gemm2(char* __restrict__ ws, __hip_bfloat16* __restrict__ out) {
  __shared__ __attribute__((aligned(16))) f4 red[4 * 3 * 5 * 64];
  const int tid = threadIdx.x, lane = tid & 63;
  const int wv = __builtin_amdgcn_readfirstlane(tid >> 6);
  const int b = blockIdx.x;
  const int xcd = b & 7, slot = b >> 3;
  const int mblk = (xcd >> 1) * 8 + (slot & 7), cs = (xcd & 1) * 4 + (slot >> 3);
  const int g = lane >> 4, r = lane & 15;
  constexpr int KID = FINAL ? 6 : 3;
  (void)KID;
  asm volatile("" ::: "a191");
  PT_DECL; PT(0);
  const unsigned l16 = lane * 16;
  const rsrc_t Hr = make_rsrc(ws + OFF_H + ((size_t)mblk * 4 * H_TS + wv * 12) * 1024, (4 * H_TS - wv * 12) * 1024);
  const rsrc_t Wr = make_rsrc(ws + (FINAL ? OFF_W2C : OFF_W2P) + ((size_t)cs * 5 * H_TS + wv * 12) * 1024, (5 * H_TS - wv * 12) * 1024);
  int hoff[4];
#pragma unroll
  for (int m = 0; m < 4; m++) hoff[m] = ((m + wv) & 3) * H_TS * 1024;
  constexpr int PF = G2_PF, ND = G2_ND, S0 = 12 - ND;
  bf8 hf[12][4], wf[12][5];
#define G2_LH(S, M) hf[S][M] = __builtin_bit_cast(bf8, __builtin_amdgcn_raw_buffer_load_b128(Hr, l16 + ((S) & 3) * 1024, hoff[M] + ((S) >> 2) * 4096, 0))
#define G2_LW(S, C) wf[S][C] = bload(Wr, l16, ((C) * H_TS + (S)) * 1024)
#define G2_LOAD(S, J) do { constexpr int s_ = (S), j_ = (J); \
    if (s_ < 12) { constexpr int q_ = s_ < 12 ? s_ : 0; \
      if (j_ == 0) { G2_LH(q_, 1); G2_LW(q_, 0); } if (j_ == 1) { G2_LH(q_, 2); G2_LW(q_, 1); } if (j_ == 2) { G2_LH(q_, 3); G2_LW(q_, 2); } \
      if (j_ == 3) { G2_LW(q_, 3); if (q_ < S0) G2_LH(q_, 0); } if (j_ == 4) G2_LW(q_, 4); } \
    else { constexpr int per_ = (ND + PF - 1) / PF, i_ = (s_ - 12) * per_ + j_; \
      if (j_ < per_ && i_ < ND) G2_LH(S0 + (i_ < ND ? i_ : 0), 0); \
      if (j_ == 4) { _Pragma("unroll") for (int e_ = 5; e_ < per_; e_++) if ((s_ - 12) * per_ + e_ < ND) G2_LH(S0 + ((s_ - 12) * per_ + e_) % ND, 0); } } \
    } while (0)
  const int mrow = mblk * 64 + wv * 16 + r;
  u2 res[5];
  {
    const rsrc_t Rr = FINAL ? make_rsrc(ws + OFF_X3, 2048 * 1280) : make_rsrc(ws + OFF_XB + (size_t)mblk * XB_MS, XB_MS);
    const unsigned ro = FINAL ? (mrow * 640 + 4 * g) * 2 : g * 512 + (((wv * 16 + r) * 8) ^ ((g >> 1) << 7));
    static_for<PF>([&](auto sI) {
      constexpr int s = sI.value;
      G2_LOAD(s, 0); G2_LOAD(s, 1); G2_LOAD(s, 2); G2_LOAD(s, 3); G2_LOAD(s, 4);
      SCHED_FENCE();
      if (s == 0) {
#pragma unroll
        for (int c = 0; c < 5; c++)
          res[c] = FINAL ? __builtin_amdgcn_raw_buffer_load_b64(Rr, ro + c * 32, cs * 160, 0)
                         : __builtin_amdgcn_raw_buffer_load_b64(Rr, ro + (c & 1) * 2048, cs * 10240 + (c >> 1) * 4096, 0);
        SCHED_FENCE();
      }
    });
  }
  PT(6);
  f4 acc[5][4];
#pragma unroll
  for (int c = 0; c < 5; c++)
#pragma unroll
    for (int m = 0; m < 4; m++) acc[c][m] = f4{0.f, 0.f, 0.f, 0.f};
#define G2_MFMA(S, C, M) acc[C][M] = __builtin_amdgcn_mfma_f32_16x16x32_bf16(wf[S][C], hf[S][M], acc[C][M], 0, 0, 0)
  f4* rw[3];
#pragma unroll
  for (int m = 1; m < 4; m++) rw[m - 1] = red + (((m + wv) & 3) * 3 + m - 1) * 5 * 64 + lane;
#define G2_WRITE(I) do { if ((I) >= 0 && (I) < 15) rw[(I) / 5][((I) % 5) * 64] = acc[(I) % 5][1 + (I) / 5]; } while (0)
  static_for<11>([&](auto sI) {
    constexpr int s = sI.value;
    static_for<5>([&](auto cI) {
      constexpr int c = cI.value;
      G2_MFMA(s, c, 1);
      if (s == 0 && c == 0) PT(2);
      G2_MFMA(s, c, 2); G2_MFMA(s, c, 3);
      if (s < S0) G2_MFMA(s, c, 0);
      G2_LOAD(s + PF, c);
      SCHED_FENCE();
    });
    if (s == 0) PT(1);
  });
  constexpr int NM = 15 + (S0 == 12 ? 1 : ND) * 5, W0 = 7, NB = W0 + 15 * G2_WS + G2_BD;
  f4 part[15];
  static_for<NM>([&](auto iI) {
    constexpr int i = iI.value;
    if (i < 15) G2_MFMA(11, i % 5, 1 + i / 5);
    else G2_MFMA((S0 == 12 ? 11 : S0) + (i - 15) / 5, i % 5, 0);
    if (i < 5) G2_LOAD(11 + PF, i);
    if (i >= W0 && (i - W0) % G2_WS == 0) G2_WRITE((i - W0) / G2_WS);
    if (i == 15) PT(3);
    if (i == (NB < NM ? NB : NM - 1)) {
#pragma unroll
      for (int k = (NM - 1 - W0) / G2_WS + 1; k < 15; k++) G2_WRITE(k);
      asm volatile("s_waitcnt lgkmcnt(0)\n\ts_barrier" ::: "memory");
      PT(4);
#pragma unroll
      for (int k = 0; k < 15; k++) part[k] = red[(wv * 15 + k) * 64 + lane];
    }
    SCHED_FENCE();
  });
  PT(5);
  f4 own[5];
#pragma unroll
  for (int c = 0; c < 5; c++) own[c] = (part[c] + part[5 + c]) + (part[10 + c] + acc[c][0]);
  float s = 0.f;
#pragma unroll
  for (int c = 0; c < 5; c++) {
    f4 o = f4{bf_lo(res[c].x), bf_hi(res[c].x), bf_lo(res[c].y), bf_hi(res[c].y)} + own[c];
    u2 pk = u2{pack_bf(o.x, o.y), pack_bf(o.z, o.w)};
    if (FINAL) {
      __builtin_amdgcn_raw_buffer_store_b64(pk, make_rsrc(out, 2048 * 1280), (mrow * 640 + 4 * g) * 2 + c * 32, cs * 160, AUX_G2);
    } else {
      __builtin_amdgcn_raw_buffer_store_b64(pk, make_rsrc(ws + OFF_XB + (size_t)mblk * XB_MS, XB_MS), g * 512 + (((wv * 16 + r) * 8) ^ ((g >> 1) << 7)) + (c & 1) * 2048, cs * 10240 + (c >> 1) * 4096, AUX_G2);
      unsigned p0 = (cs == 0 && c == 0 && g == 0) ? (pk.x & 0xffff0000u) : pk.x;
      s = dot2(p0, s);
      s = dot2(pk.y, s);
    }
  }
  if (!FINAL) {
    s += __shfl_xor(s, 16);
    s += __shfl_xor(s, 32);
    if (g == 0) unsafeAtomicAdd((double*)(ws + OFF_SSQ) + mrow, (double)s);
  }
  if (FINAL) asm volatile("k6_code_end:");
  PT(7); PT_FLUSH(KID);
}

extern "C" void kernel_launch(void* const* d_in, const int* in_sizes, int n_in, void* d_out, int out_size, void* d_ws,
                              size_t ws_size, hipStream_t stream) {
  const uint16_t* x = (const uint16_t*)d_in[0];
  const uint16_t* wr = (const uint16_t*)d_in[1];
  const uint16_t* wi = (const uint16_t*)d_in[2];
  const uint16_t* w1 = (const uint16_t*)d_in[4];
  const uint16_t* w2 = (const uint16_t*)d_in[5];
  char* ws = (char*)d_ws;
  __hip_bfloat16* out = (__hip_bfloat16*)d_out;
  (void)hipFuncSetAttribute((const void*)k_colfilter<true>, hipFuncAttributeMaxDynamicSharedMemorySize, LDS_CF);
  (void)hipFuncSetAttribute((const void*)k_colfilter<false>, hipFuncAttributeMaxDynamicSharedMemorySize, LDS_CF);
  (void)hipFuncSetAttribute((const void*)k_gemm1<false>, hipFuncAttributeMaxDynamicSharedMemorySize, LDS_G1);
  (void)hipFuncSetAttribute((const void*)k_gemm1<true>, hipFuncAttributeMaxDynamicSharedMemorySize, LDS_G1I);
#define NK 7
  k_prep<<<K0_X + K0_W1 + K0_WT, 512, 0, stream>>>(x, wr, wi, w1, w2, (const char*)d_out, ws);
  if (NK > 1) k_colfilter<true><<<256, 256, LDS_CF, stream>>>(w2, ws);
  if (NK > 2) k_gemm1<false><<<256, 256, LDS_G1, stream>>>(ws);
  if (NK > 3) k_gemm2<false><<<256, 256, 0, stream>>>(ws, out);
  if (NK > 4) k_colfilter<false><<<256, 256, LDS_CF, stream>>>(w2, ws);
  if (NK > 5) k_gemm1<true><<<256, 256, LDS_G1I, stream>>>(ws);
  if (NK > 6) k_gemm2<true><<<256, 256, 0, stream>>>(ws, out);
}
```

```cpp
#pragma clang diagnostic ignored "-Warray-bounds"
#include <hip/hip_runtime.h>
#include <hip/hip_bf16.h>
#include <stdint.h>

typedef float c2 __attribute__((ext_vector_type(2)));
typedef float f4 __attribute__((ext_vector_type(4)));
typedef unsigned u2 __attribute__((ext_vector_type(2)));
typedef unsigned u4 __attribute__((ext_vector_type(4)));
typedef __bf16 bf2 __attribute__((ext_vector_type(2)));
typedef __bf16 bf8 __attribute__((ext_vector_type(8)));

#define DEV __device__ __forceinline__
#define SCHED_FENCE() __builtin_amdgcn_sched_barrier(0)

constexpr int S_ = 2048, D_ = 640, F_ = 1536, DF_ = 321;
constexpr float LN_EPS = 1e-5f;

constexpr int XB_MS = 81920 + 512;
constexpr int H_TS = 49;
constexpr int W1_TS = 21;
constexpr size_t OFF_XB = 0;
constexpr size_t OFF_H = OFF_XB + 32ull * XB_MS;
constexpr size_t OFF_W1P = OFF_H + 128ull * H_TS * 1024;
constexpr size_t OFF_W2P = OFF_W1P + 96ull * W1_TS * 1024;
constexpr size_t OFF_W2C = OFF_W2P + 40ull * H_TS * 1024;
constexpr size_t OFF_WT = OFF_W2C + 40ull * H_TS * 1024;
constexpr size_t OFF_X3 = OFF_WT + 160ull * 4 * 256 * 16;
constexpr size_t OFF_RS0 = OFF_X3 + 2048ull * 640 * 2;
constexpr size_t OFF_SSQ = OFF_RS0 + 2048 * 4;

#define TSTAMP(K, P) do {} while (0)
#define PT_DECL do {} while (0)
#define PT(i) do {} while (0)
#define PT_FLUSH(K) do {} while (0)
#define ST_AUX 16
#define AUX_X3 16
#define AUX_G2 16
#define AUX_K0 16
#define AUX_W1 16
#define AUX_CF 16
typedef __amdgpu_buffer_rsrc_t rsrc_t;
DEV rsrc_t make_rsrc(const void* p, unsigned bytes) { return __builtin_amdgcn_make_buffer_rsrc((void*)p, 0, bytes, 0x00020000); }
DEV bf8 bload(rsrc_t rs, unsigned voff, int OFF) {
  return __builtin_bit_cast(bf8, __builtin_amdgcn_raw_buffer_load_b128(rs, voff + (OFF & 4095), OFF & ~4095, 0));
}
DEV c2 add_mi(c2 a, c2 b) {
  c2 d; asm("v_pk_add_f32 %0, %1, %2 op_sel:[0,1] op_sel_hi:[1,0] neg_hi:[0,1]" : "=v"(d) : "v"(a), "v"(b)); return d;
}
DEV c2 add_pi(c2 a, c2 b) {
  c2 d; asm("v_pk_add_f32 %0, %1, %2 op_sel:[0,1] op_sel_hi:[1,0] neg_lo:[0,1]" : "=v"(d) : "v"(a), "v"(b)); return d;
}
DEV c2 cmul(c2 a, c2 w) {
  c2 t, d;
  asm("v_pk_mul_f32 %0, %1, %2 op_sel:[0,0] op_sel_hi:[0,1]" : "=v"(t) : "v"(a), "v"(w));
  asm("v_pk_fma_f32 %0, %1, %2, %3 op_sel:[1,1,0] op_sel_hi:[1,0,1] neg_lo:[0,1,0]" : "=v"(d) : "v"(a), "v"(w), "v"(t));
  return d;
}
DEV c2 cmulc(c2 a, c2 w) {
  c2 t, d;
  asm("v_pk_mul_f32 %0, %1, %2 op_sel:[0,0] op_sel_hi:[0,1] neg_hi:[0,1]" : "=v"(t) : "v"(a), "v"(w));
  asm("v_pk_fma_f32 %0, %1, %2, %3 op_sel:[1,1,0] op_sel_hi:[1,0,1]" : "=v"(d) : "v"(a), "v"(w), "v"(t));
  return d;
}
template <int SGN> DEV c2 twmul(c2 a, c2 w) { return SGN < 0 ? cmul(a, w) : cmulc(a, w); }
template <int SGN> DEV c2 add_rot(c2 a, c2 b) { return SGN < 0 ? add_mi(a, b) : add_pi(a, b); }
template <int SGN> DEV c2 sub_rot(c2 a, c2 b) { return SGN < 0 ? add_pi(a, b) : add_mi(a, b); }
DEV c2 twiddle_rev(float rev) {
  c2 t = c2{__builtin_amdgcn_cosf(rev), -__builtin_amdgcn_sinf(rev)};
  asm("s_nop 1" : "+v"(t));
  return t;
}
template <int N> DEV void tw_powers(c2* t, c2 w) {
  t[0] = w;
#pragma unroll
  for (int k = 2; k <= N; k++) {
    if ((k & 1) == 0) t[k - 1] = cmul(t[k / 2 - 1], t[k / 2 - 1]);
    else {
      int hi = k >= 8 ? 8 : (k >= 4 ? 4 : 2);
      t[k - 1] = cmul(t[hi - 1], t[k - hi - 1]);
    }
  }
}

template <int SGN> DEV void dft4(c2& a, c2& b, c2& c, c2& d) {
  c2 s0 = a + c, s1 = a - c, s2 = b + d, s3 = b - d;
  a = s0 + s2; c = s0 - s2;
  b = add_rot<SGN>(s1, s3); d = sub_rot<SGN>(s1, s3);
}
template <int SGN> DEV c2 rot1(c2 a) { return add_rot<SGN>(a, a) * 0.70710678118654752f; }
template <int SGN> DEV c2 rot3(c2 a) { return sub_rot<SGN>(a, a) * -0.70710678118654752f; }

template <int SGN> DEV void dft8(c2* v) {
  c2 e0 = v[0], e1 = v[2], e2 = v[4], e3 = v[6], o0 = v[1], o1 = v[3], o2 = v[5], o3 = v[7];
  dft4<SGN>(e0, e1, e2, e3);
  dft4<SGN>(o0, o1, o2, o3);
  o1 = rot1<SGN>(o1); o3 = rot3<SGN>(o3);
  v[0] = e0 + o0; v[4] = e0 - o0;
  v[1] = e1 + o1; v[5] = e1 - o1;
  v[2] = add_rot<SGN>(e2, o2); v[6] = sub_rot<SGN>(e2, o2);
  v[3] = e3 + o3; v[7] = e3 - o3;
}
template <int SGN> DEV void dft16(c2* v) {
  const c2 w1 = c2{0.92387953251128674f, -0.38268343236508977f};
  const c2 w3 = c2{0.38268343236508977f, -0.92387953251128674f};
  c2 y[4][4];
#pragma unroll
  for (int i = 0; i < 4; i++) {
    c2 a = v[i], b = v[i + 4], c = v[i + 8], d = v[i + 12];
    dft4<SGN>(a, b, c, d);
    y[i][0] = a; y[i][1] = b; y[i][2] = c; y[i][3] = d;
  }
  y[1][1] = twmul<SGN>(y[1][1], w1);
  y[1][2] = rot1<SGN>(y[1][2]);
  y[1][3] = twmul<SGN>(y[1][3], w3);
  y[2][1] = rot1<SGN>(y[2][1]);
  y[2][3] = rot3<SGN>(y[2][3]);
  y[3][1] = twmul<SGN>(y[3][1], w3);
  y[3][2] = rot3<SGN>(y[3][2]);
  y[3][3] = -twmul<SGN>(y[3][3], w1);
#pragma unroll
  for (int q = 0; q < 4; q++) {
    c2 a = y[0][q], b = y[1][q], c = y[2][q], d = y[3][q];
    if (q == 2) {
      c2 s0 = add_rot<SGN>(a, c), s1 = sub_rot<SGN>(a, c), s2 = b + d, s3 = b - d;
      a = s0 + s2; c = s0 - s2;
      b = add_rot<SGN>(s1, s3); d = sub_rot<SGN>(s1, s3);
    } else {
      dft4<SGN>(a, b, c, d);
    }
    v[q] = a; v[q + 4] = b; v[q + 8] = c; v[q + 12] = d;
  }
}
template <int SGN> DEV void twmulp(c2& a0, c2 w0, c2& a1, c2 w1) {
  c2 d0, d1;
  if (SGN < 0)
    asm("v_pk_mul_f32 %0, %2, %4 op_sel:[0,0] op_sel_hi:[0,1]\n\t"
        "v_pk_mul_f32 %1, %3, %5 op_sel:[0,0] op_sel_hi:[0,1]\n\t"
        "v_pk_fma_f32 %0, %2, %4, %0 op_sel:[1,1,0] op_sel_hi:[1,0,1] neg_lo:[0,1,0]\n\t"
        "v_pk_fma_f32 %1, %3, %5, %1 op_sel:[1,1,0] op_sel_hi:[1,0,1] neg_lo:[0,1,0]"
        : "=&v"(d0), "=&v"(d1) : "v"(a0), "v"(a1), "v"(w0), "v"(w1));
  else
    asm("v_pk_mul_f32 %0, %2, %4 op_sel:[0,0] op_sel_hi:[0,1] neg_hi:[0,1]\n\t"
        "v_pk_mul_f32 %1, %3, %5 op_sel:[0,0] op_sel_hi:[0,1] neg_hi:[0,1]\n\t"
        "v_pk_fma_f32 %0, %2, %4, %0 op_sel:[1,1,0] op_sel_hi:[1,0,1]\n\t"
        "v_pk_fma_f32 %1, %3, %5, %1 op_sel:[1,1,0] op_sel_hi:[1,0,1]"
        : "=&v"(d0), "=&v"(d1) : "v"(a0), "v"(a1), "v"(w0), "v"(w1));
  a0 = d0; a1 = d1;
}
template <int SGN, typename PRE, typename POST> DEV void dft16s(c2* v, PRE pre, POST post) {
  const c2 w1 = c2{0.92387953251128674f, -0.38268343236508977f};
  const c2 w3 = c2{0.38268343236508977f, -0.92387953251128674f};
  const c2 w9 = c2{-0.92387953251128674f, 0.38268343236508977f};
  c2 y[4][4];
#pragma unroll
  for (int i = 0; i < 4; i++) {
    pre(i);
    c2 a = v[i], b = v[i + 4], c = v[i + 8], d = v[i + 12];
    dft4<SGN>(a, b, c, d);
    y[i][0] = a; y[i][1] = b; y[i][2] = c; y[i][3] = d;
    if (i == 1) { twmulp<SGN>(y[1][1], w1, y[1][3], w3); y[1][2] = rot1<SGN>(y[1][2]); }
    if (i == 2) { y[2][1] = rot1<SGN>(y[2][1]); y[2][3] = rot3<SGN>(y[2][3]); }
    if (i == 3) { twmulp<SGN>(y[3][1], w3, y[3][3], w9); y[3][2] = rot3<SGN>(y[3][2]); }
    SCHED_FENCE();
  }
#pragma unroll
  for (int q = 0; q < 4; q++) {
    c2 a = y[0][q], b = y[1][q], c = y[2][q], d = y[3][q];
    if (q == 2) {
      c2 s0 = add_rot<SGN>(a, c), s1 = sub_rot<SGN>(a, c), s2 = b + d, s3 = b - d;
      a = s0 + s2; c = s0 - s2;
      b = add_rot<SGN>(s1, s3); d = sub_rot<SGN>(s1, s3);
    } else {
      dft4<SGN>(a, b, c, d);
    }
    v[q] = a; v[q + 4] = b; v[q + 8] = c; v[q + 12] = d;
    post(q);
    SCHED_FENCE();
  }
}
template <int SGN> DEV void dft5(c2* x) {
  const float c1 = 0.30901699437494742f, c2_ = -0.80901699437494742f;
  const float s1 = 0.95105651629515357f, s2 = 0.58778525229247313f;
  c2 t1 = x[1] + x[4], t2 = x[2] + x[3], t3 = x[1] - x[4], t4 = x[2] - x[3];
  c2 a1 = x[0] + c1 * t1 + c2_ * t2, a2 = x[0] + c2_ * t1 + c1 * t2;
  c2 b1 = s1 * t3 + s2 * t4, b2 = s2 * t3 - s1 * t4;
  x[0] = x[0] + t1 + t2;
  x[1] = add_rot<SGN>(a1, b1); x[4] = sub_rot<SGN>(a1, b1);
  x[2] = add_rot<SGN>(a2, b2); x[3] = sub_rot<SGN>(a2, b2);
}

DEV void lds_fence() { asm volatile("" ::: "memory"); __builtin_amdgcn_wave_barrier(); asm volatile("" ::: "memory"); }
DEV float bf_lo(unsigned u) { return __builtin_bit_cast(float, u << 16); }
DEV float bf_hi(unsigned u) { return __builtin_bit_cast(float, u & 0xffff0000u); }
DEV unsigned pack_bf(float a, float b) { return __builtin_bit_cast(unsigned, __builtin_convertvector(c2{a, b}, bf2)); }

template <int SGN> DEV void twmul2(c2& a0, c2& a1, c2 w) {
  c2 d0, d1;
  if (SGN < 0)
    asm("v_pk_mul_f32 %0, %2, %4 op_sel:[0,0] op_sel_hi:[0,1]\n\t"
        "v_pk_mul_f32 %1, %3, %4 op_sel:[0,0] op_sel_hi:[0,1]\n\t"
        "v_pk_fma_f32 %0, %2, %4, %0 op_sel:[1,1,0] op_sel_hi:[1,0,1] neg_lo:[0,1,0]\n\t"
        "v_pk_fma_f32 %1, %3, %4, %1 op_sel:[1,1,0] op_sel_hi:[1,0,1] neg_lo:[0,1,0]"
        : "=&v"(d0), "=&v"(d1) : "v"(a0), "v"(a1), "v"(w));
  else
    asm("v_pk_mul_f32 %0, %2, %4 op_sel:[0,0] op_sel_hi:[0,1] neg_hi:[0,1]\n\t"
        "v_pk_mul_f32 %1, %3, %4 op_sel:[0,0] op_sel_hi:[0,1] neg_hi:[0,1]\n\t"
        "v_pk_fma_f32 %0, %2, %4, %0 op_sel:[1,1,0] op_sel_hi:[1,0,1]\n\t"
        "v_pk_fma_f32 %1, %3, %4, %1 op_sel:[1,1,0] op_sel_hi:[1,0,1]"
        : "=&v"(d0), "=&v"(d1) : "v"(a0), "v"(a1), "v"(w));
  a0 = d0; a1 = d1;
}
DEV void tangle2(c2& d0, c2& d1, c2 A0, c2 B0, c2 A1, c2 B1, c2 w) {
  c2 e0, e1, o0, o1;
  asm("v_pk_add_f32 %0, %4, %5 neg_hi:[0,1]\n\t"
      "v_pk_add_f32 %1, %6, %7 neg_hi:[0,1]\n\t"
      "v_pk_add_f32 %2, %4, %5 neg_lo:[0,1]\n\t"
      "v_pk_add_f32 %3, %6, %7 neg_lo:[0,1]\n\t"
      "v_pk_fma_f32 %0, %2, %8, %0 op_sel:[0,0,0] op_sel_hi:[0,1,1]\n\t"
      "v_pk_fma_f32 %1, %3, %8, %1 op_sel:[0,0,0] op_sel_hi:[0,1,1]\n\t"
      "v_pk_fma_f32 %0, %2, %8, %0 op_sel:[1,1,0] op_sel_hi:[1,0,1] neg_lo:[0,1,0]\n\t"
      "v_pk_fma_f32 %1, %3, %8, %1 op_sel:[1,1,0] op_sel_hi:[1,0,1] neg_lo:[0,1,0]"
      : "=&v"(e0), "=&v"(e1), "=&v"(o0), "=&v"(o1) : "v"(A0), "v"(B0), "v"(A1), "v"(B1), "v"(w));
  d0 = e0; d1 = e1;
}

constexpr int SCR_F4 = 361;
struct Tw320 { c2 a[4]; c2 b[7]; };
DEV void tw320_init(Tw320& tw, int lane, c2 u0, float scale) {
  tw_powers<4>(tw.a, cmul(u0, u0));
  tw_powers<7>(tw.b, twiddle_rev((float)(lane & 7) * (1.0f / 64.0f)));
#pragma unroll
  for (int i = 0; i < 7; i++) tw.b[i] = tw.b[i] * scale;
}
#define FFT_HOOK_NONE(i)
template <int SGN, int NP, typename HOOK> DEV void fft320x2(c2 (*z)[5], const Tw320& tw, float scale, f4* scr, int lane, c2 (*o)[8], HOOK hook) {
  const int u = lane < 40 ? lane : 39;
  const int q = u >> 3, a = u & 7;
#pragma unroll
  for (int p = 0; p < NP; p++) {
    dft5<SGN>(z[2 * p]); dft5<SGN>(z[2 * p + 1]);
#pragma unroll
    for (int i = 1; i < 5; i++) twmul2<SGN>(z[2 * p][i], z[2 * p + 1][i], tw.a[i - 1]);
    lds_fence();
#pragma unroll
    for (int i = 0; i < 5; i++) scr[p * SCR_F4 + i * 72 + lane] = f4{z[2 * p][i].x, z[2 * p][i].y, z[2 * p + 1][i].x, z[2 * p + 1][i].y};
    lds_fence();
#pragma unroll
    for (int b = 0; b < 8; b++) { f4 t = scr[p * SCR_F4 + q * 72 + a + 8 * b]; o[2 * p][b] = t.xy; o[2 * p + 1][b] = t.zw; }
    SCHED_FENCE();
  }
  hook(0);
#pragma unroll
  for (int p = 0; p < NP; p++) {
    dft8<SGN>(o[2 * p]); dft8<SGN>(o[2 * p + 1]);
    hook(1);
    o[2 * p][0] = o[2 * p][0] * scale; o[2 * p + 1][0] = o[2 * p + 1][0] * scale;
#pragma unroll
    for (int c = 1; c < 8; c++) twmul2<SGN>(o[2 * p][c], o[2 * p + 1][c], tw.b[c - 1]);
    lds_fence();
#pragma unroll
    for (int c = 0; c < 8; c++) scr[p * SCR_F4 + (q * 8 + c) * 9 + a] = f4{o[2 * p][c].x, o[2 * p][c].y, o[2 * p + 1][c].x, o[2 * p + 1][c].y};
    lds_fence();
#pragma unroll
    for (int i = 0; i < 8; i++) { f4 t = scr[p * SCR_F4 + u * 9 + i]; o[2 * p][i] = t.xy; o[2 * p + 1][i] = t.zw; }
    SCHED_FENCE();
  }
  hook(2);
  dft8<SGN>(o[0]);
  hook(3);
  dft8<SGN>(o[1]);
  lds_fence();
}
constexpr float FWD_SC = 0.5f * 0.05590169943749474f;
template <int NP> DEV void rfft2_rows(const unsigned (*rw)[5], const Tw320& tw, f4* scr, int lane, c2 (*o)[8]) {
  c2 z[2 * NP][5];
#pragma unroll
  for (int i = 0; i < 2 * NP; i++)
#pragma unroll
    for (int j = 0; j < 5; j++) z[i][j] = c2{bf_lo(rw[i][j]), bf_hi(rw[i][j])};
  fft320x2<-1, NP>(z, tw, FWD_SC, scr, lane, o, [](int) {});
  const int u = lane < 40 ? lane : 39;
  f4* pp = scr + (u >> 3) + 5 * (u & 7);
#pragma unroll
  for (int p = 0; p < NP; p++) {
    if (p > 0) { dft8<-1>(o[2 * p]); dft8<-1>(o[2 * p + 1]); lds_fence(); }
#pragma unroll
    for (int e = 0; e < 8; e++) pp[p * SCR_F4 + 40 * e] = f4{o[2 * p][e].x, o[2 * p][e].y, o[2 * p + 1][e].x, o[2 * p + 1][e].y};
  }
}
DEV void untangle2(c2& x0, c2& x1, f4 A, f4 B, c2 w, bool k_is_0) {
  tangle2(x0, x1, A.xy, B.xy, A.zw, B.zw, w);
  if (k_is_0) {
    const float r = 1.41421356237309505f;
    x0 = c2{(A.x + A.y) * r, (A.x - A.y) * r};
    x1 = c2{(A.z + A.w) * r, (A.z - A.w) * r};
  }
}
DEV c2 mul_mi(c2 w) { return c2{w.y, -w.x}; }

template <int CTRL> DEV float dpp_add(float v) {
  return v + __builtin_bit_cast(float, __builtin_amdgcn_update_dpp(0, __builtin_bit_cast(int, v), CTRL, 0xf, 0xf, true));
}
DEV float wave_sum63(float v) {
  v = dpp_add<0x111>(v);
  v = dpp_add<0x112>(v);
  v = dpp_add<0x114>(v);
  v = dpp_add<0x118>(v);
  v = v + __builtin_bit_cast(float, __builtin_amdgcn_update_dpp(0, __builtin_bit_cast(int, v), 0x142, 0xa, 0xf, true));
  v = v + __builtin_bit_cast(float, __builtin_amdgcn_update_dpp(0, __builtin_bit_cast(int, v), 0x143, 0xc, 0xf, true));
  return v;
}

#define CODE_ADDR(A, SYM) asm("s_getpc_b64 vcc\n\ts_add_u32 vcc_lo, vcc_lo, " SYM "@rel32@lo+4\n\ts_addc_u32 vcc_hi, vcc_hi, " SYM "@rel32@hi+12\n\ts_mov_b64 %0, vcc" \
                              : "=s"(A) :: "vcc", "scc")
#define CODE_PF(P, COND, SYM, END) do { const char *b_, *e_; CODE_ADDR(b_, SYM); CODE_ADDR(e_, END); \
    long n_ = e_ - b_ - 4; n_ = n_ < 0 ? 0 : n_; long o_ = (long)threadIdx.x * 64; o_ = o_ < n_ ? o_ : n_ & ~3l; P = (COND) ? b_ + o_ : P; } while (0)
#define PF_LOAD(P) (*(const __attribute__((address_space(1))) unsigned*)(P))
#define SYM_K1 "_Z11k_colfilterILb1EEvPKtPc"
#define SYM_K4 "_Z11k_colfilterILb0EEvPKtPc"
#define SYM_K2 "_Z7k_gemm1ILb0EEvPc"
#define SYM_K5 "_Z7k_gemm1ILb1EEvPc"
#define SYM_K3 "_Z7k_gemm2ILb0EEvPcP14__hip_bfloat16"
#define SYM_K6 "_Z7k_gemm2ILb1EEvPcP14__hip_bfloat16"
#define CODE_PF_K1(P, C) CODE_PF(P, C, SYM_K1, SYM_K4)
#define CODE_PF_K4(P, C) CODE_PF(P, C, SYM_K4, SYM_K2)
#define CODE_PF_K2(P, C) CODE_PF(P, C, SYM_K2, SYM_K5)
#define CODE_PF_K5(P, C) CODE_PF(P, C, SYM_K5, SYM_K3)
#define CODE_PF_K3(P, C) CODE_PF(P, C, SYM_K3, SYM_K6)
#define CODE_PF_K6(P, C) CODE_PF(P, C, SYM_K6, "k6_code_end")

DEV int frag_f(int T, int g, int j) {
  int G = T / 3, r = T - 3 * G;
  int ta = r == 0 ? 0 : (r == 1 ? 3 : 2), tb = r == 0 ? 1 : (r == 1 ? 4 : 5);
  return 96 * G + 16 * (j < 4 ? ta : tb) + 4 * g + (j & 3);
}

constexpr int K0_X = 128, K0_W1 = 96, K0_WT = 128;
template <bool is_x>
DEV void prep_rows(const uint16_t* __restrict__ x, const uint16_t* __restrict__ w1, char* __restrict__ ws, f4* scr_all, u2* stage, int grp) {
  const int tid = threadIdx.x, lane = tid & 63, wv = tid >> 6;
  f4* scr = scr_all + wv * SCR_F4;
  unsigned rw2[2][5];
  unsigned *r0 = rw2[0], *r1 = rw2[1];
  u4 s0[2], s1[2];
  if (is_x) {
    const unsigned* row = (const unsigned*)(x + (size_t)(16 * grp + 2 * wv) * D_);
#pragma unroll
    for (int j = 0; j < 5; j++) { r0[j] = row[lane + 64 * j]; r1[j] = row[320 + lane + 64 * j]; }
    if (tid < 16) ((double*)(ws + OFF_SSQ))[16 * grp + tid] = 0.0;
  } else {
#pragma unroll
    for (int it = 0; it < 2; it++) {
      int n = it == 0 ? tid : 512 + (tid & 127);
      const uint16_t* p = w1 + (size_t)(n >> 1) * (2 * F_) + grp * 16 + (n & 1) * 8;
      s0[it] = *(const u4*)p; s1[it] = *(const u4*)(p + F_);
    }
  }
  SCHED_FENCE();
  Tw320 tw;
  tw320_init(tw, lane, twiddle_rev((float)lane * (1.0f / 640.0f)), FWD_SC);
  c2 wo[3][2];
  wo[0][0] = mul_mi(twiddle_rev((float)(tid >> 3) * (1.0f / 320.0f)));
  wo[1][0] = cmul(wo[0][0], c2{0.30901699437494742f, -0.95105651629515357f});
  wo[2][0] = cmul(wo[0][0], c2{-0.80901699437494742f, -0.58778525229247313f});
#pragma unroll
  for (int it = 0; it < 3; it++) wo[it][1] = cmul(wo[it][0], c2{0.99995180895932817f, -0.00981731933714962f});
  SCHED_FENCE();
  if (!is_x) {
    asm volatile("" : "+v"(s0[1]), "+v"(s1[1]));
#pragma unroll
    for (int it = 0; it < 2; it++) {
      int n = tid + 512 * it;
      if (it == 0 || tid < 128) {
#pragma unroll
        for (int i = 0; i < 4; i++)
          stage[((n & 1) * 4 + i) * 324 + (n >> 1)] = u2{__builtin_amdgcn_perm(s1[it][i], s0[it][i], 0x05040100u), __builtin_amdgcn_perm(s1[it][i], s0[it][i], 0x07060302u)};
      }
    }
    __syncthreads();
#pragma unroll
    for (int j = 0; j < 5; j++) { u2 t = stage[wv * 324 + lane + 64 * j]; r0[j] = t.x; r1[j] = t.y; }
  }
  TSTAMP(0, 1);
  c2 o[2][8];
  rfft2_rows<1>(rw2, tw, scr, lane, o);
  if (is_x) {
    float ss[2];
#pragma unroll
    for (int rr = 0; rr < 2; rr++) {
      c2 t = o[rr][0];
      if (lane == 0) t = c2{(t.x - t.y) * 0.70710678118654752f, 0.f};
      c2 acc = t * t;
#pragma unroll
      for (int e = 1; e < 8; e++) acc += o[rr][e] * o[rr][e];
      ss[rr] = wave_sum63(lane < 40 ? acc.x + acc.y : 0.f);
    }
    if (lane == 63)
      *(c2*)(ws + OFF_RS0 + (size_t)(16 * grp + 2 * wv) * 4) =
          c2{__builtin_amdgcn_rsqf(ss[0] * (1.0f / 160.0f) + LN_EPS), __builtin_amdgcn_rsqf(ss[1] * (1.0f / 160.0f) + LN_EPS)} * (1.0f / 2048.0f);
  }
  TSTAMP(0, 2);
  __syncthreads();
  const int rp = tid & 7;
  const f4* S = scr_all + rp * SCR_F4;
#pragma unroll
  for (int it = 0; it < 3; it++) {
    if (it == 2 && tid >= 256) break;
    const int q4 = (tid >> 3) + 64 * it;
    f4 A0 = S[2 * q4], A1 = S[2 * q4 + 1], B0 = S[(it == 0 && q4 == 0) ? 0 : 320 - 2 * q4], B1 = S[319 - 2 * q4];
    c2 x00, x10, x01, x11;
    untangle2(x00, x10, A0, B0, wo[it][0], it == 0 && q4 == 0);
    untangle2(x01, x11, A1, B1, wo[it][1], false);
    if (is_x) {
      u4 v = u4{pack_bf(x00.x, x00.y), pack_bf(x01.x, x01.y), pack_bf(x10.x, x10.y), pack_bf(x11.x, x11.y)};
      __builtin_amdgcn_raw_buffer_store_b128(v, make_rsrc(ws + OFF_XB + (size_t)(grp >> 2) * XB_MS, XB_MS), q4 * 512 + (((grp & 3) * 128 + rp * 16) ^ (((q4 >> 1) & 1) << 7)), 0, AUX_K0);
    } else {
      if (it == 0 && q4 == 0) { x00.x = 0.f; x10.x = 0.f; }
      const rsrc_t Wr = make_rsrc(ws + OFF_W1P + (size_t)grp * W1_TS * 1024, W1_TS * 1024);
      const bool od = q4 & 1;
      const u2 c0 = u2{pack_bf(x00.x, x00.y), pack_bf(x01.x, x01.y)}, c1 = u2{pack_bf(x10.x, x10.y), pack_bf(x11.x, x11.y)};
      const u2 snd = od ? c0 : c1, kp = od ? c1 : c0;
      const u2 rcv = u2{(unsigned)__builtin_amdgcn_update_dpp(0, (int)snd.x, 0x128, 0xf, 0xf, false), (unsigned)__builtin_amdgcn_update_dpp(0, (int)snd.y, 0x128, 0xf, 0xf, false)};
      const u4 v = od ? u4{rcv.x, rcv.y, kp.x, kp.y} : u4{kp.x, kp.y, rcv.x, rcv.y};
      __builtin_amdgcn_raw_buffer_store_b128(v, Wr, ((q4 >> 1) * 16 + 2 * rp + (q4 & 1)) * 16, 0, AUX_W1);
    }
  }
  TSTAMP(0, 7);
}

__global__ __launch_bounds__(512) void k_prep(const uint16_t* __restrict__ x, const uint16_t* __restrict__ wr,
                                              const uint16_t* __restrict__ wi, const uint16_t* __restrict__ w1,
                                              const uint16_t* __restrict__ w2, const char* outp, char* __restrict__ ws) {
  __shared__ __attribute__((aligned(16))) f4 scr_all[8 * SCR_F4];
  __shared__ __attribute__((aligned(16))) u2 stage[8 * 324];
  const int tid = threadIdx.x, lane = tid & 63, wv = tid >> 6;
  const int b = blockIdx.x;
  TSTAMP(0, 0);
  const char* ps = nullptr;
  CODE_PF(ps, true, "_Z6k_prepPKtS0_S0_S0_S0_PKcPc", SYM_K1);
  const unsigned spf = tid < 192 ? PF_LOAD(ps) : 0u;
  SCHED_FENCE();

  if (b >= K0_X + K0_W1) {
    int bb = b - (K0_X + K0_W1);
    int unit = (bb & 7) * 16 + (bb >> 3);
    int u = unit & 3, bbit = (unit >> 2) & 1, kc = unit >> 3;
    unsigned* t32 = (unsigned*)scr_all;
    unsigned short re[2][6], im[2][6];
#pragma unroll
    for (int hh = 0; hh < 2; hh++) {
      int ks = kc + 16 * (2 * u + hh + 8 * bbit) + 256 * wv;
      const uint16_t* pr = wr + (size_t)ks * DF_;
      const uint16_t* pi = wi + (size_t)ks * DF_;
#pragma unroll
      for (int i = 0; i < 6; i++) {
        int c = lane + 64 * i;
        c = c < DF_ ? c : DF_ - 1;
        re[hh][i] = pr[c]; im[hh][i] = pi[c];
      }
    }
    SCHED_FENCE();
    const int sl = bb >> 3;
    const char* pa = ws + (size_t)(sl == 8 && tid * 65536u < OFF_RS0 ? tid * 65536u : 0u);
    if (sl == 9) pa = outp + (tid < 40 ? tid : 0) * 65536u;
    if (sl == 10) pa = (const char*)w2 + (tid < 30 ? tid : 0) * 65536u;
    CODE_PF_K1(pa, sl == 0); CODE_PF_K2(pa, sl == 1); CODE_PF_K3(pa, sl == 2); CODE_PF_K4(pa, sl == 3); CODE_PF_K5(pa, sl == 4); CODE_PF_K6(pa, sl == 5);
    CODE_PF(pa, sl == 6, SYM_K1 ".kd", SYM_K6 ".kd");
    { const char* a_; CODE_ADDR(a_, SYM_K6 ".kd"); pa = sl == 7 ? a_ : pa; }
    const int wi_ = (sl < 12 ? sl : 11) * 8 + (bb & 7), t3 = tid < 320 ? tid : 319, rr = t3 / 20;
    const unsigned cpf = PF_LOAD(pa);
    const unsigned wpf = *(const unsigned*)(w2 + (size_t)frag_f(wi_ >> 1, (wi_ & 1) * 2 + (rr >> 3), rr & 7) * D_ + (t3 % 20) * 32);
    SCHED_FENCE();
#pragma unroll
    for (int hh = 0; hh < 2; hh++)
#pragma unroll
      for (int i = 0; i < 6; i++) {
        int c = lane + 64 * i;
        if (c < DF_) t32[(hh * 8 + wv) * 324 + c] = (unsigned)re[hh][i] | ((unsigned)im[hh][i] << 16);
      }
    __syncthreads();
    const rsrc_t WT = make_rsrc(ws + OFF_WT, 160 * 4 * 256 * 16);
    for (int n = tid; n < 1280; n += 512) {
      int col = n & 1, hh = (n >> 1) & 1, ih = (n >> 2) & 1, q4 = n >> 3;
      int cc = 2 * q4 + col;
      if (cc == 0) cc = 320;
      u4 v;
#pragma unroll
      for (int e = 0; e < 4; e++) v[e] = t32[(hh * 8 + ih * 4 + e) * 324 + cc];
      int tau = (2 * u + hh) + 8 * kc;
      __builtin_amdgcn_raw_buffer_store_b128(v, WT, ((q4 * 4 + 2 * bbit + ih) * 256 + 2 * tau + col) * 16, 0, AUX_K0);
    }
    asm volatile("" :: "v"(cpf), "v"(spf), "v"(wpf));
    TSTAMP(0, 7);
    return;
  }

  if (b < K0_X) prep_rows<true>(x, w1, ws, scr_all, stage, (b & 7) * 16 + (b >> 3));
  else prep_rows<false>(x, w1, ws, scr_all, stage, ((b - K0_X) & 7) * 12 + ((b - K0_X) >> 3));
  asm volatile("" :: "v"(spf));
}

constexpr int P1 = 152;
constexpr int LDS_CF = 84 * 1024;
template <bool FIRST>
__global__ __launch_bounds__(256, 1) void k_colfilter(const uint16_t* __restrict__ w2, char* __restrict__ ws) {
  extern __shared__ __attribute__((aligned(16))) char smem[];
  const int tid = threadIdx.x;
  const int xcd = blockIdx.x & 7, slot = blockIdx.x >> 3;
  constexpr int KID = FIRST ? 1 : 4;
  (void)KID;
  if (slot >= 20) {
    TSTAMP(KID, 0);
    const int lane = tid & 63, wv = tid >> 6;
    const int i = (slot - 20) * 8 + xcd;
    const int T = i >> 1, g0 = (i & 1) * 2;
    unsigned pf[6], cpf = 0;
    const char* pfbase = ws + OFF_W1P + (size_t)(xcd & 1) * 48 * W1_TS * 1024 + tid * 64;
#define L2_PREFETCH() do { SCHED_FENCE(); _Pragma("unroll") for (int j = 0; j < 6; j++) { int blk = (slot - 20) + 12 * j; blk = blk < 63 ? blk : 62; \
      pf[j] = *(const unsigned*)(pfbase + blk * 16384); } \
      const char* pc_ = nullptr; \
      if (FIRST) { CODE_PF_K3(pc_, true); CODE_PF_K4(pc_, slot & 1); } else { CODE_PF_K5(pc_, true); CODE_PF_K6(pc_, slot & 1); } \
      cpf = PF_LOAD(pc_); \
      SCHED_FENCE(); } while (0)
    if (FIRST) {
      f4* scr_all = (f4*)smem;
      unsigned raw[4][5];
#pragma unroll
      for (int rr = 0; rr < 4; rr++) {
        int r = wv * 4 + rr;
        const unsigned* row = (const unsigned*)(w2 + (size_t)frag_f(T, g0 + (r >> 3), r & 7) * D_);
#pragma unroll
        for (int j = 0; j < 5; j++) raw[rr][j] = row[lane + 64 * j];
      }
      L2_PREFETCH();
      Tw320 tw;
      tw320_init(tw, lane, twiddle_rev((float)lane * (1.0f / 640.0f)), FWD_SC);
      const int k0 = (tid & 7) + 8 * (tid >> 4), gg = (tid >> 3) & 1;
      c2 wo[3];
      wo[0] = mul_mi(twiddle_rev((float)k0 * (1.0f / 640.0f)));
      wo[1] = cmul(wo[0], c2{0.30901699437494742f, -0.95105651629515357f});
      wo[2] = cmul(wo[0], c2{-0.80901699437494742f, -0.58778525229247313f});
      SCHED_FENCE();
      c2 o[4][8];
      rfft2_rows<2>(raw, tw, scr_all + (wv * 2) * SCR_F4, lane, o);
      __syncthreads();
      const f4* S = scr_all + gg * 4 * SCR_F4;
#pragma unroll
      for (int it = 0; it < 3; it++) {
        if (it == 2 && tid >= 128) break;
        const int k = k0 + 128 * it;
        const bool z = it == 0 && k == 0;
        f4 A[4], B[4];
#pragma unroll
        for (int jp = 0; jp < 4; jp++) { A[jp] = S[jp * SCR_F4 + k]; B[jp] = S[jp * SCR_F4 + (z ? 0 : 320 - k)]; }
        u4 re, im;
#pragma unroll
        for (int jp = 0; jp < 4; jp++) {
          c2 x0, x1;
          untangle2(x0, x1, A[jp], B[jp], wo[it], z);
          re[jp] = pack_bf(x0.x, x1.x); im[jp] = pack_bf(x0.y, x1.y);
        }
        const rsrc_t Wr = make_rsrc(ws + OFF_W2P, 40 * H_TS * 1024);
        const unsigned vo = (((k >> 3) * H_TS + T) * 64 + (g0 + gg) * 16 + (k & 7)) * 16;
        __builtin_amdgcn_raw_buffer_store_b128(re, Wr, vo, 0, AUX_W1);
        __builtin_amdgcn_raw_buffer_store_b128(im, Wr, vo + 128, 0, AUX_W1);
      }
    } else {
      const rsrc_t W2C = make_rsrc(ws + OFF_W2C, 40 * H_TS * 1024);
      unsigned short v[5][8];
#pragma unroll
      for (int it = 0; it < 5; it++) {
        int n = tid + 256 * it;
        int cl = n & 15, gg = (n >> 4) & 1, ct = n >> 5;
#pragma unroll
        for (int j = 0; j < 8; j++) v[it][j] = w2[(size_t)frag_f(T, g0 + gg, j) * D_ + ct * 16 + cl];
      }
      L2_PREFETCH();
#pragma unroll
      for (int it = 0; it < 5; it++) {
        int n = tid + 256 * it;
        int cl = n & 15, gg = (n >> 4) & 1, ct = n >> 5;
        u4 o;
#pragma unroll
        for (int j = 0; j < 4; j++) o[j] = (unsigned)v[it][2 * j] | ((unsigned)v[it][2 * j + 1] << 16);
        __builtin_amdgcn_raw_buffer_store_b128(o, W2C, ((ct * H_TS + T) * 64 + (g0 + gg) * 16 + cl) * 16, 0, AUX_K0);
      }
    }
    asm volatile("" :: "v"(pf[0]), "v"(pf[1]), "v"(pf[2]), "v"(pf[3]), "v"(pf[4]), "v"(pf[5]), "v"(cpf));
    TSTAMP(KID, 7);
    return;
  }
  typedef __attribute__((address_space(3))) volatile c2* ldsp;
  const int col = tid & 1, tau = tid >> 1;
  const int q4 = xcd * 20 + slot;
  const int lo = tau & 7, kc = tau >> 3;
  PT_DECL; PT(0);
  const rsrc_t XBr = make_rsrc(ws + OFF_XB + q4 * 512, 32 * XB_MS);
  const unsigned xoff = (tau >> 6) * XB_MS + ((((tau & 63) << 1) + col) ^ (((q4 >> 1) & 1) << 5)) * 4;
  unsigned xr[16];
  c2 v[16];
  float rs[16];
  u2 sq[16];
  u4 wt[4];
  {
    const rsrc_t Sr = make_rsrc(ws + OFF_RS0, 2048 * 12);
#pragma unroll
    for (int rq = 0; rq < 16; rq++) {
      const int r = (rq >> 2) + 4 * (rq & 3);
      if ((rq & 3) == 0) SCHED_FENCE();
      xr[r] = __builtin_amdgcn_raw_buffer_load_b32(XBr, xoff, r * (2 * XB_MS), 0);
      if (FIRST) rs[r] = __builtin_bit_cast(float, __builtin_amdgcn_raw_buffer_load_b32(Sr, tau * 4 + (r & 7) * 512, (r >> 3) * 4096, 0));
      else sq[r] = __builtin_amdgcn_raw_buffer_load_b64(Sr, tau * 8 + (r & 3) * 1024, 8192 + (r >> 2) * 4096, 0);
    }
    SCHED_FENCE();
    const rsrc_t Wr = make_rsrc(ws + OFF_WT + (size_t)q4 * 16384, 16384);
#pragma unroll
    for (int i = 0; i < 4; i++) wt[i] = __builtin_amdgcn_raw_buffer_load_b128(Wr, tid * 16, i * 4096, 0);
  }
  SCHED_FENCE();
  c2 tw1[15], tw2[15];
  tw_powers<15>(tw1, twiddle_rev((float)tau * (1.0f / 2048.0f)));
  tw_powers<15>(tw2, twiddle_rev((float)lo * (1.0f / 128.0f)));
  const bool special = (q4 == 0) && (col == 0);
  const unsigned sel_lo = special ? 0x07060c0cu : 0x05040c0cu, sel_hi = special ? 0x0c0c0c0cu : 0x07060c0cu;
  ldsp e1 = (ldsp)(uintptr_t)(unsigned)(uintptr_t)(__attribute__((address_space(3))) char*)smem + tid;
  ldsp e2 = (ldsp)(uintptr_t)(unsigned)(uintptr_t)(__attribute__((address_space(3))) char*)smem + (kc * P1 + lo) * 2 + col;
  ldsp e3 = (ldsp)(uintptr_t)(unsigned)(uintptr_t)(__attribute__((address_space(3))) char*)smem + (kc * P1 + lo * 9) * 2 + col;
  SCHED_FENCE();
  c2 xv[16];
  dft16s<-1>(v,
    [&](int i) {
#pragma unroll
      for (int j = 0; j < 4; j++) {
        const int r = i + 4 * j;
        if (!FIRST) {
          u2 t = sq[r];
          rs[r] = __builtin_amdgcn_rsqf((float)__builtin_bit_cast(double, t) * (2048.0f * 2048.0f / 640.0f) + LN_EPS * 2048.0f * 2048.0f);
        }
        xv[r] = c2{__builtin_bit_cast(float, __builtin_amdgcn_perm(xr[r], 0u, sel_lo)), __builtin_bit_cast(float, __builtin_amdgcn_perm(xr[r], 0u, sel_hi))};
        v[r] = xv[r] * rs[r];
      }
      if (i == 0) PT(1);
    },
    [&](int q) {
      if (q == 0) v[4] = cmul(v[4], tw1[3]); else twmulp<-1>(v[q], tw1[q - 1], v[q + 4], tw1[q + 3]);
      e1[q * (2 * P1)] = v[q]; e1[(q + 4) * (2 * P1)] = v[q + 4];
      twmulp<-1>(v[q + 8], tw1[q + 7], v[q + 12], tw1[q + 11]);
      e1[(q + 8) * (2 * P1)] = v[q + 8]; e1[(q + 12) * (2 * P1)] = v[q + 12];
    });
  __syncthreads();
#pragma unroll
  for (int i = 0; i < 4; i++)
#pragma unroll
    for (int j = 0; j < 4; j++) v[i + 4 * j] = e2[(i + 4 * j) * 16];
  SCHED_FENCE();
  c2 wf[16];
#pragma unroll
  for (int r = 0; r < 16; r++) { unsigned w = wt[r >> 2][r & 3]; wf[r] = c2{bf_lo(w), bf_hi(w)}; }
  SCHED_FENCE();
  dft16s<-1>(v, [&](int) {},
    [&](int q) {
      if (q == 0) v[4] = cmul(v[4], tw2[3]); else twmulp<-1>(v[q], tw2[q - 1], v[q + 4], tw2[q + 3]);
      e2[q * 18] = v[q]; e2[(q + 4) * 18] = v[q + 4];
      twmulp<-1>(v[q + 8], tw2[q + 7], v[q + 12], tw2[q + 11]);
      e2[(q + 8) * 18] = v[q + 8]; e2[(q + 12) * 18] = v[q + 12];
    });
#pragma unroll
  for (int bb = 0; bb < 2; bb++)
#pragma unroll
    for (int sa = 0; sa < 8; sa++) v[bb * 8 + sa] = e3[(bb * 72 + sa) * 2];
  SCHED_FENCE();
  PT(2);
#pragma unroll
  for (int bb = 0; bb < 2; bb++) {
    dft8<-1>(v + 8 * bb);
#pragma unroll
    for (int r = 0; r < 8; r += 2) twmulp<-1>(v[8 * bb + r], wf[8 * bb + r], v[8 * bb + r + 1], wf[8 * bb + r + 1]);
    SCHED_FENCE();
  }
  dft8<1>(v);
  SCHED_FENCE();
  {
    c2* u = v + 8;
    c2 a0 = u[0], a1 = u[2], a2 = u[4], a3 = u[6], o0 = u[1], o1 = u[3], o2 = u[5], o3 = u[7];
    e3[0] = v[0]; e3[2] = v[1];
    dft4<1>(a0, a1, a2, a3);
    SCHED_FENCE();
    e3[4] = v[2]; e3[6] = v[3];
    dft4<1>(o0, o1, o2, o3);
    SCHED_FENCE();
    e3[8] = v[4]; e3[10] = v[5];
    o1 = rot1<1>(o1); o3 = rot3<1>(o3);
    u[0] = a0 + o0; u[4] = a0 - o0;
    u[2] = add_rot<1>(a2, o2); u[6] = sub_rot<1>(a2, o2);
    SCHED_FENCE();
    e3[12] = v[6]; e3[14] = v[7];
    u[1] = a1 + o1; u[5] = a1 - o1;
    u[3] = a3 + o3; u[7] = a3 - o3;
    SCHED_FENCE();
#pragma unroll
    for (int sa = 0; sa < 8; sa++) e3[(72 + sa) * 2] = u[sa];
  }
#pragma unroll
  for (int i = 0; i < 4; i++)
#pragma unroll
    for (int j = 0; j < 4; j++) v[i + 4 * j] = e2[(i + 4 * j) * 18];
  SCHED_FENCE();
  dft16s<1>(v,
    [&](int i) {
      if (i == 0) v[4] = cmulc(v[4], tw2[3]); else twmulp<1>(v[i], tw2[i - 1], v[i + 4], tw2[i + 3]);
      twmulp<1>(v[i + 8], tw2[i + 7], v[i + 12], tw2[i + 11]);
    },
    [&](int q) {
#pragma unroll
      for (int j = 0; j < 4; j++) e2[(q + 4 * j) * 16] = v[q + 4 * j];
    });
  __syncthreads();
#pragma unroll
  for (int i = 0; i < 4; i++)
#pragma unroll
    for (int j = 0; j < 4; j++) v[i + 4 * j] = e1[(i + 4 * j) * (2 * P1)];
  SCHED_FENCE();
  const unsigned sel_o = special ? 0x05040100u : 0x07060504u;
  PT(3);
  dft16s<1>(v,
    [&](int i) {
      if (i == 0) v[4] = cmulc(v[4], tw1[3]); else twmulp<1>(v[i], tw1[i - 1], v[i + 4], tw1[i + 3]);
      twmulp<1>(v[i + 8], tw1[i + 7], v[i + 12], tw1[i + 11]);
    },
    [&](int q) {
#pragma unroll
      for (int j = 0; j < 4; j++) {
        const int r = q + 4 * j;
        c2 o = xv[r] + v[r];
        __builtin_amdgcn_raw_buffer_store_b32(__builtin_amdgcn_perm(pack_bf(o.x, o.y), xr[r], sel_o), XBr, xoff, r * (2 * XB_MS), AUX_CF);
      }
    });
  PT(7); PT_FLUSH(KID);
}

constexpr int G1_A_BYTES = 81920;
constexpr int G1_WSCR = SCR_F4 * 16;
#define G1_PF 4
#define G1_PFI 5
#define G1_VOL volatile
#define G1_LGKM 0xc87f
#define G1_LS 4
#define G1_NP1 42
#define G2_PF 3
#define G2_ND 12
#define G2_WS 2
#define G2_BD 4
template <int I> struct ic { static constexpr int value = I; };
template <int N, int I = 0, typename F> DEV void static_for(F f) { if constexpr (I < N) { f(ic<I>{}); static_for<N, I + 1>(f); } }
constexpr int LDS_G1 = G1_A_BYTES + 512 + 4096;
constexpr int LDS_G1I = LDS_G1 + 4 * G1_WSCR;
DEV float dot2(unsigned a, float c) {
  bf2 q = __builtin_bit_cast(bf2, a);
  return __builtin_amdgcn_fdot2_f32_bf16(q, q, c, false);
}
template <bool WITH_INV>
__global__ __launch_bounds__(256, 1) void k_gemm1(char* __restrict__ ws) {
  extern __shared__ __attribute__((aligned(16))) char smem[];
  const int tid = threadIdx.x, lane = tid & 63;
  const int wv = __builtin_amdgcn_readfirstlane(tid >> 6);
  const int b = blockIdx.x;
  const int xcd = b & 7, slot = b >> 3;
  const int mblk = (xcd >> 1) * 8 + (slot & 7), fs = (xcd & 1) * 4 + (slot >> 3);
  constexpr int KID = WITH_INV ? 5 : 2;
  (void)KID;
  asm volatile("" ::: "a191");
  PT_DECL; PT(0);
  const char* Ag = ws + OFF_XB + (size_t)mblk * XB_MS;
  const unsigned lds0 = (unsigned)(uintptr_t)(__attribute__((address_space(3))) char*)smem + wv * 20480;
  const unsigned l16 = lane * 16;
#define G1_DMA(I) do { unsigned keep_; asm volatile( \
          "s_mov_b32 %0, m0\n\ts_mov_b32 m0, %3\n\ts_nop 0\n\t" \
          "global_load_lds_dwordx4 %1, %2\n\t" \
          "global_load_lds_dwordx4 %1, %2 offset:1024\n\t" \
          "global_load_lds_dwordx4 %1, %2 offset:2048\n\t" \
          "global_load_lds_dwordx4 %1, %2 offset:3072\n\t" \
          "s_mov_b32 m0, %0" \
          : "=&s"(keep_) : "v"(l16), "s"(Ag + wv * 20480 + (I) * 4096), "s"(lds0 + (I) * 4096) : "memory"); SCHED_FENCE(); } while (0)
  const rsrc_t Wg = make_rsrc(ws + OFF_W1P + (size_t)(fs * 12 + wv * 3) * W1_TS * 1024, 3 * W1_TS * 1024);
  constexpr int PF = WITH_INV ? G1_PFI : G1_PF;
  constexpr int NL = 3 * PF;
  bf8 wf[60];
#define G1_LOAD1(I) do { if ((I) < 60) wf[(I) < 60 ? (I) : 0] = bload(Wg, l16, ((I) / 20 * W1_TS + (I) % 20) * 1024); } while (0)
#define G1_LOADW(S) do { if ((S) < PF) { G1_LOAD1(3 * (S)); G1_LOAD1(3 * (S) + 1); G1_LOAD1(3 * (S) + 2); SCHED_FENCE(); } } while (0)
  unsigned pk3[2][8];
  const int m0 = fs * 8 + 2 * wv;
  const int iu = lane < 40 ? lane : 39;
  const int iq = iu >> 3, ia = iu & 7;
  if (!WITH_INV) {
    G1_DMA(0); G1_DMA(1); G1_DMA(2); G1_DMA(3); G1_DMA(4);
#pragma unroll
    for (int s = 0; s < PF; s++) G1_LOADW(s);
    PT(6);
  } else {
    f4* scr = (f4*)(smem + LDS_G1 + wv * G1_WSCR);
    u2* buf = (u2*)scr;
    u4 rw[3];
#pragma unroll
    for (int i = 0; i < 3; i++) {
      int q = lane + 64 * i;
      if (i == 2) q = q < 160 ? q : 159;
      unsigned vo = q * 512 + ((m0 * 8) ^ (((q >> 1) & 1) << 7));
      asm volatile("global_load_dwordx4 %0, %1, %2" : "=v"(rw[i]) : "v"(vo), "s"(Ag) : "memory");
    }
    SCHED_FENCE();
    G1_DMA(0); G1_DMA(1);
    const float sc = 0.5f * 17.88854381999832f / 320.0f;
    const float r2 = 1.41421356237309505f;
    Tw320 tw;
    c2 wu[5];
    {
      c2 u0 = twiddle_rev((float)lane * (1.0f / 640.0f));
      tw320_init(tw, lane, u0, sc);
      wu[0] = c2{u0.y, u0.x};
      wu[1] = cmul(wu[0], c2{0.80901699437494742f, 0.58778525229247313f});
      wu[2] = cmul(wu[0], c2{0.30901699437494742f, 0.95105651629515357f});
      wu[3] = cmul(wu[0], c2{-0.30901699437494742f, 0.95105651629515357f});
      wu[4] = cmul(wu[0], c2{-0.80901699437494742f, 0.58778525229247313f});
    }
    SCHED_FENCE();
    G1_DMA(2); G1_DMA(3); G1_DMA(4);
    PT(4);
    asm volatile("s_waitcnt vmcnt(20)" : "+v"(rw[0]), "+v"(rw[1]), "+v"(rw[2]) :: "memory");
    PT(5);
#pragma unroll
    for (int i = 0; i < 3; i++) {
      int q = lane + 64 * i;
      if (i == 2) q = q < 160 ? q : 159;
      *(u4*)&buf[2 * q] = u4{rw[i].x, rw[i].z, rw[i].y, rw[i].w};
    }
    lds_fence();
    c2 z[2][5], o[2][8];
    u2 ua[5], ub[5];
#pragma unroll
    for (int j = 0; j < 5; j++) {
      int k = lane + 64 * j;
      ua[j] = buf[k]; ub[j] = buf[(j == 0 && k == 0) ? 0 : 320 - k];
    }
    SCHED_FENCE();
    G1_LOADW(0); G1_LOADW(1);
#pragma unroll
    for (int j = 0; j < 5; j++) {
      c2 A0 = c2{bf_lo(ua[j].x), bf_hi(ua[j].x)}, B0 = c2{bf_lo(ub[j].x), bf_hi(ub[j].x)};
      c2 A1 = c2{bf_lo(ua[j].y), bf_hi(ua[j].y)}, B1 = c2{bf_lo(ub[j].y), bf_hi(ub[j].y)};
      if (j == 0 && lane == 0) {
        B0 = c2{A0.y * r2, 0.f}; A0 = c2{A0.x * r2, 0.f};
        B1 = c2{A1.y * r2, 0.f}; A1 = c2{A1.x * r2, 0.f};
      }
      tangle2(z[0][j], z[1][j], A0, B0, A1, B1, wu[j]);
    }
    fft320x2<1, 1>(z, tw, sc, scr, lane, o, [&](int h) {
      if (h == 0) { G1_LOADW(2); G1_LOADW(3); }
      if (h == 2) { G1_LOADW(4); G1_LOADW(5); }
    });
    PT(6);
#pragma unroll
    for (int rr = 0; rr < 2; rr++)
#pragma unroll
      for (int e = 0; e < 8; e++) pk3[rr][e] = pack_bf(o[rr][e].x, o[rr][e].y);
    SCHED_FENCE();
#pragma unroll
    for (int s = 6; s < PF; s++) G1_LOADW(s);
  }
  const rsrc_t Hr = make_rsrc(ws + OFF_H + (size_t)mblk * 4 * H_TS * 1024, 4 * H_TS * 1024);
  const int G = fs * 2 + (wv >> 1);
  int hfull[4], hhalf[4];
#pragma unroll
  for (int m = 0; m < 4; m++) {
    hfull[m] = ((m ^ wv) * H_TS + 3 * G + (wv & 1)) * 1024; hhalf[m] = ((m ^ wv) * H_TS + 3 * G + 2) * 1024;
    asm("" : "+s"(hfull[m]), "+s"(hhalf[m]));
  }
  SCHED_FENCE();
  asm volatile("s_waitcnt vmcnt(%0)\n\ts_barrier" :: "n"(3 * PF) : "memory");
  if (WITH_INV) {
    const rsrc_t Xr = make_rsrc(ws + OFF_X3 + (size_t)(mblk * 64 + m0) * 1280, 2560);
    const unsigned vo = (iq + 5 * ia) * 4;
#pragma unroll
    for (int rr = 0; rr < 2; rr++)
#pragma unroll
      for (int e = 0; e < 8; e++) __builtin_amdgcn_raw_buffer_store_b32(pk3[rr][e], Xr, vo + (rr * 320 + 40 * e) * 4, 0, AUX_X3);
    SCHED_FENCE();
  }
  PT(1);
  const int g = lane >> 4, r = lane & 15;
  const int swz = (g & 1) << 7;
  typedef __attribute__((address_space(3))) const char* ldsc;
  typedef __attribute__((address_space(3))) G1_VOL const u2* ldsu2;
  ldsc abase[4], abase2[4];
#pragma unroll
  for (int m = 0; m < 4; m++) {
    unsigned o = (2 * g) * 512 + (((16 * (m ^ wv) + r) * 8) ^ swz), o2 = o + 65536;
    asm("" : "+v"(o2));
    abase[m] = (ldsc)smem + o; abase2[m] = (ldsc)smem + o2;
  }
  SCHED_FENCE();
  f4 acc[3][4];
#pragma unroll
  for (int t = 0; t < 3; t++)
#pragma unroll
    for (int m = 0; m < 4; m++) acc[t][m] = f4{0.f, 0.f, 0.f, 0.f};
  u4 af[20][4];
#define G1_READA(KS, M) do { ldsc ap_ = (KS) < 16 ? abase[M] + (KS) * 4096 : abase2[M] + ((KS) - 16) * 4096; \
    u2 lo_ = *(ldsu2)ap_, hi_ = *(ldsu2)(ap_ + 512); af[KS][M] = u4{lo_.x, lo_.y, hi_.x, hi_.y}; } while (0)
#define G1_MFMA(ACC, W, B) ACC = __builtin_amdgcn_mfma_f32_16x16x32_bf16(W, __builtin_bit_cast(bf8, B), ACC, 0, 0, 0)
  G1_READA(0, 0); G1_READA(1, 0); G1_READA(0, 1); G1_READA(0, 2); G1_READA(0, 3);
  G1_READA(2, 0); G1_READA(3, 0); G1_READA(1, 1); G1_READA(1, 2); G1_READA(1, 3);
  typedef __attribute__((address_space(3))) volatile float* ldsf;
  ldsf stw = (ldsf)((ldsc)smem + G1_A_BYTES) + (g == (r >> 2) ? wv * 16 + r : 64 + (lane & 31));
  ldsf str = (ldsf)((ldsc)smem + G1_A_BYTES) + r;
  float rl[4], irs[4], e0, e1;
  unsigned pk[3][4][2];
  SCHED_FENCE();
  f4 gram = f4{0.f, 0.f, 0.f, 0.f};
  constexpr int NP1 = G1_NP1;
  static_for<20>([&](auto kI) {
    constexpr int ks = kI.value;
    constexpr int l0 = NL + (NP1 - NL) * ks / 20, l1 = NL + (NP1 - NL) * (ks + 1) / 20;
    constexpr int infl = (ks == 0 ? 10 : ks <= 8 ? 10 : ks == 12 ? 7 : ks == 14 ? 10 : ks < 19 ? 6 : 0);
    __builtin_amdgcn_s_waitcnt(0xc07f | ((infl & 15) << 8));
    SCHED_FENCE();
    if (ks < 10) {
      u4 ga = af[2 * ks][0];
      if (ks == 0 && g == 0) ga.x &= 0xffff0000u;
      G1_MFMA(gram, __builtin_bit_cast(bf8, ga), af[2 * ks][0]);
      if (ks < 8) G1_READA(ks < 8 ? 2 * ks + 4 : 0, 0);
      SCHED_FENCE();
    }
    static_for<4>([&](auto mI) {
      constexpr int m = mI.value;
      G1_MFMA(acc[0][m], wf[ks], af[ks][m]);
      if (m == 0 && ks < 8) G1_READA(ks < 8 ? 2 * ks + 5 : 0, 0);
      if (m > 0 && ks + 2 < 20) G1_READA(ks + 2 < 20 ? ks + 2 : 0, m);
      if (m == 0 && l0 < l1) G1_LOAD1(l0);
      if (m == 2 && l0 + 1 < l1) G1_LOAD1(l0 + 1);
      if (m == 0 && ks == 11) {
        float sv = (r & 2) ? ((r & 1) ? gram.w : gram.z) : ((r & 1) ? gram.y : gram.x);
        *stw = sv * (1.0f / 640.0f) + LN_EPS;
      }
      if (m == 0 && ks == 13) {
        asm volatile("s_barrier" ::: "memory");
#pragma unroll
        for (int q = 0; q < 4; q++) irs[q] = str[(q ^ wv) * 16];
      }
      if (m == 0 && ks >= 15 && ks < 19) {
        constexpr int q = (ks - 15) & 3;
        float rs = __builtin_amdgcn_rsqf(irs[q]);
        asm("s_nop 0\n\tv_mul_f32 %0, %0, %2\n\tv_mul_f32 %1, 0xbfb8aa3b, %2" : "+v"(irs[q]), "=v"(rl[q]) : "v"(rs));
      }
      SCHED_FENCE();
      if (m == 1 && ks < 10) {
        G1_MFMA(gram, __builtin_bit_cast(bf8, af[2 * ks + 1][0]), af[2 * ks + 1][0]);
        SCHED_FENCE();
      }
    });
  });
  PT(2);
  typedef __attribute__((address_space(3))) volatile u2* ldsx;
  ldsx xw = (ldsx)((ldsc)smem + G1_A_BYTES + 512) + wv * 128 + lane, xr = (ldsx)((ldsc)smem + G1_A_BYTES + 512) + (wv ^ 1) * 128 + lane;
  float a0, a1;
  auto silu_op = [&](auto cI, auto oI) {
    constexpr int c = cI.value, o = oI.value, t = c >> 3, m = (c >> 1) & 3, e = 2 * (c & 1);
    if (o == 0) { a0 = acc[t][m][e]; a1 = acc[t][m][e + 1]; asm("" : "+v"(a0), "+v"(a1)); }
    if (o == 1) asm("v_mul_f32 %0, %2, %4\n\tv_mul_f32 %1, %3, %4" : "=&v"(e0), "=&v"(e1) : "v"(a0), "v"(a1), "v"(rl[m]));
    if (o == 2) asm("v_exp_f32 %0, %0" : "+v"(e0));
    if (o == 3) asm("v_exp_f32 %0, %0" : "+v"(e1));
    if (o == 4) asm("v_fma_f32 %0, %0, %2, %2\n\tv_fma_f32 %1, %1, %2, %2" : "+v"(e0), "+v"(e1) : "v"(irs[m]));
    if (o == 5) asm("v_fma_f32 %0, %0, %2, %2\n\tv_fma_f32 %1, %1, %2, %2\n\tv_rcp_f32 %0, %0" : "+v"(e0), "+v"(e1) : "v"(irs[m]));
    if (o == 6) asm("v_rcp_f32 %0, %0" : "+v"(e0));
    if (o == 7) asm("v_rcp_f32 %0, %0" : "+v"(e1));
    if (o == 8) asm("v_rcp_f32 %1, %1\n\tv_mul_f32 %0, %0, %2" : "+v"(e0), "+v"(e1) : "v"(a0));
    if (o == 9) asm("v_mul_f32 %0, %0, %2\n\tv_mul_f32 %1, %1, %3" : "+v"(e0), "+v"(e1) : "v"(a0), "v"(a1));
    if (o == 10) asm("v_cvt_pk_bf16_f32 %0, %1, %2" : "=v"(pk[t][m][c & 1]) : "v"(e0), "v"(e1));
    if (o == 11) asm("v_mul_f32 %2, %2, %3\n\tv_cvt_pk_bf16_f32 %0, %1, %2" : "=v"(pk[t][m][c & 1]), "+v"(e0), "+v"(e1) : "v"(a1));
    if (o == 10 || o == 11) {
      if (t == 1 && (c & 1))
        __builtin_amdgcn_raw_buffer_store_b128(u4{pk[0][m][0], pk[0][m][1], pk[1][m][0], pk[1][m][1]}, Hr, l16, hfull[m], ST_AUX);
      if (t == 2 && (c & 1) && m == 1) xw[0] = u2{pk[2][1][0], pk[2][1][1]};
    }
    SCHED_FENCE();
  };
#define SOP(C, O) silu_op(ic<(C)>{}, ic<(O)>{})
  static_for<160>([&](auto jI) {
    constexpr int j = jI.value;
    constexpr int t = j < 80 ? 1 : 2;
    constexpr int ks = j < 40 ? j >> 2 : j < 80 ? 10 + ((j - 40) % 20) / 2 : ((j - 80) % 40) >> 1;
    constexpr int m = j < 40 ? j & 3 : j < 80 ? ((j - 40) / 20) * 2 + (j & 1) : ((j - 80) / 40) * 2 + (j & 1);
    G1_MFMA(acc[t][m], wf[20 * t + ks], af[ks][m]);
    SCHED_FENCE();
    constexpr int c = j >> 3, o = j & 7;
    if (j == 0) SOP(0, 0);
    if (o == 0) SOP(c, 1);
    if (o == 1) SOP(c, 2);
    if (o == 2) SOP(c, 3);
    if (o == 3) SOP(c, 4);
    if (o == 4) SOP(c, 6);
    if (o == 5) SOP(c, 7);
    if (o == 6) SOP(c, 9);
    if (o == 7) { SOP(c, 10); if (c < 19) SOP(c + 1, 0); }
    if (j % G1_LS == 3) G1_LOAD1(NP1 + j / G1_LS);
    if (j == 80) PT(3);
    SCHED_FENCE();
  });
  PT(4 + (WITH_INV ? 3 : 0));
  {
#pragma unroll
    for (int m = 3; m >= 2; m--) {
      float x[4];
#pragma unroll
      for (int i = 0; i < 4; i++) x[i] = __builtin_amdgcn_exp2f(acc[2][m][i] * rl[m]);
#pragma unroll
      for (int i = 0; i < 4; i++) x[i] = __builtin_amdgcn_rcpf(__builtin_fmaf(x[i], irs[m], irs[m])) * acc[2][m][i];
      pk[2][m][0] = pack_bf(x[0], x[1]); pk[2][m][1] = pack_bf(x[2], x[3]);
      if (m == 3) xw[64] = u2{pk[2][3][0], pk[2][3][1]};
      SCHED_FENCE();
    }
    asm volatile("s_waitcnt lgkmcnt(0)\n\ts_barrier" ::: "memory");
    const u2 ox[2] = {xr[0], xr[64]};
#pragma unroll
    for (int h = 0; h < 2; h++) {
      const u2 o = ox[h];
      const u4 v = (wv & 1) ? u4{o.x, o.y, pk[2][2 * h][0], pk[2][2 * h][1]} : u4{pk[2][2 * h][0], pk[2][2 * h][1], o.x, o.y};
      __builtin_amdgcn_raw_buffer_store_b128(v, Hr, l16, hhalf[2 * h], ST_AUX);
    }
  }
  PT(7); PT_FLUSH(KID);
}

template <bool FINAL>
__global__ __launch_bounds__(256, 1) void k_gemm2(char* __restrict__ ws, __hip_bfloat16* __restrict__ out) {
  __shared__ __attribute__((aligned(16))) f4 red[4 * 3 * 5 * 64];
  const int tid = threadIdx.x, lane = tid & 63;
  const int wv = __builtin_amdgcn_readfirstlane(tid >> 6);
  const int b = blockIdx.x;
  const int xcd = b & 7, slot = b >> 3;
  const int mblk = (xcd >> 1) * 8 + (slot & 7), cs = (xcd & 1) * 4 + (slot >> 3);
  const int g = lane >> 4, r = lane & 15;
  constexpr int KID = FINAL ? 6 : 3;
  (void)KID;
  asm volatile("" ::: "a191");
  PT_DECL; PT(0);
  const unsigned l16 = lane * 16;
  const rsrc_t Hr = make_rsrc(ws + OFF_H + ((size_t)mblk * 4 * H_TS + wv * 12) * 1024, (4 * H_TS - wv * 12) * 1024);
  const rsrc_t Wr = make_rsrc(ws + (FINAL ? OFF_W2C : OFF_W2P) + ((size_t)cs * 5 * H_TS + wv * 12) * 1024, (5 * H_TS - wv * 12) * 1024);
  int hoff[4];
#pragma unroll
  for (int m = 0; m < 4; m++) hoff[m] = ((m + wv) & 3) * H_TS * 1024;
  constexpr int PF = G2_PF, ND = G2_ND, S0 = 12 - ND;
  bf8 hf[12][4], wf[12][5];
#define G2_LH(S, M) hf[S][M] = __builtin_bit_cast(bf8, __builtin_amdgcn_raw_buffer_load_b128(Hr, l16 + ((S) & 3) * 1024, hoff[M] + ((S) >> 2) * 4096, 0))
#define G2_LW(S, C) wf[S][C] = bload(Wr, l16, ((C) * H_TS + (S)) * 1024)
#define G2_LOAD(S, J) do { constexpr int s_ = (S), j_ = (J); \
    if (s_ < 12) { constexpr int q_ = s_ < 12 ? s_ : 0; \
      if (j_ == 0) { G2_LH(q_, 1); G2_LW(q_, 0); } if (j_ == 1) { G2_LH(q_, 2); G2_LW(q_, 1); } if (j_ == 2) { G2_LH(q_, 3); G2_LW(q_, 2); } \
      if (j_ == 3) { G2_LW(q_, 3); if (q_ < S0) G2_LH(q_, 0); } if (j_ == 4) G2_LW(q_, 4); } \
    else { constexpr int per_ = (ND + PF - 1) / PF, i_ = (s_ - 12) * per_ + j_; \
      if (j_ < per_ && i_ < ND) G2_LH(S0 + (i_ < ND ? i_ : 0), 0); \
      if (j_ == 4) { _Pragma("unroll") for (int e_ = 5; e_ < per_; e_++) if ((s_ - 12) * per_ + e_ < ND) G2_LH(S0 + ((s_ - 12) * per_ + e_) % ND, 0); } } \
    } while (0)
  const int mrow = mblk * 64 + wv * 16 + r;
  const int gq = 2 * (g & 1) + (g >> 1);
  const unsigned xo = gq * 512 + (((wv * 16 + r) * 8) ^ ((gq >> 1) << 7));
  u2 res[5];
  {
    const rsrc_t Rr = FINAL ? make_rsrc(ws + OFF_X3, 2048 * 1280) : make_rsrc(ws + OFF_XB + (size_t)mblk * XB_MS, XB_MS);
    const unsigned ro = FINAL ? (mrow * 640 + 4 * g) * 2 : xo;
    static_for<PF>([&](auto sI) {
      constexpr int s = sI.value;
      G2_LOAD(s, 0); G2_LOAD(s, 1); G2_LOAD(s, 2); G2_LOAD(s, 3); G2_LOAD(s, 4);
      SCHED_FENCE();
      if (s == 0) {
#pragma unroll
        for (int c = 0; c < 5; c++)
          res[c] = FINAL ? __builtin_amdgcn_raw_buffer_load_b64(Rr, ro + c * 32, cs * 160, 0)
                         : __builtin_amdgcn_raw_buffer_load_b64(Rr, ro + (c & 1) * 2048, cs * 10240 + (c >> 1) * 4096, 0);
        SCHED_FENCE();
      }
    });
  }
  PT(6);
  f4 acc[5][4];
#pragma unroll
  for (int c = 0; c < 5; c++)
#pragma unroll
    for (int m = 0; m < 4; m++) acc[c][m] = f4{0.f, 0.f, 0.f, 0.f};
#define G2_MFMA(S, C, M) acc[C][M] = __builtin_amdgcn_mfma_f32_16x16x32_bf16(wf[S][C], hf[S][M], acc[C][M], 0, 0, 0)
  f4* rw[3];
#pragma unroll
  for (int m = 1; m < 4; m++) rw[m - 1] = red + (((m + wv) & 3) * 3 + m - 1) * 5 * 64 + lane;
#define G2_WRITE(I) do { if ((I) >= 0 && (I) < 15) rw[(I) / 5][((I) % 5) * 64] = acc[(I) % 5][1 + (I) / 5]; } while (0)
  static_for<11>([&](auto sI) {
    constexpr int s = sI.value;
    static_for<5>([&](auto cI) {
      constexpr int c = cI.value;
      G2_MFMA(s, c, 1);
      if (s == 0 && c == 0) PT(2);
      G2_MFMA(s, c, 2); G2_MFMA(s, c, 3);
      if (s < S0) G2_MFMA(s, c, 0);
      G2_LOAD(s + PF, c);
      SCHED_FENCE();
    });
    if (s == 0) PT(1);
  });
  constexpr int NM = 15 + (S0 == 12 ? 1 : ND) * 5, W0 = 7, NB = W0 + 15 * G2_WS + G2_BD;
  f4 part[15];
  static_for<NM>([&](auto iI) {
    constexpr int i = iI.value;
    if (i < 15) G2_MFMA(11, i % 5, 1 + i / 5);
    else G2_MFMA((S0 == 12 ? 11 : S0) + (i - 15) / 5, i % 5, 0);
    if (i < 5) G2_LOAD(11 + PF, i);
    if (i >= W0 && (i - W0) % G2_WS == 0) G2_WRITE((i - W0) / G2_WS);
    if (i == 15) PT(3);
    if (i == (NB < NM ? NB : NM - 1)) {
#pragma unroll
      for (int k = (NM - 1 - W0) / G2_WS + 1; k < 15; k++) G2_WRITE(k);
      asm volatile("s_waitcnt lgkmcnt(0)\n\ts_barrier" ::: "memory");
      PT(4);
#pragma unroll
      for (int k = 0; k < 15; k++) part[k] = red[(wv * 15 + k) * 64 + lane];
    }
    SCHED_FENCE();
  });
  PT(5);
  f4 own[5];
#pragma unroll
  for (int c = 0; c < 5; c++) own[c] = (part[c] + part[5 + c]) + (part[10 + c] + acc[c][0]);
  float s = 0.f;
#pragma unroll
  for (int c = 0; c < 5; c++) {
    if (!FINAL) {
      const u4 ob = __builtin_bit_cast(u4, own[c]);
      u2 s0 = __builtin_amdgcn_permlane32_swap(ob.x, ob.z, false, false);
      u2 s1 = __builtin_amdgcn_permlane32_swap(ob.y, ob.w, false, false);
      own[c] = __builtin_bit_cast(f4, u4{s0.x, s0.y, s1.x, s1.y});
    }
    f4 o = f4{bf_lo(res[c].x), bf_hi(res[c].x), bf_lo(res[c].y), bf_hi(res[c].y)} + own[c];
    u2 pk = u2{pack_bf(o.x, o.y), pack_bf(o.z, o.w)};
    if (FINAL) {
      __builtin_amdgcn_raw_buffer_store_b64(pk, make_rsrc(out, 2048 * 1280), (mrow * 640 + 4 * g) * 2 + c * 32, cs * 160, AUX_G2);
    } else {
      __builtin_amdgcn_raw_buffer_store_b64(pk, make_rsrc(ws + OFF_XB + (size_t)mblk * XB_MS, XB_MS), xo + (c & 1) * 2048, cs * 10240 + (c >> 1) * 4096, AUX_G2);
      unsigned p0 = (cs == 0 && c == 0 && g == 0) ? (pk.x & 0xffff0000u) : pk.x;
      s = dot2(p0, s);
      s = dot2(pk.y, s);
    }
  }
  if (!FINAL) {
    s += __shfl_xor(s, 16);
    s += __shfl_xor(s, 32);
    if (g == 0) unsafeAtomicAdd((double*)(ws + OFF_SSQ) + mrow, (double)s);
  }
  if (FINAL) asm volatile("k6_code_end:");
  PT(7); PT_FLUSH(KID);
}

extern "C" void kernel_launch(void* const* d_in, const int* in_sizes, int n_in, void* d_out, int out_size, void* d_ws,
                              size_t ws_size, hipStream_t stream) {
  const uint16_t* x = (const uint16_t*)d_in[0];
  const uint16_t* wr = (const uint16_t*)d_in[1];
  const uint16_t* wi = (const uint16_t*)d_in[2];
  const uint16_t* w1 = (const uint16_t*)d_in[4];
  const uint16_t* w2 = (const uint16_t*)d_in[5];
  char* ws = (char*)d_ws;
  __hip_bfloat16* out = (__hip_bfloat16*)d_out;
  (void)hipFuncSetAttribute((const void*)k_colfilter<true>, hipFuncAttributeMaxDynamicSharedMemorySize, LDS_CF);
  (void)hipFuncSetAttribute((const void*)k_colfilter<false>, hipFuncAttributeMaxDynamicSharedMemorySize, LDS_CF);
  (void)hipFuncSetAttribute((const void*)k_gemm1<false>, hipFuncAttributeMaxDynamicSharedMemorySize, LDS_G1);
  (void)hipFuncSetAttribute((const void*)k_gemm1<true>, hipFuncAttributeMaxDynamicSharedMemorySize, LDS_G1I);
#define NK 7
  k_prep<<<K0_X + K0_W1 + K0_WT, 512, 0, stream>>>(x, wr, wi, w1, w2, (const char*)d_out, ws);
  if (NK > 1) k_colfilter<true><<<256, 256, LDS_CF, stream>>>(w2, ws);
  if (NK > 2) k_gemm1<false><<<256, 256, LDS_G1, stream>>>(ws);
  if (NK > 3) k_gemm2<false><<<256, 256, 0, stream>>>(ws, out);
  if (NK > 4) k_colfilter<false><<<256, 256, LDS_CF, stream>>>(w2, ws);
  if (NK > 5) k_gemm1<true><<<256, 256, LDS_G1I, stream>>>(ws);
  if (NK > 6) k_gemm2<true><<<256, 256, 0, stream>>>(ws, out);
}
```

```cpp
#pragma clang diagnostic ignored "-Warray-bounds"
#include <hip/hip_runtime.h>
#include <hip/hip_bf16.h>
#include <stdint.h>

typedef float c2 __attribute__((ext_vector_type(2)));
typedef float f4 __attribute__((ext_vector_type(4)));
typedef unsigned u2 __attribute__((ext_vector_type(2)));
typedef unsigned u4 __attribute__((ext_vector_type(4)));
typedef __bf16 bf2 __attribute__((ext_vector_type(2)));
typedef __bf16 bf8 __attribute__((ext_vector_type(8)));

#define DEV __device__ __forceinline__
#define SCHED_FENCE() __builtin_amdgcn_sched_barrier(0)

constexpr int S_ = 2048, D_ = 640, F_ = 1536, DF_ = 321;
constexpr float LN_EPS = 1e-5f;

constexpr int XB_MS = 81920 + 512;
constexpr int H_TS = 49;
constexpr int W1_TS = 21;
constexpr size_t OFF_XB = 0;
constexpr size_t OFF_H = OFF_XB + 32ull * XB_MS;
constexpr size_t OFF_W1P = OFF_H + 128ull * H_TS * 1024;
constexpr size_t OFF_W2P = OFF_W1P + 96ull * W1_TS * 1024;
constexpr size_t OFF_W2C = OFF_W2P + 40ull * H_TS * 1024;
constexpr size_t OFF_WT = OFF_W2C + 40ull * H_TS * 1024;
constexpr size_t OFF_X3 = OFF_WT + 160ull * 4 * 256 * 16;
constexpr size_t OFF_RS0 = OFF_X3 + 2048ull * 640 * 2;
constexpr size_t OFF_SSQ = OFF_RS0 + 2048 * 4;

#define TSTAMP(K, P) do {} while (0)
#define PT_DECL do {} while (0)
#define PT(i) do {} while (0)
#define PT_FLUSH(K) do {} while (0)
#define ST_AUX 16
#define AUX_X3 16
#define AUX_G2 16
#define AUX_K0 16
#define AUX_W1 16
#define AUX_CF 16
typedef __amdgpu_buffer_rsrc_t rsrc_t;
DEV rsrc_t make_rsrc(const void* p, unsigned bytes) { return __builtin_amdgcn_make_buffer_rsrc((void*)p, 0, bytes, 0x00020000); }
DEV bf8 bload(rsrc_t rs, unsigned voff, int OFF) {
  return __builtin_bit_cast(bf8, __builtin_amdgcn_raw_buffer_load_b128(rs, voff + (OFF & 4095), OFF & ~4095, 0));
}
DEV c2 add_mi(c2 a, c2 b) {
  c2 d; asm("v_pk_add_f32 %0, %1, %2 op_sel:[0,1] op_sel_hi:[1,0] neg_hi:[0,1]" : "=v"(d) : "v"(a), "v"(b)); return d;
}
DEV c2 add_pi(c2 a, c2 b) {
  c2 d; asm("v_pk_add_f32 %0, %1, %2 op_sel:[0,1] op_sel_hi:[1,0] neg_lo:[0,1]" : "=v"(d) : "v"(a), "v"(b)); return d;
}
DEV c2 cmul(c2 a, c2 w) {
  c2 t, d;
  asm("v_pk_mul_f32 %0, %1, %2 op_sel:[0,0] op_sel_hi:[0,1]" : "=v"(t) : "v"(a), "v"(w));
  asm("v_pk_fma_f32 %0, %1, %2, %3 op_sel:[1,1,0] op_sel_hi:[1,0,1] neg_lo:[0,1,0]" : "=v"(d) : "v"(a), "v"(w), "v"(t));
  return d;
}
DEV c2 cmulc(c2 a, c2 w) {
  c2 t, d;
  asm("v_pk_mul_f32 %0, %1, %2 op_sel:[0,0] op_sel_hi:[0,1] neg_hi:[0,1]" : "=v"(t) : "v"(a), "v"(w));
  asm("v_pk_fma_f32 %0, %1, %2, %3 op_sel:[1,1,0] op_sel_hi:[1,0,1]" : "=v"(d) : "v"(a), "v"(w), "v"(t));
  return d;
}
template <int SGN> DEV c2 twmul(c2 a, c2 w) { return SGN < 0 ? cmul(a, w) : cmulc(a, w); }
template <int SGN> DEV c2 add_rot(c2 a, c2 b) { return SGN < 0 ? add_mi(a, b) : add_pi(a, b); }
template <int SGN> DEV c2 sub_rot(c2 a, c2 b) { return SGN < 0 ? add_pi(a, b) : add_mi(a, b); }
DEV c2 twiddle_rev(float rev) {
  c2 t = c2{__builtin_amdgcn_cosf(rev), -__builtin_amdgcn_sinf(rev)};
  asm("s_nop 1" : "+v"(t));
  return t;
}
template <int N> DEV void tw_powers(c2* t, c2 w) {
  t[0] = w;
#pragma unroll
  for (int k = 2; k <= N; k++) {
    if ((k & 1) == 0) t[k - 1] = cmul(t[k / 2 - 1], t[k / 2 - 1]);
    else {
      int hi = k >= 8 ? 8 : (k >= 4 ? 4 : 2);
      t[k - 1] = cmul(t[hi - 1], t[k - hi - 1]);
    }
  }
}

template <int SGN> DEV void dft4(c2& a, c2& b, c2& c, c2& d) {
  c2 s0 = a + c, s1 = a - c, s2 = b + d, s3 = b - d;
  a = s0 + s2; c = s0 - s2;
  b = add_rot<SGN>(s1, s3); d = sub_rot<SGN>(s1, s3);
}
template <int SGN> DEV c2 rot1(c2 a) { return add_rot<SGN>(a, a) * 0.70710678118654752f; }
template <int SGN> DEV c2 rot3(c2 a) { return sub_rot<SGN>(a, a) * -0.70710678118654752f; }

template <int SGN> DEV void dft8(c2* v) {
  c2 e0 = v[0], e1 = v[2], e2 = v[4], e3 = v[6], o0 = v[1], o1 = v[3], o2 = v[5], o3 = v[7];
  dft4<SGN>(e0, e1, e2, e3);
  dft4<SGN>(o0, o1, o2, o3);
  o1 = rot1<SGN>(o1); o3 = rot3<SGN>(o3);
  v[0] = e0 + o0; v[4] = e0 - o0;
  v[1] = e1 + o1; v[5] = e1 - o1;
  v[2] = add_rot<SGN>(e2, o2); v[6] = sub_rot<SGN>(e2, o2);
  v[3] = e3 + o3; v[7] = e3 - o3;
}
template <int SGN> DEV void dft16(c2* v) {
  const c2 w1 = c2{0.92387953251128674f, -0.38268343236508977f};
  const c2 w3 = c2{0.38268343236508977f, -0.92387953251128674f};
  c2 y[4][4];
#pragma unroll
  for (int i = 0; i < 4; i++) {
    c2 a = v[i], b = v[i + 4], c = v[i + 8], d = v[i + 12];
    dft4<SGN>(a, b, c, d);
    y[i][0] = a; y[i][1] = b; y[i][2] = c; y[i][3] = d;
  }
  y[1][1] = twmul<SGN>(y[1][1], w1);
  y[1][2] = rot1<SGN>(y[1][2]);
  y[1][3] = twmul<SGN>(y[1][3], w3);
  y[2][1] = rot1<SGN>(y[2][1]);
  y[2][3] = rot3<SGN>(y[2][3]);
  y[3][1] = twmul<SGN>(y[3][1], w3);
  y[3][2] = rot3<SGN>(y[3][2]);
  y[3][3] = -twmul<SGN>(y[3][3], w1);
#pragma unroll
  for (int q = 0; q < 4; q++) {
    c2 a = y[0][q], b = y[1][q], c = y[2][q], d = y[3][q];
    if (q == 2) {
      c2 s0 = add_rot<SGN>(a, c), s1 = sub_rot<SGN>(a, c), s2 = b + d, s3 = b - d;
      a = s0 + s2; c = s0 - s2;
      b = add_rot<SGN>(s1, s3); d = sub_rot<SGN>(s1, s3);
    } else {
      dft4<SGN>(a, b, c, d);
    }
    v[q] = a; v[q + 4] = b; v[q + 8] = c; v[q + 12] = d;
  }
}
template <int SGN> DEV void twmulp(c2& a0, c2 w0, c2& a1, c2 w1) {
  c2 d0, d1;
  if (SGN < 0)
    asm("v_pk_mul_f32 %0, %2, %4 op_sel:[0,0] op_sel_hi:[0,1]\n\t"
        "v_pk_mul_f32 %1, %3, %5 op_sel:[0,0] op_sel_hi:[0,1]\n\t"
        "v_pk_fma_f32 %0, %2, %4, %0 op_sel:[1,1,0] op_sel_hi:[1,0,1] neg_lo:[0,1,0]\n\t"
        "v_pk_fma_f32 %1, %3, %5, %1 op_sel:[1,1,0] op_sel_hi:[1,0,1] neg_lo:[0,1,0]"
        : "=&v"(d0), "=&v"(d1) : "v"(a0), "v"(a1), "v"(w0), "v"(w1));
  else
    asm("v_pk_mul_f32 %0, %2, %4 op_sel:[0,0] op_sel_hi:[0,1] neg_hi:[0,1]\n\t"
        "v_pk_mul_f32 %1, %3, %5 op_sel:[0,0] op_sel_hi:[0,1] neg_hi:[0,1]\n\t"
        "v_pk_fma_f32 %0, %2, %4, %0 op_sel:[1,1,0] op_sel_hi:[1,0,1]\n\t"
        "v_pk_fma_f32 %1, %3, %5, %1 op_sel:[1,1,0] op_sel_hi:[1,0,1]"
        : "=&v"(d0), "=&v"(d1) : "v"(a0), "v"(a1), "v"(w0), "v"(w1));
  a0 = d0; a1 = d1;
}
template <int SGN, typename PRE, typename POST> DEV void dft16s(c2* v, PRE pre, POST post) {
  const c2 w1 = c2{0.92387953251128674f, -0.38268343236508977f};
  const c2 w3 = c2{0.38268343236508977f, -0.92387953251128674f};
  const c2 w9 = c2{-0.92387953251128674f, 0.38268343236508977f};
  c2 y[4][4];
#pragma unroll
  for (int i = 0; i < 4; i++) {
    pre(i);
    c2 a = v[i], b = v[i + 4], c = v[i + 8], d = v[i + 12];
    dft4<SGN>(a, b, c, d);
    y[i][0] = a; y[i][1] = b; y[i][2] = c; y[i][3] = d;
    if (i == 1) { twmulp<SGN>(y[1][1], w1, y[1][3], w3); y[1][2] = rot1<SGN>(y[1][2]); }
    if (i == 2) { y[2][1] = rot1<SGN>(y[2][1]); y[2][3] = rot3<SGN>(y[2][3]); }
    if (i == 3) { twmulp<SGN>(y[3][1], w3, y[3][3], w9); y[3][2] = rot3<SGN>(y[3][2]); }
    SCHED_FENCE();
  }
#pragma unroll
  for (int q = 0; q < 4; q++) {
    c2 a = y[0][q], b = y[1][q], c = y[2][q], d = y[3][q];
    if (q == 2) {
      c2 s0 = add_rot<SGN>(a, c), s1 = sub_rot<SGN>(a, c), s2 = b + d, s3 = b - d;
      a = s0 + s2; c = s0 - s2;
      b = add_rot<SGN>(s1, s3); d = sub_rot<SGN>(s1, s3);
    } else {
      dft4<SGN>(a, b, c, d);
    }
    v[q] = a; v[q + 4] = b; v[q + 8] = c; v[q + 12] = d;
    post(q);
    SCHED_FENCE();
  }
}
template <int SGN> DEV void dft5(c2* x) {
  const float c1 = 0.30901699437494742f, c2_ = -0.80901699437494742f;
  const float s1 = 0.95105651629515357f, s2 = 0.58778525229247313f;
  c2 t1 = x[1] + x[4], t2 = x[2] + x[3], t3 = x[1] - x[4], t4 = x[2] - x[3];
  c2 a1 = x[0] + c1 * t1 + c2_ * t2, a2 = x[0] + c2_ * t1 + c1 * t2;
  c2 b1 = s1 * t3 + s2 * t4, b2 = s2 * t3 - s1 * t4;
  x[0] = x[0] + t1 + t2;
  x[1] = add_rot<SGN>(a1, b1); x[4] = sub_rot<SGN>(a1, b1);
  x[2] = add_rot<SGN>(a2, b2); x[3] = sub_rot<SGN>(a2, b2);
}

DEV void lds_fence() { asm volatile("" ::: "memory"); __builtin_amdgcn_wave_barrier(); asm volatile("" ::: "memory"); }
DEV float bf_lo(unsigned u) { return __builtin_bit_cast(float, u << 16); }
DEV float bf_hi(unsigned u) { return __builtin_bit_cast(float, u & 0xffff0000u); }
DEV unsigned pack_bf(float a, float b) { return __builtin_bit_cast(unsigned, __builtin_convertvector(c2{a, b}, bf2)); }

template <int SGN> DEV void twmul2(c2& a0, c2& a1, c2 w) {
  c2 d0, d1;
  if (SGN < 0)
    asm("v_pk_mul_f32 %0, %2, %4 op_sel:[0,0] op_sel_hi:[0,1]\n\t"
        "v_pk_mul_f32 %1, %3, %4 op_sel:[0,0] op_sel_hi:[0,1]\n\t"
        "v_pk_fma_f32 %0, %2, %4, %0 op_sel:[1,1,0] op_sel_hi:[1,0,1] neg_lo:[0,1,0]\n\t"
        "v_pk_fma_f32 %1, %3, %4, %1 op_sel:[1,1,0] op_sel_hi:[1,0,1] neg_lo:[0,1,0]"
        : "=&v"(d0), "=&v"(d1) : "v"(a0), "v"(a1), "v"(w));
  else
    asm("v_pk_mul_f32 %0, %2, %4 op_sel:[0,0] op_sel_hi:[0,1] neg_hi:[0,1]\n\t"
        "v_pk_mul_f32 %1, %3, %4 op_sel:[0,0] op_sel_hi:[0,1] neg_hi:[0,1]\n\t"
        "v_pk_fma_f32 %0, %2, %4, %0 op_sel:[1,1,0] op_sel_hi:[1,0,1]\n\t"
        "v_pk_fma_f32 %1, %3, %4, %1 op_sel:[1,1,0] op_sel_hi:[1,0,1]"
        : "=&v"(d0), "=&v"(d1) : "v"(a0), "v"(a1), "v"(w));
  a0 = d0; a1 = d1;
}
DEV void tangle2(c2& d0, c2& d1, c2 A0, c2 B0, c2 A1, c2 B1, c2 w) {
  c2 e0, e1, o0, o1;
  asm("v_pk_add_f32 %0, %4, %5 neg_hi:[0,1]\n\t"
      "v_pk_add_f32 %1, %6, %7 neg_hi:[0,1]\n\t"
      "v_pk_add_f32 %2, %4, %5 neg_lo:[0,1]\n\t"
      "v_pk_add_f32 %3, %6, %7 neg_lo:[0,1]\n\t"
      "v_pk_fma_f32 %0, %2, %8, %0 op_sel:[0,0,0] op_sel_hi:[0,1,1]\n\t"
      "v_pk_fma_f32 %1, %3, %8, %1 op_sel:[0,0,0] op_sel_hi:[0,1,1]\n\t"
      "v_pk_fma_f32 %0, %2, %8, %0 op_sel:[1,1,0] op_sel_hi:[1,0,1] neg_lo:[0,1,0]\n\t"
      "v_pk_fma_f32 %1, %3, %8, %1 op_sel:[1,1,0] op_sel_hi:[1,0,1] neg_lo:[0,1,0]"
      : "=&v"(e0), "=&v"(e1), "=&v"(o0), "=&v"(o1) : "v"(A0), "v"(B0), "v"(A1), "v"(B1), "v"(w));
  d0 = e0; d1 = e1;
}

constexpr int SCR_F4 = 361;
struct Tw320 { c2 a[4]; c2 b[7]; };
DEV void tw320_init(Tw320& tw, int lane, c2 u0, float scale) {
  tw_powers<4>(tw.a, cmul(u0, u0));
  tw_powers<7>(tw.b, twiddle_rev((float)(lane & 7) * (1.0f / 64.0f)));
#pragma unroll
  for (int i = 0; i < 7; i++) tw.b[i] = tw.b[i] * scale;
}
#define FFT_HOOK_NONE(i)
template <int SGN, int NP, typename HOOK> DEV void fft320x2(c2 (*z)[5], const Tw320& tw, float scale, f4* scr, int lane, c2 (*o)[8], HOOK hook) {
  const int u = lane < 40 ? lane : 39;
  const int q = u >> 3, a = u & 7;
#pragma unroll
  for (int p = 0; p < NP; p++) {
    dft5<SGN>(z[2 * p]); dft5<SGN>(z[2 * p + 1]);
#pragma unroll
    for (int i = 1; i < 5; i++) twmul2<SGN>(z[2 * p][i], z[2 * p + 1][i], tw.a[i - 1]);
    lds_fence();
#pragma unroll
    for (int i = 0; i < 5; i++) scr[p * SCR_F4 + i * 72 + lane] = f4{z[2 * p][i].x, z[2 * p][i].y, z[2 * p + 1][i].x, z[2 * p + 1][i].y};
    lds_fence();
#pragma unroll
    for (int b = 0; b < 8; b++) { f4 t = scr[p * SCR_F4 + q * 72 + a + 8 * b]; o[2 * p][b] = t.xy; o[2 * p + 1][b] = t.zw; }
    SCHED_FENCE();
  }
  hook(0);
#pragma unroll
  for (int p = 0; p < NP; p++) {
    dft8<SGN>(o[2 * p]); dft8<SGN>(o[2 * p + 1]);
    hook(1);
    o[2 * p][0] = o[2 * p][0] * scale; o[2 * p + 1][0] = o[2 * p + 1][0] * scale;
#pragma unroll
    for (int c = 1; c < 8; c++) twmul2<SGN>(o[2 * p][c], o[2 * p + 1][c], tw.b[c - 1]);
    lds_fence();
#pragma unroll
    for (int c = 0; c < 8; c++) scr[p * SCR_F4 + (q * 8 + c) * 9 + a] = f4{o[2 * p][c].x, o[2 * p][c].y, o[2 * p + 1][c].x, o[2 * p + 1][c].y};
    lds_fence();
#pragma unroll
    for (int i = 0; i < 8; i++) { f4 t = scr[p * SCR_F4 + u * 9 + i]; o[2 * p][i] = t.xy; o[2 * p + 1][i] = t.zw; }
    SCHED_FENCE();
  }
  hook(2);
  dft8<SGN>(o[0]);
  hook(3);
  dft8<SGN>(o[1]);
  lds_fence();
}
constexpr float FWD_SC = 0.5f * 0.05590169943749474f;
template <int NP> DEV void rfft2_rows(const unsigned (*rw)[5], const Tw320& tw, f4* scr, int lane, c2 (*o)[8]) {
  c2 z[2 * NP][5];
#pragma unroll
  for (int i = 0; i < 2 * NP; i++)
#pragma unroll
    for (int j = 0; j < 5; j++) z[i][j] = c2{bf_lo(rw[i][j]), bf_hi(rw[i][j])};
  fft320x2<-1, NP>(z, tw, FWD_SC, scr, lane, o, [](int) {});
  const int u = lane < 40 ? lane : 39;
  f4* pp = scr + (u >> 3) + 5 * (u & 7);
#pragma unroll
  for (int p = 0; p < NP; p++) {
    if (p > 0) { dft8<-1>(o[2 * p]); dft8<-1>(o[2 * p + 1]); lds_fence(); }
#pragma unroll
    for (int e = 0; e < 8; e++) pp[p * SCR_F4 + 40 * e] = f4{o[2 * p][e].x, o[2 * p][e].y, o[2 * p + 1][e].x, o[2 * p + 1][e].y};
  }
}
DEV void untangle2(c2& x0, c2& x1, f4 A, f4 B, c2 w, bool k_is_0) {
  tangle2(x0, x1, A.xy, B.xy, A.zw, B.zw, w);
  if (k_is_0) {
    const float r = 1.41421356237309505f;
    x0 = c2{(A.x + A.y) * r, (A.x - A.y) * r};
    x1 = c2{(A.z + A.w) * r, (A.z - A.w) * r};
  }
}
DEV c2 mul_mi(c2 w) { return c2{w.y, -w.x}; }

template <int CTRL> DEV float dpp_add(float v) {
  return v + __builtin_bit_cast(float, __builtin_amdgcn_update_dpp(0, __builtin_bit_cast(int, v), CTRL, 0xf, 0xf, true));
}
DEV float wave_sum63(float v) {
  v = dpp_add<0x111>(v);
  v = dpp_add<0x112>(v);
  v = dpp_add<0x114>(v);
  v = dpp_add<0x118>(v);
  v = v + __builtin_bit_cast(float, __builtin_amdgcn_update_dpp(0, __builtin_bit_cast(int, v), 0x142, 0xa, 0xf, true));
  v = v + __builtin_bit_cast(float, __builtin_amdgcn_update_dpp(0, __builtin_bit_cast(int, v), 0x143, 0xc, 0xf, true));
  return v;
}

#define CODE_ADDR(A, SYM) asm("s_getpc_b64 vcc\n\ts_add_u32 vcc_lo, vcc_lo, " SYM "@rel32@lo+4\n\ts_addc_u32 vcc_hi, vcc_hi, " SYM "@rel32@hi+12\n\ts_mov_b64 %0, vcc" \
                              : "=s"(A) :: "vcc", "scc")
#define CODE_PF(P, COND, SYM, END) do { const char *b_, *e_; CODE_ADDR(b_, SYM); CODE_ADDR(e_, END); \
    long n_ = e_ - b_ - 4; n_ = n_ < 0 ? 0 : n_; long o_ = (long)threadIdx.x * 64; o_ = o_ < n_ ? o_ : n_ & ~3l; P = (COND) ? b_ + o_ : P; } while (0)
#define PF_LOAD(P) (*(const __attribute__((address_space(1))) unsigned*)(P))
#define SYM_K1 "_Z11k_colfilterILb1EEvPKtPc"
#define SYM_K4 "_Z11k_colfilterILb0EEvPKtPc"
#define SYM_K2 "_Z7k_gemm1ILb0EEvPc"
#define SYM_K5 "_Z7k_gemm1ILb1EEvPc"
#define SYM_K3 "_Z7k_gemm2ILb0EEvPcP14__hip_bfloat16"
#define SYM_K6 "_Z7k_gemm2ILb1EEvPcP14__hip_bfloat16"
#define CODE_PF_K1(P, C) CODE_PF(P, C, SYM_K1, SYM_K4)
#define CODE_PF_K4(P, C) CODE_PF(P, C, SYM_K4, SYM_K2)
#define CODE_PF_K2(P, C) CODE_PF(P, C, SYM_K2, SYM_K5)
#define CODE_PF_K5(P, C) CODE_PF(P, C, SYM_K5, SYM_K3)
#define CODE_PF_K3(P, C) CODE_PF(P, C, SYM_K3, SYM_K6)
#define CODE_PF_K6(P, C) CODE_PF(P, C, SYM_K6, "k6_code_end")

DEV int frag_f(int T, int g, int j) {
  int G = T / 3, r = T - 3 * G;
  int ta = r == 0 ? 0 : (r == 1 ? 3 : 2), tb = r == 0 ? 1 : (r == 1 ? 4 : 5);
  return 96 * G + 16 * (j < 4 ? ta : tb) + 4 * g + (j & 3);
}

constexpr int K0_X = 128, K0_W1 = 96, K0_WT = 128;
template <bool is_x>
DEV void prep_rows(const uint16_t* __restrict__ x, const uint16_t* __restrict__ w1, char* __restrict__ ws, f4* scr_all, u2* stage, int grp) {
  const int tid = threadIdx.x, lane = tid & 63, wv = tid >> 6;
  f4* scr = scr_all + wv * SCR_F4;
  unsigned rw2[2][5];
  unsigned *r0 = rw2[0], *r1 = rw2[1];
  u4 s0[2], s1[2];
  if (is_x) {
    const unsigned* row = (const unsigned*)(x + (size_t)(16 * grp + 2 * wv) * D_);
#pragma unroll
    for (int j = 0; j < 5; j++) { r0[j] = row[lane + 64 * j]; r1[j] = row[320 + lane + 64 * j]; }
    if (tid < 16) ((double*)(ws + OFF_SSQ))[16 * grp + tid] = 0.0;
  } else {
#pragma unroll
    for (int it = 0; it < 2; it++) {
      int n = it == 0 ? tid : 512 + (tid & 127);
      const uint16_t* p = w1 + (size_t)(n >> 1) * (2 * F_) + grp * 16 + (n & 1) * 8;
      s0[it] = *(const u4*)p; s1[it] = *(const u4*)(p + F_);
    }
  }
  SCHED_FENCE();
  Tw320 tw;
  tw320_init(tw, lane, twiddle_rev((float)lane * (1.0f / 640.0f)), FWD_SC);
  c2 wo[3][2];
  wo[0][0] = mul_mi(twiddle_rev((float)(tid >> 3) * (1.0f / 320.0f)));
  wo[1][0] = cmul(wo[0][0], c2{0.30901699437494742f, -0.95105651629515357f});
  wo[2][0] = cmul(wo[0][0], c2{-0.80901699437494742f, -0.58778525229247313f});
#pragma unroll
  for (int it = 0; it < 3; it++) wo[it][1] = cmul(wo[it][0], c2{0.99995180895932817f, -0.00981731933714962f});
  SCHED_FENCE();
  if (!is_x) {
    asm volatile("" : "+v"(s0[1]), "+v"(s1[1]));
#pragma unroll
    for (int it = 0; it < 2; it++) {
      int n = tid + 512 * it;
      if (it == 0 || tid < 128) {
#pragma unroll
        for (int i = 0; i < 4; i++)
          stage[((n & 1) * 4 + i) * 324 + (n >> 1)] = u2{__builtin_amdgcn_perm(s1[it][i], s0[it][i], 0x05040100u), __builtin_amdgcn_perm(s1[it][i], s0[it][i], 0x07060302u)};
      }
    }
    __syncthreads();
#pragma unroll
    for (int j = 0; j < 5; j++) { u2 t = stage[wv * 324 + lane + 64 * j]; r0[j] = t.x; r1[j] = t.y; }
  }
  TSTAMP(0, 1);
  c2 o[2][8];
  rfft2_rows<1>(rw2, tw, scr, lane, o);
  if (is_x) {
    float ss[2];
#pragma unroll
    for (int rr = 0; rr < 2; rr++) {
      c2 t = o[rr][0];
      if (lane == 0) t = c2{(t.x - t.y) * 0.70710678118654752f, 0.f};
      c2 acc = t * t;
#pragma unroll
      for (int e = 1; e < 8; e++) acc += o[rr][e] * o[rr][e];
      ss[rr] = wave_sum63(lane < 40 ? acc.x + acc.y : 0.f);
    }
    if (lane == 63)
      *(c2*)(ws + OFF_RS0 + (size_t)(16 * grp + 2 * wv) * 4) =
          c2{__builtin_amdgcn_rsqf(ss[0] * (1.0f / 160.0f) + LN_EPS), __builtin_amdgcn_rsqf(ss[1] * (1.0f / 160.0f) + LN_EPS)} * (1.0f / 2048.0f);
  }
  TSTAMP(0, 2);
  __syncthreads();
  const int rp = tid & 7;
  const f4* S = scr_all + rp * SCR_F4;
#pragma unroll
  for (int it = 0; it < 3; it++) {
    if (it == 2 && tid >= 256) break;
    const int q4 = (tid >> 3) + 64 * it;
    f4 A0 = S[2 * q4], A1 = S[2 * q4 + 1], B0 = S[(it == 0 && q4 == 0) ? 0 : 320 - 2 * q4], B1 = S[319 - 2 * q4];
    c2 x00, x10, x01, x11;
    untangle2(x00, x10, A0, B0, wo[it][0], it == 0 && q4 == 0);
    untangle2(x01, x11, A1, B1, wo[it][1], false);
    if (is_x) {
      u4 v = u4{pack_bf(x00.x, x00.y), pack_bf(x01.x, x01.y), pack_bf(x10.x, x10.y), pack_bf(x11.x, x11.y)};
      __builtin_amdgcn_raw_buffer_store_b128(v, make_rsrc(ws + OFF_XB + (size_t)(grp >> 2) * XB_MS, XB_MS), q4 * 512 + (((grp & 3) * 128 + rp * 16) ^ (((q4 >> 1) & 1) << 7)), 0, AUX_K0);
    } else {
      if (it == 0 && q4 == 0) { x00.x = 0.f; x10.x = 0.f; }
      const rsrc_t Wr = make_rsrc(ws + OFF_W1P + (size_t)grp * W1_TS * 1024, W1_TS * 1024);
      const bool od = q4 & 1;
      const u2 c0 = u2{pack_bf(x00.x, x00.y), pack_bf(x01.x, x01.y)}, c1 = u2{pack_bf(x10.x, x10.y), pack_bf(x11.x, x11.y)};
      const u2 snd = od ? c0 : c1, kp = od ? c1 : c0;
      const u2 rcv = u2{(unsigned)__builtin_amdgcn_update_dpp(0, (int)snd.x, 0x128, 0xf, 0xf, false), (unsigned)__builtin_amdgcn_update_dpp(0, (int)snd.y, 0x128, 0xf, 0xf, false)};
      const u4 v = od ? u4{rcv.x, rcv.y, kp.x, kp.y} : u4{kp.x, kp.y, rcv.x, rcv.y};
      __builtin_amdgcn_raw_buffer_store_b128(v, Wr, ((q4 >> 1) * 16 + 2 * rp + (q4 & 1)) * 16, 0, AUX_W1);
    }
  }
  TSTAMP(0, 7);
}

__global__ __launch_bounds__(512) void k_prep(const uint16_t* __restrict__ x, const uint16_t* __restrict__ wr,
                                              const uint16_t* __restrict__ wi, const uint16_t* __restrict__ w1,
                                              const uint16_t* __restrict__ w2, const char* outp, char* __restrict__ ws) {
  __shared__ __attribute__((aligned(16))) f4 scr_all[8 * SCR_F4];
  __shared__ __attribute__((aligned(16))) u2 stage[8 * 324];
  const int tid = threadIdx.x, lane = tid & 63, wv = tid >> 6;
  const int b = blockIdx.x;
  TSTAMP(0, 0);
  const char* ps = nullptr;
  CODE_PF(ps, true, "_Z6k_prepPKtS0_S0_S0_S0_PKcPc", SYM_K1);
  const unsigned spf = tid < 192 ? PF_LOAD(ps) : 0u;
  SCHED_FENCE();

  if (b >= K0_X + K0_W1) {
    int bb = b - (K0_X + K0_W1);
    int unit = (bb & 7) * 16 + (bb >> 3);
    int u = unit & 3, bbit = (unit >> 2) & 1, kc = unit >> 3;
    unsigned* t32 = (unsigned*)scr_all;
    unsigned short re[2][6], im[2][6];
#pragma unroll
    for (int hh = 0; hh < 2; hh++) {
      int ks = kc + 16 * (2 * u + hh + 8 * bbit) + 256 * wv;
      const uint16_t* pr = wr + (size_t)ks * DF_;
      const uint16_t* pi = wi + (size_t)ks * DF_;
#pragma unroll
      for (int i = 0; i < 6; i++) {
        int c = lane + 64 * i;
        c = c < DF_ ? c : DF_ - 1;
        re[hh][i] = pr[c]; im[hh][i] = pi[c];
      }
    }
    SCHED_FENCE();
    const int sl = bb >> 3;
    const char* pa = ws + (size_t)(sl == 8 && tid * 65536u < OFF_RS0 ? tid * 65536u : 0u);
    if (sl == 9) pa = outp + (tid < 40 ? tid : 0) * 65536u;
    if (sl == 10) pa = (const char*)w2 + (tid < 30 ? tid : 0) * 65536u;
    CODE_PF_K1(pa, sl == 0); CODE_PF_K2(pa, sl == 1); CODE_PF_K3(pa, sl == 2); CODE_PF_K4(pa, sl == 3); CODE_PF_K5(pa, sl == 4); CODE_PF_K6(pa, sl == 5);
    CODE_PF(pa, sl == 6, SYM_K1 ".kd", SYM_K6 ".kd");
    { const char* a_; CODE_ADDR(a_, SYM_K6 ".kd"); pa = sl == 7 ? a_ : pa; }
    const int wi_ = (sl < 12 ? sl : 11) * 8 + (bb & 7), t3 = tid < 320 ? tid : 319, rr = t3 / 20;
    const unsigned cpf = PF_LOAD(pa);
    const unsigned wpf = *(const unsigned*)(w2 + (size_t)frag_f(wi_ >> 1, (wi_ & 1) * 2 + (rr >> 3), rr & 7) * D_ + (t3 % 20) * 32);
    SCHED_FENCE();
#pragma unroll
    for (int hh = 0; hh < 2; hh++)
#pragma unroll
      for (int i = 0; i < 6; i++) {
        int c = lane + 64 * i;
        if (c < DF_) t32[(hh * 8 + wv) * 324 + c] = (unsigned)re[hh][i] | ((unsigned)im[hh][i] << 16);
      }
    __syncthreads();
    const rsrc_t WT = make_rsrc(ws + OFF_WT, 160 * 4 * 256 * 16);
    for (int n = tid; n < 1280; n += 512) {
      int col = n & 1, hh = (n >> 1) & 1, ih = (n >> 2) & 1, q4 = n >> 3;
      int cc = 2 * q4 + col;
      if (cc == 0) cc = 320;
      u4 v;
#pragma unroll
      for (int e = 0; e < 4; e++) v[e] = t32[(hh * 8 + ih * 4 + e) * 324 + cc];
      int tau = (2 * u + hh) + 8 * kc;
      __builtin_amdgcn_raw_buffer_store_b128(v, WT, ((q4 * 4 + 2 * bbit + ih) * 256 + 2 * tau + col) * 16, 0, AUX_K0);
    }
    asm volatile("" :: "v"(cpf), "v"(spf), "v"(wpf));
    TSTAMP(0, 7);
    return;
  }

  if (b < K0_X) prep_rows<true>(x, w1, ws, scr_all, stage, (b & 7) * 16 + (b >> 3));
  else prep_rows<false>(x, w1, ws, scr_all, stage, ((b - K0_X) & 7) * 12 + ((b - K0_X) >> 3));
  asm volatile("" :: "v"(spf));
}

constexpr int P1 = 152;
constexpr int LDS_CF = 84 * 1024;
template <bool FIRST>
__global__ __launch_bounds__(256, 1) void k_colfilter(const uint16_t* __restrict__ w2, char* __restrict__ ws) {
  extern __shared__ __attribute__((aligned(16))) char smem[];
  const int tid = threadIdx.x;
  const int xcd = blockIdx.x & 7, slot = blockIdx.x >> 3;
  constexpr int KID = FIRST ? 1 : 4;
  (void)KID;
  if (slot >= 20) {
    TSTAMP(KID, 0);
    const int lane = tid & 63, wv = tid >> 6;
    const int i = (slot - 20) * 8 + xcd;
    const int T = i >> 1, g0 = (i & 1) * 2;
    unsigned pf[6], cpf = 0;
    const char* pfbase = ws + OFF_W1P + (size_t)(xcd & 1) * 48 * W1_TS * 1024 + tid * 64;
#define L2_PREFETCH() do { SCHED_FENCE(); _Pragma("unroll") for (int j = 0; j < 6; j++) { int blk = (slot - 20) + 12 * j; blk = blk < 63 ? blk : 62; \
      pf[j] = *(const unsigned*)(pfbase + blk * 16384); } \
      const char* pc_ = nullptr; \
      if (FIRST) { CODE_PF_K3(pc_, true); CODE_PF_K4(pc_, slot & 1); } else { CODE_PF_K5(pc_, true); CODE_PF_K6(pc_, slot & 1); } \
      cpf = PF_LOAD(pc_); \
      SCHED_FENCE(); } while (0)
    if (FIRST) {
      f4* scr_all = (f4*)smem;
      unsigned raw[4][5];
#pragma unroll
      for (int rr = 0; rr < 4; rr++) {
        int r = wv * 4 + rr;
        const unsigned* row = (const unsigned*)(w2 + (size_t)frag_f(T, g0 + (r >> 3), r & 7) * D_);
#pragma unroll
        for (int j = 0; j < 5; j++) raw[rr][j] = row[lane + 64 * j];
      }
      L2_PREFETCH();
      Tw320 tw;
      tw320_init(tw, lane, twiddle_rev((float)lane * (1.0f / 640.0f)), FWD_SC);
      const int k0 = (tid & 7) + 8 * (tid >> 4), gg = (tid >> 3) & 1;
      c2 wo[3];
      wo[0] = mul_mi(twiddle_rev((float)k0 * (1.0f / 640.0f)));
      wo[1] = cmul(wo[0], c2{0.30901699437494742f, -0.95105651629515357f});
      wo[2] = cmul(wo[0], c2{-0.80901699437494742f, -0.58778525229247313f});
      SCHED_FENCE();
      c2 o[4][8];
      rfft2_rows<2>(raw, tw, scr_all + (wv * 2) * SCR_F4, lane, o);
      __syncthreads();
      const f4* S = scr_all + gg * 4 * SCR_F4;
#pragma unroll
      for (int it = 0; it < 3; it++) {
        if (it == 2 && tid >= 128) break;
        const int k = k0 + 128 * it;
        const bool z = it == 0 && k == 0;
        f4 A[4], B[4];
#pragma unroll
        for (int jp = 0; jp < 4; jp++) { A[jp] = S[jp * SCR_F4 + k]; B[jp] = S[jp * SCR_F4 + (z ? 0 : 320 - k)]; }
        u4 re, im;
#pragma unroll
        for (int jp = 0; jp < 4; jp++) {
          c2 x0, x1;
          untangle2(x0, x1, A[jp], B[jp], wo[it], z);
          re[jp] = pack_bf(x0.x, x1.x); im[jp] = pack_bf(x0.y, x1.y);
        }
        const rsrc_t Wr = make_rsrc(ws + OFF_W2P, 40 * H_TS * 1024);
        const unsigned vo = (((k >> 3) * H_TS + T) * 64 + (g0 + gg) * 16 + (k & 7)) * 16;
        __builtin_amdgcn_raw_buffer_store_b128(re, Wr, vo, 0, AUX_W1);
        __builtin_amdgcn_raw_buffer_store_b128(im, Wr, vo + 128, 0, AUX_W1);
      }
    } else {
      const rsrc_t W2C = make_rsrc(ws + OFF_W2C, 40 * H_TS * 1024);
      unsigned short v[5][8];
#pragma unroll
      for (int it = 0; it < 5; it++) {
        int n = tid + 256 * it;
        int cl = n & 15, gg = (n >> 4) & 1, ct = n >> 5;
#pragma unroll
        for (int j = 0; j < 8; j++) v[it][j] = w2[(size_t)frag_f(T, g0 + gg, j) * D_ + ct * 16 + cl];
      }
      L2_PREFETCH();
#pragma unroll
      for (int it = 0; it < 5; it++) {
        int n = tid + 256 * it;
        int cl = n & 15, gg = (n >> 4) & 1, ct = n >> 5;
        u4 o;
#pragma unroll
        for (int j = 0; j < 4; j++) o[j] = (unsigned)v[it][2 * j] | ((unsigned)v[it][2 * j + 1] << 16);
        __builtin_amdgcn_raw_buffer_store_b128(o, W2C, ((ct * H_TS + T) * 64 + (g0 + gg) * 16 + cl) * 16, 0, AUX_K0);
      }
    }
    asm volatile("" :: "v"(pf[0]), "v"(pf[1]), "v"(pf[2]), "v"(pf[3]), "v"(pf[4]), "v"(pf[5]), "v"(cpf));
    TSTAMP(KID, 7);
    return;
  }
  typedef __attribute__((address_space(3))) volatile c2* ldsp;
  const int col = tid & 1, tau = tid >> 1;
  const int q4 = xcd * 20 + slot;
  const int lo = tau & 7, kc = tau >> 3;
  PT_DECL; PT(0);
  const rsrc_t XBr = make_rsrc(ws + OFF_XB + q4 * 512, 32 * XB_MS);
  const unsigned xoff = (tau >> 6) * XB_MS + ((((tau & 63) << 1) + col) ^ (((q4 >> 1) & 1) << 5)) * 4;
  unsigned xr[16];
  c2 v[16];
  float rs[16];
  u2 sq[16];
  u4 wt[4];
  {
    const rsrc_t Sr = make_rsrc(ws + OFF_RS0, 2048 * 12);
#pragma unroll
    for (int rq = 0; rq < 16; rq++) {
      const int r = (rq >> 2) + 4 * (rq & 3);
      if ((rq & 3) == 0) SCHED_FENCE();
      xr[r] = __builtin_amdgcn_raw_buffer_load_b32(XBr, xoff, r * (2 * XB_MS), 0);
      if (FIRST) rs[r] = __builtin_bit_cast(float, __builtin_amdgcn_raw_buffer_load_b32(Sr, tau * 4 + (r & 7) * 512, (r >> 3) * 4096, 0));
      else sq[r] = __builtin_amdgcn_raw_buffer_load_b64(Sr, tau * 8 + (r & 3) * 1024, 8192 + (r >> 2) * 4096, 0);
    }
    SCHED_FENCE();
    const rsrc_t Wr = make_rsrc(ws + OFF_WT + (size_t)q4 * 16384, 16384);
#pragma unroll
    for (int i = 0; i < 4; i++) wt[i] = __builtin_amdgcn_raw_buffer_load_b128(Wr, tid * 16, i * 4096, 0);
  }
  SCHED_FENCE();
  c2 tw1[15], tw2[15];
  tw_powers<15>(tw1, twiddle_rev((float)tau * (1.0f / 2048.0f)));
  tw_powers<15>(tw2, twiddle_rev((float)lo * (1.0f / 128.0f)));
  const bool special = (q4 == 0) && (col == 0);
  const unsigned sel_lo = special ? 0x07060c0cu : 0x05040c0cu, sel_hi = special ? 0x0c0c0c0cu : 0x07060c0cu;
  ldsp e1 = (ldsp)(uintptr_t)(unsigned)(uintptr_t)(__attribute__((address_space(3))) char*)smem + tid;
  ldsp e2 = (ldsp)(uintptr_t)(unsigned)(uintptr_t)(__attribute__((address_space(3))) char*)smem + (kc * P1 + lo) * 2 + col;
  ldsp e3 = (ldsp)(uintptr_t)(unsigned)(uintptr_t)(__attribute__((address_space(3))) char*)smem + (kc * P1 + lo * 9) * 2 + col;
  SCHED_FENCE();
  c2 xv[16];
  dft16s<-1>(v,
    [&](int i) {
#pragma unroll
      for (int j = 0; j < 4; j++) {
        const int r = i + 4 * j;
        if (!FIRST) {
          u2 t = sq[r];
          rs[r] = __builtin_amdgcn_rsqf((float)__builtin_bit_cast(double, t) * (2048.0f * 2048.0f / 640.0f) + LN_EPS * 2048.0f * 2048.0f);
        }
        xv[r] = c2{__builtin_bit_cast(float, __builtin_amdgcn_perm(xr[r], 0u, sel_lo)), __builtin_bit_cast(float, __builtin_amdgcn_perm(xr[r], 0u, sel_hi))};
        v[r] = xv[r] * rs[r];
      }
      if (i == 0) PT(1);
    },
    [&](int q) {
      if (q == 0) v[4] = cmul(v[4], tw1[3]); else twmulp<-1>(v[q], tw1[q - 1], v[q + 4], tw1[q + 3]);
      e1[q * (2 * P1)] = v[q]; e1[(q + 4) * (2 * P1)] = v[q + 4];
      twmulp<-1>(v[q + 8], tw1[q + 7], v[q + 12], tw1[q + 11]);
      e1[(q + 8) * (2 * P1)] = v[q + 8]; e1[(q + 12) * (2 * P1)] = v[q + 12];
    });
  __syncthreads();
#pragma unroll
  for (int i = 0; i < 4; i++)
#pragma unroll
    for (int j = 0; j < 4; j++) v[i + 4 * j] = e2[(i + 4 * j) * 16];
  SCHED_FENCE();
  c2 wf[16];
#pragma unroll
  for (int r = 0; r < 16; r++) { unsigned w = wt[r >> 2][r & 3]; wf[r] = c2{bf_lo(w), bf_hi(w)}; }
  SCHED_FENCE();
  dft16s<-1>(v, [&](int) {},
    [&](int q) {
      if (q == 0) v[4] = cmul(v[4], tw2[3]); else twmulp<-1>(v[q], tw2[q - 1], v[q + 4], tw2[q + 3]);
      e2[q * 18] = v[q]; e2[(q + 4) * 18] = v[q + 4];
      twmulp<-1>(v[q + 8], tw2[q + 7], v[q + 12], tw2[q + 11]);
      e2[(q + 8) * 18] = v[q + 8]; e2[(q + 12) * 18] = v[q + 12];
    });
#pragma unroll
  for (int bb = 0; bb < 2; bb++)
#pragma unroll
    for (int sa = 0; sa < 8; sa++) v[bb * 8 + sa] = e3[(bb * 72 + sa) * 2];
  SCHED_FENCE();
  PT(2);
#pragma unroll
  for (int bb = 0; bb < 2; bb++) {
    dft8<-1>(v + 8 * bb);
#pragma unroll
    for (int r = 0; r < 8; r += 2) twmulp<-1>(v[8 * bb + r], wf[8 * bb + r], v[8 * bb + r + 1], wf[8 * bb + r + 1]);
    SCHED_FENCE();
  }
  dft8<1>(v);
  SCHED_FENCE();
  {
    c2* u = v + 8;
    c2 a0 = u[0], a1 = u[2], a2 = u[4], a3 = u[6], o0 = u[1], o1 = u[3], o2 = u[5], o3 = u[7];
    e3[0] = v[0]; e3[2] = v[1];
    dft4<1>(a0, a1, a2, a3);
    SCHED_FENCE();
    e3[4] = v[2]; e3[6] = v[3];
    dft4<1>(o0, o1, o2, o3);
    SCHED_FENCE();
    e3[8] = v[4]; e3[10] = v[5];
    o1 = rot1<1>(o1); o3 = rot3<1>(o3);
    u[0] = a0 + o0; u[4] = a0 - o0;
    u[2] = add_rot<1>(a2, o2); u[6] = sub_rot<1>(a2, o2);
    SCHED_FENCE();
    e3[12] = v[6]; e3[14] = v[7];
    u[1] = a1 + o1; u[5] = a1 - o1;
    u[3] = a3 + o3; u[7] = a3 - o3;
    SCHED_FENCE();
#pragma unroll
    for (int sa = 0; sa < 8; sa++) e3[(72 + sa) * 2] = u[sa];
  }
#pragma unroll
  for (int i = 0; i < 4; i++)
#pragma unroll
    for (int j = 0; j < 4; j++) v[i + 4 * j] = e2[(i + 4 * j) * 18];
  SCHED_FENCE();
  dft16s<1>(v,
    [&](int i) {
      if (i == 0) v[4] = cmulc(v[4], tw2[3]); else twmulp<1>(v[i], tw2[i - 1], v[i + 4], tw2[i + 3]);
      twmulp<1>(v[i + 8], tw2[i + 7], v[i + 12], tw2[i + 11]);
    },
    [&](int q) {
#pragma unroll
      for (int j = 0; j < 4; j++) e2[(q + 4 * j) * 16] = v[q + 4 * j];
    });
  __syncthreads();
#pragma unroll
  for (int i = 0; i < 4; i++)
#pragma unroll
    for (int j = 0; j < 4; j++) v[i + 4 * j] = e1[(i + 4 * j) * (2 * P1)];
  SCHED_FENCE();
  const unsigned sel_o = special ? 0x05040100u : 0x07060504u;
  PT(3);
  dft16s<1>(v,
    [&](int i) {
      if (i == 0) v[4] = cmulc(v[4], tw1[3]); else twmulp<1>(v[i], tw1[i - 1], v[i + 4], tw1[i + 3]);
      twmulp<1>(v[i + 8], tw1[i + 7], v[i + 12], tw1[i + 11]);
    },
    [&](int q) {
#pragma unroll
      for (int j = 0; j < 4; j++) {
        const int r = q + 4 * j;
        c2 o = xv[r] + v[r];
        __builtin_amdgcn_raw_buffer_store_b32(__builtin_amdgcn_perm(pack_bf(o.x, o.y), xr[r], sel_o), XBr, xoff, r * (2 * XB_MS), AUX_CF);
      }
    });
  PT(7); PT_FLUSH(KID);
}

constexpr int G1_A_BYTES = 81920;
constexpr int G1_WSCR = SCR_F4 * 16;
#define G1_PF 4
#define G1_PFI 5
#define G1_VOL volatile
#define G1_LGKM 0xc87f
#define G1_LS 4
#define G1_NP1 42
#define G2_PF 3
#define G2_ND 12
#define G2_WS 2
#define G2_BD 4
template <int I> struct ic { static constexpr int value = I; };
template <int N, int I = 0, typename F> DEV void static_for(F f) { if constexpr (I < N) { f(ic<I>{}); static_for<N, I + 1>(f); } }
constexpr int LDS_G1 = G1_A_BYTES + 512;
constexpr int LDS_G1I = LDS_G1 + 4 * G1_WSCR;
DEV float dot2(unsigned a, float c) {
  bf2 q = __builtin_bit_cast(bf2, a);
  return __builtin_amdgcn_fdot2_f32_bf16(q, q, c, false);
}
template <bool WITH_INV>
__global__ __launch_bounds__(256, 1) void k_gemm1(char* __restrict__ ws) {
  extern __shared__ __attribute__((aligned(16))) char smem[];
  const int tid = threadIdx.x, lane = tid & 63;
  const int wv = __builtin_amdgcn_readfirstlane(tid >> 6);
  const int b = blockIdx.x;
  const int xcd = b & 7, slot = b >> 3;
  const int mblk = (xcd >> 1) * 8 + (slot & 7), fs = (xcd & 1) * 4 + (slot >> 3);
  constexpr int KID = WITH_INV ? 5 : 2;
  (void)KID;
  asm volatile("" ::: "a191");
  PT_DECL; PT(0);
  const char* Ag = ws + OFF_XB + (size_t)mblk * XB_MS;
  const unsigned lds0 = (unsigned)(uintptr_t)(__attribute__((address_space(3))) char*)smem + wv * 20480;
  const unsigned l16 = lane * 16;
#define G1_DMA(I) do { unsigned keep_; asm volatile( \
          "s_mov_b32 %0, m0\n\ts_mov_b32 m0, %3\n\ts_nop 0\n\t" \
          "global_load_lds_dwordx4 %1, %2\n\t" \
          "global_load_lds_dwordx4 %1, %2 offset:1024\n\t" \
          "global_load_lds_dwordx4 %1, %2 offset:2048\n\t" \
          "global_load_lds_dwordx4 %1, %2 offset:3072\n\t" \
          "s_mov_b32 m0, %0" \
          : "=&s"(keep_) : "v"(l16), "s"(Ag + wv * 20480 + (I) * 4096), "s"(lds0 + (I) * 4096) : "memory"); SCHED_FENCE(); } while (0)
  const rsrc_t Wg = make_rsrc(ws + OFF_W1P + (size_t)(fs * 12 + wv * 3) * W1_TS * 1024, 3 * W1_TS * 1024);
  constexpr int PF = WITH_INV ? G1_PFI : G1_PF;
  constexpr int NL = 3 * PF;
  bf8 wf[60];
#define G1_LOAD1(I) do { if ((I) < 60) wf[(I) < 60 ? (I) : 0] = bload(Wg, l16, ((I) / 20 * W1_TS + (I) % 20) * 1024); } while (0)
#define G1_LOADW(S) do { if ((S) < PF) { G1_LOAD1(3 * (S)); G1_LOAD1(3 * (S) + 1); G1_LOAD1(3 * (S) + 2); SCHED_FENCE(); } } while (0)
  unsigned pk3[2][8];
  const int m0 = fs * 8 + 2 * wv;
  const int iu = lane < 40 ? lane : 39;
  const int iq = iu >> 3, ia = iu & 7;
  if (!WITH_INV) {
    G1_DMA(0); G1_DMA(1); G1_DMA(2); G1_DMA(3); G1_DMA(4);
#pragma unroll
    for (int s = 0; s < PF; s++) G1_LOADW(s);
    PT(6);
  } else {
    f4* scr = (f4*)(smem + LDS_G1 + wv * G1_WSCR);
    u2* buf = (u2*)scr;
    u4 rw[3];
#pragma unroll
    for (int i = 0; i < 3; i++) {
      int q = lane + 64 * i;
      if (i == 2) q = q < 160 ? q : 159;
      unsigned vo = q * 512 + ((m0 * 8) ^ (((q >> 1) & 1) << 7));
      asm volatile("global_load_dwordx4 %0, %1, %2" : "=v"(rw[i]) : "v"(vo), "s"(Ag) : "memory");
    }
    SCHED_FENCE();
    G1_DMA(0); G1_DMA(1);
    const float sc = 0.5f * 17.88854381999832f / 320.0f;
    const float r2 = 1.41421356237309505f;
    Tw320 tw;
    c2 wu[5];
    {
      c2 u0 = twiddle_rev((float)lane * (1.0f / 640.0f));
      tw320_init(tw, lane, u0, sc);
      wu[0] = c2{u0.y, u0.x};
      wu[1] = cmul(wu[0], c2{0.80901699437494742f, 0.58778525229247313f});
      wu[2] = cmul(wu[0], c2{0.30901699437494742f, 0.95105651629515357f});
      wu[3] = cmul(wu[0], c2{-0.30901699437494742f, 0.95105651629515357f});
      wu[4] = cmul(wu[0], c2{-0.80901699437494742f, 0.58778525229247313f});
    }
    SCHED_FENCE();
    G1_DMA(2); G1_DMA(3); G1_DMA(4);
    PT(4);
    asm volatile("s_waitcnt vmcnt(20)" : "+v"(rw[0]), "+v"(rw[1]), "+v"(rw[2]) :: "memory");
    PT(5);
#pragma unroll
    for (int i = 0; i < 3; i++) {
      int q = lane + 64 * i;
      if (i == 2) q = q < 160 ? q : 159;
      *(u4*)&buf[2 * q] = u4{rw[i].x, rw[i].z, rw[i].y, rw[i].w};
    }
    lds_fence();
    c2 z[2][5], o[2][8];
    u2 ua[5], ub[5];
#pragma unroll
    for (int j = 0; j < 5; j++) {
      int k = lane + 64 * j;
      ua[j] = buf[k]; ub[j] = buf[(j == 0 && k == 0) ? 0 : 320 - k];
    }
    SCHED_FENCE();
    G1_LOADW(0); G1_LOADW(1);
#pragma unroll
    for (int j = 0; j < 5; j++) {
      c2 A0 = c2{bf_lo(ua[j].x), bf_hi(ua[j].x)}, B0 = c2{bf_lo(ub[j].x), bf_hi(ub[j].x)};
      c2 A1 = c2{bf_lo(ua[j].y), bf_hi(ua[j].y)}, B1 = c2{bf_lo(ub[j].y), bf_hi(ub[j].y)};
      if (j == 0 && lane == 0) {
        B0 = c2{A0.y * r2, 0.f}; A0 = c2{A0.x * r2, 0.f};
        B1 = c2{A1.y * r2, 0.f}; A1 = c2{A1.x * r2, 0.f};
      }
      tangle2(z[0][j], z[1][j], A0, B0, A1, B1, wu[j]);
    }
    fft320x2<1, 1>(z, tw, sc, scr, lane, o, [&](int h) {
      if (h == 0) { G1_LOADW(2); G1_LOADW(3); }
      if (h == 2) { G1_LOADW(4); G1_LOADW(5); }
    });
    PT(6);
#pragma unroll
    for (int rr = 0; rr < 2; rr++)
#pragma unroll
      for (int e = 0; e < 8; e++) pk3[rr][e] = pack_bf(o[rr][e].x, o[rr][e].y);
    SCHED_FENCE();
#pragma unroll
    for (int s = 6; s < PF; s++) G1_LOADW(s);
  }
  const rsrc_t Hr = make_rsrc(ws + OFF_H + (size_t)mblk * 4 * H_TS * 1024, 4 * H_TS * 1024);
  const int G = fs * 2 + (wv >> 1);
  int hfull[4], hhalf[4];
#pragma unroll
  for (int m = 0; m < 4; m++) {
    hfull[m] = (((m + wv) & 3) * H_TS + 3 * G + (wv & 1)) * 1024; hhalf[m] = (((m + wv) & 3) * H_TS + 3 * G + 2) * 1024;
    asm("" : "+s"(hfull[m]), "+s"(hhalf[m]));
  }
  SCHED_FENCE();
  asm volatile("s_waitcnt vmcnt(%0)\n\ts_barrier" :: "n"(3 * PF) : "memory");
  if (WITH_INV) {
    const rsrc_t Xr = make_rsrc(ws + OFF_X3 + (size_t)(mblk * 64 + m0) * 1280, 2560);
    const unsigned vo = (iq + 5 * ia) * 4;
#pragma unroll
    for (int rr = 0; rr < 2; rr++)
#pragma unroll
      for (int e = 0; e < 8; e++) __builtin_amdgcn_raw_buffer_store_b32(pk3[rr][e], Xr, vo + (rr * 320 + 40 * e) * 4, 0, AUX_X3);
    SCHED_FENCE();
  }
  PT(1);
  const int g = lane >> 4, r = lane & 15;
  const int swz = (g & 1) << 7;
  typedef __attribute__((address_space(3))) const char* ldsc;
  typedef __attribute__((address_space(3))) G1_VOL const u2* ldsu2;
  ldsc abase[4], abase2[4];
#pragma unroll
  for (int m = 0; m < 4; m++) {
    unsigned o = (2 * g) * 512 + (((16 * ((m + wv) & 3) + r) * 8) ^ swz), o2 = o + 65536;
    asm("" : "+v"(o2));
    abase[m] = (ldsc)smem + o; abase2[m] = (ldsc)smem + o2;
  }
  SCHED_FENCE();
  f4 acc[3][4];
#pragma unroll
  for (int t = 0; t < 3; t++)
#pragma unroll
    for (int m = 0; m < 4; m++) acc[t][m] = f4{0.f, 0.f, 0.f, 0.f};
  u4 af[20][4];
#define G1_READA(KS, M) do { ldsc ap_ = (KS) < 16 ? abase[M] + (KS) * 4096 : abase2[M] + ((KS) - 16) * 4096; \
    u2 lo_ = *(ldsu2)ap_, hi_ = *(ldsu2)(ap_ + 512); af[KS][M] = u4{lo_.x, lo_.y, hi_.x, hi_.y}; } while (0)
#define G1_MFMA(ACC, W, B) ACC = __builtin_amdgcn_mfma_f32_16x16x32_bf16(W, __builtin_bit_cast(bf8, B), ACC, 0, 0, 0)
  G1_READA(0, 0); G1_READA(1, 0); G1_READA(0, 1); G1_READA(0, 2); G1_READA(0, 3);
  G1_READA(2, 0); G1_READA(3, 0); G1_READA(1, 1); G1_READA(1, 2); G1_READA(1, 3);
  typedef __attribute__((address_space(3))) volatile float* ldsf;
  ldsf stw = (ldsf)((ldsc)smem + G1_A_BYTES) + (g == (r >> 2) ? wv * 16 + r : 64 + (lane & 31));
  ldsf str = (ldsf)((ldsc)smem + G1_A_BYTES) + r;
  float rl[4], irs[4], e0, e1;
  unsigned pk[3][4][2];
  SCHED_FENCE();
  f4 gram = f4{0.f, 0.f, 0.f, 0.f};
  constexpr int NP1 = G1_NP1;
  static_for<20>([&](auto kI) {
    constexpr int ks = kI.value;
    constexpr int l0 = NL + (NP1 - NL) * ks / 20, l1 = NL + (NP1 - NL) * (ks + 1) / 20;
    constexpr int infl = (ks == 0 ? 10 : ks <= 8 ? 10 : ks == 12 ? 7 : ks == 14 ? 10 : ks < 19 ? 6 : 0);
    __builtin_amdgcn_s_waitcnt(0xc07f | ((infl & 15) << 8));
    SCHED_FENCE();
    if (ks < 10) {
      u4 ga = af[2 * ks][0];
      if (ks == 0 && g == 0) ga.x &= 0xffff0000u;
      G1_MFMA(gram, __builtin_bit_cast(bf8, ga), af[2 * ks][0]);
      if (ks < 8) G1_READA(ks < 8 ? 2 * ks + 4 : 0, 0);
      SCHED_FENCE();
    }
    static_for<4>([&](auto mI) {
      constexpr int m = mI.value;
      G1_MFMA(acc[0][m], wf[ks], af[ks][m]);
      if (m == 0 && ks < 8) G1_READA(ks < 8 ? 2 * ks + 5 : 0, 0);
      if (m > 0 && ks + 2 < 20) G1_READA(ks + 2 < 20 ? ks + 2 : 0, m);
      if (m == 0 && l0 < l1) G1_LOAD1(l0);
      if (m == 2 && l0 + 1 < l1) G1_LOAD1(l0 + 1);
      if (m == 0 && ks == 11) {
        float sv = (r & 2) ? ((r & 1) ? gram.w : gram.z) : ((r & 1) ? gram.y : gram.x);
        *stw = sv * (1.0f / 640.0f) + LN_EPS;
      }
      if (m == 0 && ks == 13) {
        asm volatile("s_barrier" ::: "memory");
#pragma unroll
        for (int q = 0; q < 4; q++) irs[q] = str[((q + wv) & 3) * 16];
      }
      if (m == 0 && ks >= 15 && ks < 19) {
        constexpr int q = (ks - 15) & 3;
        float rs = __builtin_amdgcn_rsqf(irs[q]);
        asm("s_nop 0\n\tv_mul_f32 %0, %0, %2\n\tv_mul_f32 %1, 0xbfb8aa3b, %2" : "+v"(irs[q]), "=v"(rl[q]) : "v"(rs));
      }
      SCHED_FENCE();
      if (m == 1 && ks < 10) {
        G1_MFMA(gram, __builtin_bit_cast(bf8, af[2 * ks + 1][0]), af[2 * ks + 1][0]);
        SCHED_FENCE();
      }
    });
  });
  PT(2);
  const unsigned l16h = lane * 8 + (wv & 1) * 512;
  float a0, a1;
  auto silu_op = [&](auto cI, auto oI) {
    constexpr int c = cI.value, o = oI.value, t = c >> 3, m = (c >> 1) & 3, e = 2 * (c & 1);
    if (o == 0) { a0 = acc[t][m][e]; a1 = acc[t][m][e + 1]; asm("" : "+v"(a0), "+v"(a1)); }
    if (o == 1) asm("v_mul_f32 %0, %2, %4\n\tv_mul_f32 %1, %3, %4" : "=&v"(e0), "=&v"(e1) : "v"(a0), "v"(a1), "v"(rl[m]));
    if (o == 2) asm("v_exp_f32 %0, %0" : "+v"(e0));
    if (o == 3) asm("v_exp_f32 %0, %0" : "+v"(e1));
    if (o == 4) asm("v_fma_f32 %0, %0, %2, %2\n\tv_fma_f32 %1, %1, %2, %2" : "+v"(e0), "+v"(e1) : "v"(irs[m]));
    if (o == 5) asm("v_fma_f32 %0, %0, %2, %2\n\tv_fma_f32 %1, %1, %2, %2\n\tv_rcp_f32 %0, %0" : "+v"(e0), "+v"(e1) : "v"(irs[m]));
    if (o == 6) asm("v_rcp_f32 %0, %0" : "+v"(e0));
    if (o == 7) asm("v_rcp_f32 %0, %0" : "+v"(e1));
    if (o == 8) asm("v_rcp_f32 %1, %1\n\tv_mul_f32 %0, %0, %2" : "+v"(e0), "+v"(e1) : "v"(a0));
    if (o == 9) asm("v_mul_f32 %0, %0, %2\n\tv_mul_f32 %1, %1, %3" : "+v"(e0), "+v"(e1) : "v"(a0), "v"(a1));
    if (o == 10) asm("v_cvt_pk_bf16_f32 %0, %1, %2" : "=v"(pk[t][m][c & 1]) : "v"(e0), "v"(e1));
    if (o == 11) asm("v_mul_f32 %2, %2, %3\n\tv_cvt_pk_bf16_f32 %0, %1, %2" : "=v"(pk[t][m][c & 1]), "+v"(e0), "+v"(e1) : "v"(a1));
    if (o == 10 || o == 11) {
      if (t == 1 && (c & 1))
        __builtin_amdgcn_raw_buffer_store_b128(u4{pk[0][m][0], pk[0][m][1], pk[1][m][0], pk[1][m][1]}, Hr, l16, hfull[m], ST_AUX);
      if (t == 2 && (c & 1))
        __builtin_amdgcn_raw_buffer_store_b64(u2{pk[2][m][0], pk[2][m][1]}, Hr, l16h, hhalf[m], ST_AUX);
    }
    SCHED_FENCE();
  };
#define SOP(C, O) silu_op(ic<(C)>{}, ic<(O)>{})
  static_for<160>([&](auto jI) {
    constexpr int j = jI.value;
    constexpr int t = j < 80 ? 1 : 2;
    constexpr int ks = j < 40 ? j >> 2 : j < 80 ? 10 + ((j - 40) % 20) / 2 : ((j - 80) % 40) >> 1;
    constexpr int m = j < 40 ? j & 3 : j < 80 ? ((j - 40) / 20) * 2 + (j & 1) : ((j - 80) / 40) * 2 + (j & 1);
    G1_MFMA(acc[t][m], wf[20 * t + ks], af[ks][m]);
    SCHED_FENCE();
    constexpr int c = j >> 3, o = j & 7;
    if (j == 0) SOP(0, 0);
    if (o == 0) SOP(c, 1);
    if (o == 1) SOP(c, 2);
    if (o == 2) SOP(c, 3);
    if (o == 3) SOP(c, 4);
    if (o == 4) SOP(c, 6);
    if (o == 5) SOP(c, 7);
    if (o == 6) SOP(c, 9);
    if (o == 7) { SOP(c, 10); if (c < 19) SOP(c + 1, 0); }
    if (j % G1_LS == 3) G1_LOAD1(NP1 + j / G1_LS);
    if (j == 80) PT(3);
    SCHED_FENCE();
  });
  PT(4 + (WITH_INV ? 3 : 0));
  {
    float x[8];
#pragma unroll
    for (int i = 0; i < 8; i++) x[i] = acc[2][2 + (i >> 2)][i & 3] * rl[2 + (i >> 2)];
#pragma unroll
    for (int i = 0; i < 8; i++) x[i] = __builtin_amdgcn_exp2f(x[i]);
#pragma unroll
    for (int i = 0; i < 8; i++) x[i] = __builtin_amdgcn_rcpf(__builtin_fmaf(x[i], irs[2 + (i >> 2)], irs[2 + (i >> 2)])) * acc[2][2 + (i >> 2)][i & 3];
#pragma unroll
    for (int m = 2; m < 4; m++)
      __builtin_amdgcn_raw_buffer_store_b64(u2{pack_bf(x[4 * m - 8], x[4 * m - 7]), pack_bf(x[4 * m - 6], x[4 * m - 5])}, Hr, l16h, hhalf[m], ST_AUX);
  }
  PT(7); PT_FLUSH(KID);
}

template <bool FINAL>
__global__ __launch_bounds__(256, 1) void k_gemm2(char* __restrict__ ws, __hip_bfloat16* __restrict__ out) {
  __shared__ __attribute__((aligned(16))) f4 red[4 * 3 * 5 * 64];
  const int tid = threadIdx.x, lane = tid & 63;
  const int wv = __builtin_amdgcn_readfirstlane(tid >> 6);
  const int b = blockIdx.x;
  const int xcd = b & 7, slot = b >> 3;
  const int mblk = (xcd >> 1) * 8 + (slot & 7), cs = (xcd & 1) * 4 + (slot >> 3);
  const int g = lane >> 4, r = lane & 15;
  constexpr int KID = FINAL ? 6 : 3;
  (void)KID;
  asm volatile("" ::: "a191");
  PT_DECL; PT(0);
  const unsigned l16 = lane * 16;
  const rsrc_t Hr = make_rsrc(ws + OFF_H + ((size_t)mblk * 4 * H_TS + wv * 12) * 1024, (4 * H_TS - wv * 12) * 1024);
  const rsrc_t Wr = make_rsrc(ws + (FINAL ? OFF_W2C : OFF_W2P) + ((size_t)cs * 5 * H_TS + wv * 12) * 1024, (5 * H_TS - wv * 12) * 1024);
  int hoff[4];
#pragma unroll
  for (int m = 0; m < 4; m++) hoff[m] = ((m + wv) & 3) * H_TS * 1024;
  constexpr int PF = G2_PF, ND = G2_ND, S0 = 12 - ND;
  bf8 hf[12][4], wf[12][5];
  const unsigned l8 = lane * 8;
#define G2_LH(S, M) do { if ((S) % 3 == 2) { \
      u2 lo_ = __builtin_amdgcn_raw_buffer_load_b64(Hr, l8 + ((S) & 3) * 1024, hoff[M] + ((S) >> 2) * 4096, 0); \
      u2 hi_ = __builtin_amdgcn_raw_buffer_load_b64(Hr, l8 + ((S) & 3) * 1024 + 512, hoff[M] + ((S) >> 2) * 4096, 0); \
      hf[S][M] = __builtin_bit_cast(bf8, u4{lo_.x, lo_.y, hi_.x, hi_.y}); \
    } else hf[S][M] = __builtin_bit_cast(bf8, __builtin_amdgcn_raw_buffer_load_b128(Hr, l16 + ((S) & 3) * 1024, hoff[M] + ((S) >> 2) * 4096, 0)); } while (0)
#define G2_LW(S, C) wf[S][C] = bload(Wr, l16, ((C) * H_TS + (S)) * 1024)
#define G2_LOAD(S, J) do { constexpr int s_ = (S), j_ = (J); \
    if (s_ < 12) { constexpr int q_ = s_ < 12 ? s_ : 0; \
      if (j_ == 0) { G2_LH(q_, 1); G2_LW(q_, 0); } if (j_ == 1) { G2_LH(q_, 2); G2_LW(q_, 1); } if (j_ == 2) { G2_LH(q_, 3); G2_LW(q_, 2); } \
      if (j_ == 3) { G2_LW(q_, 3); if (q_ < S0) G2_LH(q_, 0); } if (j_ == 4) G2_LW(q_, 4); } \
    else { constexpr int per_ = (ND + PF - 1) / PF, i_ = (s_ - 12) * per_ + j_; \
      if (j_ < per_ && i_ < ND) G2_LH(S0 + (i_ < ND ? i_ : 0), 0); \
      if (j_ == 4) { _Pragma("unroll") for (int e_ = 5; e_ < per_; e_++) if ((s_ - 12) * per_ + e_ < ND) G2_LH(S0 + ((s_ - 12) * per_ + e_) % ND, 0); } } \
    } while (0)
  const int mrow = mblk * 64 + wv * 16 + r;
  const int gq = 2 * (g & 1) + (g >> 1);
  const unsigned xo = gq * 512 + (((wv * 16 + r) * 8) ^ ((gq >> 1) << 7));
  u2 res[5];
  {
    const rsrc_t Rr = FINAL ? make_rsrc(ws + OFF_X3, 2048 * 1280) : make_rsrc(ws + OFF_XB + (size_t)mblk * XB_MS, XB_MS);
    const unsigned ro = FINAL ? (mrow * 640 + 4 * g) * 2 : xo;
    static_for<PF>([&](auto sI) {
      constexpr int s = sI.value;
      G2_LOAD(s, 0); G2_LOAD(s, 1); G2_LOAD(s, 2); G2_LOAD(s, 3); G2_LOAD(s, 4);
      SCHED_FENCE();
      if (s == 0) {
#pragma unroll
        for (int c = 0; c < 5; c++)
          res[c] = FINAL ? __builtin_amdgcn_raw_buffer_load_b64(Rr, ro + c * 32, cs * 160, 0)
                         : __builtin_amdgcn_raw_buffer_load_b64(Rr, ro + (c & 1) * 2048, cs * 10240 + (c >> 1) * 4096, 0);
        SCHED_FENCE();
      }
    });
  }
  PT(6);
  f4 acc[5][4];
#pragma unroll
  for (int c = 0; c < 5; c++)
#pragma unroll
    for (int m = 0; m < 4; m++) acc[c][m] = f4{0.f, 0.f, 0.f, 0.f};
#define G2_MFMA(S, C, M) acc[C][M] = __builtin_amdgcn_mfma_f32_16x16x32_bf16(wf[S][C], hf[S][M], acc[C][M], 0, 0, 0)
  f4* rw[3];
#pragma unroll
  for (int m = 1; m < 4; m++) rw[m - 1] = red + (((m + wv) & 3) * 3 + m - 1) * 5 * 64 + lane;
#define G2_WRITE(I) do { if ((I) >= 0 && (I) < 15) rw[(I) / 5][((I) % 5) * 64] = acc[(I) % 5][1 + (I) / 5]; } while (0)
  static_for<11>([&](auto sI) {
    constexpr int s = sI.value;
    static_for<5>([&](auto cI) {
      constexpr int c = cI.value;
      G2_MFMA(s, c, 1);
      if (s == 0 && c == 0) PT(2);
      G2_MFMA(s, c, 2); G2_MFMA(s, c, 3);
      if (s < S0) G2_MFMA(s, c, 0);
      G2_LOAD(s + PF, c);
      SCHED_FENCE();
    });
    if (s == 0) PT(1);
  });
  constexpr int NM = 15 + (S0 == 12 ? 1 : ND) * 5, W0 = 7, NB = W0 + 15 * G2_WS + G2_BD;
  f4 part[15];
  static_for<NM>([&](auto iI) {
    constexpr int i = iI.value;
    if (i < 15) G2_MFMA(11, i % 5, 1 + i / 5);
    else G2_MFMA((S0 == 12 ? 11 : S0) + (i - 15) / 5, i % 5, 0);
    if (i < 5) G2_LOAD(11 + PF, i);
    if (i >= W0 && (i - W0) % G2_WS == 0) G2_WRITE((i - W0) / G2_WS);
    if (i == 15) PT(3);
    if (i == (NB < NM ? NB : NM - 1)) {
#pragma unroll
      for (int k = (NM - 1 - W0) / G2_WS + 1; k < 15; k++) G2_WRITE(k);
      asm volatile("s_waitcnt lgkmcnt(0)\n\ts_barrier" ::: "memory");
      PT(4);
#pragma unroll
      for (int k = 0; k < 15; k++) part[k] = red[(wv * 15 + k) * 64 + lane];
    }
    SCHED_FENCE();
  });
  PT(5);
  f4 own[5];
#pragma unroll
  for (int c = 0; c < 5; c++) own[c] = (part[c] + part[5 + c]) + (part[10 + c] + acc[c][0]);
  float s = 0.f;
#pragma unroll
  for (int c = 0; c < 5; c++) {
    if (!FINAL) {
      const u4 ob = __builtin_bit_cast(u4, own[c]);
      u2 s0 = __builtin_amdgcn_permlane32_swap(ob.x, ob.z, false, false);
      u2 s1 = __builtin_amdgcn_permlane32_swap(ob.y, ob.w, false, false);
      own[c] = __builtin_bit_cast(f4, u4{s0.x, s0.y, s1.x, s1.y});
    }
    f4 o = f4{bf_lo(res[c].x), bf_hi(res[c].x), bf_lo(res[c].y), bf_hi(res[c].y)} + own[c];
    u2 pk = u2{pack_bf(o.x, o.y), pack_bf(o.z, o.w)};
    if (FINAL) {
      __builtin_amdgcn_raw_buffer_store_b64(pk, make_rsrc(out, 2048 * 1280), (mrow * 640 + 4 * g) * 2 + c * 32, cs * 160, AUX_G2);
    } else {
      __builtin_amdgcn_raw_buffer_store_b64(pk, make_rsrc(ws + OFF_XB + (size_t)mblk * XB_MS, XB_MS), xo + (c & 1) * 2048, cs * 10240 + (c >> 1) * 4096, AUX_G2);
      unsigned p0 = (cs == 0 && c == 0 && g == 0) ? (pk.x & 0xffff0000u) : pk.x;
      s = dot2(p0, s);
      s = dot2(pk.y, s);
    }
  }
  if (!FINAL) {
    s += __shfl_xor(s, 16);
    s += __shfl_xor(s, 32);
    if (g == 0) unsafeAtomicAdd((double*)(ws + OFF_SSQ) + mrow, (double)s);
  }
  if (FINAL) asm volatile("k6_code_end:");
  PT(7); PT_FLUSH(KID);
}

extern "C" void kernel_launch(void* const* d_in, const int* in_sizes, int n_in, void* d_out, int out_size, void* d_ws,
                              size_t ws_size, hipStream_t stream) {
  const uint16_t* x = (const uint16_t*)d_in[0];
  const uint16_t* wr = (const uint16_t*)d_in[1];
  const uint16_t* wi = (const uint16_t*)d_in[2];
  const uint16_t* w1 = (const uint16_t*)d_in[4];
  const uint16_t* w2 = (const uint16_t*)d_in[5];
  char* ws = (char*)d_ws;
  __hip_bfloat16* out = (__hip_bfloat16*)d_out;
  (void)hipFuncSetAttribute((const void*)k_colfilter<true>, hipFuncAttributeMaxDynamicSharedMemorySize, LDS_CF);
  (void)hipFuncSetAttribute((const void*)k_colfilter<false>, hipFuncAttributeMaxDynamicSharedMemorySize, LDS_CF);
  (void)hipFuncSetAttribute((const void*)k_gemm1<false>, hipFuncAttributeMaxDynamicSharedMemorySize, LDS_G1);
  (void)hipFuncSetAttribute((const void*)k_gemm1<true>, hipFuncAttributeMaxDynamicSharedMemorySize, LDS_G1I);
#define NK 7
  k_prep<<<K0_X + K0_W1 + K0_WT, 512, 0, stream>>>(x, wr, wi, w1, w2, (const char*)d_out, ws);
  if (NK > 1) k_colfilter<true><<<256, 256, LDS_CF, stream>>>(w2, ws);
  if (NK > 2) k_gemm1<false><<<256, 256, LDS_G1, stream>>>(ws);
  if (NK > 3) k_gemm2<false><<<256, 256, 0, stream>>>(ws, out);
  if (NK > 4) k_colfilter<false><<<256, 256, LDS_CF, stream>>>(w2, ws);
  if (NK > 5) k_gemm1<true><<<256, 256, LDS_G1I, stream>>>(ws);
  if (NK > 6) k_gemm2<true><<<256, 256, 0, stream>>>(ws, out);
}
```

```cpp
#pragma clang diagnostic ignored "-Warray-bounds"
#include <hip/hip_runtime.h>
#include <hip/hip_bf16.h>
#include <stdint.h>

typedef float c2 __attribute__((ext_vector_type(2)));
typedef float f4 __attribute__((ext_vector_type(4)));
typedef unsigned u2 __attribute__((ext_vector_type(2)));
typedef unsigned u4 __attribute__((ext_vector_type(4)));
typedef __bf16 bf2 __attribute__((ext_vector_type(2)));
typedef __bf16 bf8 __attribute__((ext_vector_type(8)));

#define DEV __device__ __forceinline__
#define SCHED_FENCE() __builtin_amdgcn_sched_barrier(0)

constexpr int S_ = 2048, D_ = 640, F_ = 1536, DF_ = 321;
constexpr float LN_EPS = 1e-5f;

constexpr int XB_MS = 81920 + 512;
constexpr int H_TS = 49;
constexpr int W1_TS = 21;
constexpr size_t OFF_XB = 0;
constexpr size_t OFF_H = OFF_XB + 32ull * XB_MS;
constexpr size_t OFF_W1P = OFF_H + 128ull * H_TS * 1024;
constexpr size_t OFF_W2P = OFF_W1P + 96ull * W1_TS * 1024;
constexpr size_t OFF_W2C = OFF_W2P + 40ull * H_TS * 1024;
constexpr size_t OFF_WT = OFF_W2C + 40ull * H_TS * 1024;
constexpr size_t OFF_X3 = OFF_WT + 160ull * 4 * 256 * 16;
constexpr size_t OFF_RS0 = OFF_X3 + 2048ull * 640 * 2;
constexpr size_t OFF_SSQ = OFF_RS0 + 2048 * 4;
constexpr size_t OFF_FLG = OFF_SSQ + 2048 * 8;
constexpr size_t OFF_XB2 = OFF_FLG + 2 * 32 * 128;

#define TSTAMP(K, P) do {} while (0)
#define PT_DECL do {} while (0)
#define PT(i) do {} while (0)
#define PT_FLUSH(K) do {} while (0)
#define ST_AUX 16
#define AUX_X3 16
#define AUX_G2 16
#define AUX_K0 16
#define AUX_W1 16
#define AUX_CF 16
typedef __amdgpu_buffer_rsrc_t rsrc_t;
DEV rsrc_t make_rsrc(const void* p, unsigned bytes) { return __builtin_amdgcn_make_buffer_rsrc((void*)p, 0, bytes, 0x00020000); }
DEV bf8 bload(rsrc_t rs, unsigned voff, int OFF) {
  return __builtin_bit_cast(bf8, __builtin_amdgcn_raw_buffer_load_b128(rs, voff + (OFF & 4095), OFF & ~4095, 0));
}
DEV c2 add_mi(c2 a, c2 b) {
  c2 d; asm("v_pk_add_f32 %0, %1, %2 op_sel:[0,1] op_sel_hi:[1,0] neg_hi:[0,1]" : "=v"(d) : "v"(a), "v"(b)); return d;
}
DEV c2 add_pi(c2 a, c2 b) {
  c2 d; asm("v_pk_add_f32 %0, %1, %2 op_sel:[0,1] op_sel_hi:[1,0] neg_lo:[0,1]" : "=v"(d) : "v"(a), "v"(b)); return d;
}
DEV c2 cmul(c2 a, c2 w) {
  c2 t, d;
  asm("v_pk_mul_f32 %0, %1, %2 op_sel:[0,0] op_sel_hi:[0,1]" : "=v"(t) : "v"(a), "v"(w));
  asm("v_pk_fma_f32 %0, %1, %2, %3 op_sel:[1,1,0] op_sel_hi:[1,0,1] neg_lo:[0,1,0]" : "=v"(d) : "v"(a), "v"(w), "v"(t));
  return d;
}
DEV c2 cmulc(c2 a, c2 w) {
  c2 t, d;
  asm("v_pk_mul_f32 %0, %1, %2 op_sel:[0,0] op_sel_hi:[0,1] neg_hi:[0,1]" : "=v"(t) : "v"(a), "v"(w));
  asm("v_pk_fma_f32 %0, %1, %2, %3 op_sel:[1,1,0] op_sel_hi:[1,0,1]" : "=v"(d) : "v"(a), "v"(w), "v"(t));
  return d;
}
template <int SGN> DEV c2 twmul(c2 a, c2 w) { return SGN < 0 ? cmul(a, w) : cmulc(a, w); }
template <int SGN> DEV c2 add_rot(c2 a, c2 b) { return SGN < 0 ? add_mi(a, b) : add_pi(a, b); }
template <int SGN> DEV c2 sub_rot(c2 a, c2 b) { return SGN < 0 ? add_pi(a, b) : add_mi(a, b); }
DEV c2 twiddle_rev(float rev) {
  c2 t = c2{__builtin_amdgcn_cosf(rev), -__builtin_amdgcn_sinf(rev)};
  asm("s_nop 1" : "+v"(t));
  return t;
}
template <int N> DEV void tw_powers(c2* t, c2 w) {
  t[0] = w;
#pragma unroll
  for (int k = 2; k <= N; k++) {
    if ((k & 1) == 0) t[k - 1] = cmul(t[k / 2 - 1], t[k / 2 - 1]);
    else {
      int hi = k >= 8 ? 8 : (k >= 4 ? 4 : 2);
      t[k - 1] = cmul(t[hi - 1], t[k - hi - 1]);
    }
  }
}

template <int SGN> DEV void dft4(c2& a, c2& b, c2& c, c2& d) {
  c2 s0 = a + c, s1 = a - c, s2 = b + d, s3 = b - d;
  a = s0 + s2; c = s0 - s2;
  b = add_rot<SGN>(s1, s3); d = sub_rot<SGN>(s1, s3);
}
template <int SGN> DEV c2 rot1(c2 a) { return add_rot<SGN>(a, a) * 0.70710678118654752f; }
template <int SGN> DEV c2 rot3(c2 a) { return sub_rot<SGN>(a, a) * -0.70710678118654752f; }

template <int SGN> DEV void dft8(c2* v) {
  c2 e0 = v[0], e1 = v[2], e2 = v[4], e3 = v[6], o0 = v[1], o1 = v[3], o2 = v[5], o3 = v[7];
  dft4<SGN>(e0, e1, e2, e3);
  dft4<SGN>(o0, o1, o2, o3);
  o1 = rot1<SGN>(o1); o3 = rot3<SGN>(o3);
  v[0] = e0 + o0; v[4] = e0 - o0;
  v[1] = e1 + o1; v[5] = e1 - o1;
  v[2] = add_rot<SGN>(e2, o2); v[6] = sub_rot<SGN>(e2, o2);
  v[3] = e3 + o3; v[7] = e3 - o3;
}
template <int SGN> DEV void dft16(c2* v) {
  const c2 w1 = c2{0.92387953251128674f, -0.38268343236508977f};
  const c2 w3 = c2{0.38268343236508977f, -0.92387953251128674f};
  c2 y[4][4];
#pragma unroll
  for (int i = 0; i < 4; i++) {
    c2 a = v[i], b = v[i + 4], c = v[i + 8], d = v[i + 12];
    dft4<SGN>(a, b, c, d);
    y[i][0] = a; y[i][1] = b; y[i][2] = c; y[i][3] = d;
  }
  y[1][1] = twmul<SGN>(y[1][1], w1);
  y[1][2] = rot1<SGN>(y[1][2]);
  y[1][3] = twmul<SGN>(y[1][3], w3);
  y[2][1] = rot1<SGN>(y[2][1]);
  y[2][3] = rot3<SGN>(y[2][3]);
  y[3][1] = twmul<SGN>(y[3][1], w3);
  y[3][2] = rot3<SGN>(y[3][2]);
  y[3][3] = -twmul<SGN>(y[3][3], w1);
#pragma unroll
  for (int q = 0; q < 4; q++) {
    c2 a = y[0][q], b = y[1][q], c = y[2][q], d = y[3][q];
    if (q == 2) {
      c2 s0 = add_rot<SGN>(a, c), s1 = sub_rot<SGN>(a, c), s2 = b + d, s3 = b - d;
      a = s0 + s2; c = s0 - s2;
      b = add_rot<SGN>(s1, s3); d = sub_rot<SGN>(s1, s3);
    } else {
      dft4<SGN>(a, b, c, d);
    }
    v[q] = a; v[q + 4] = b; v[q + 8] = c; v[q + 12] = d;
  }
}
template <int SGN> DEV void twmulp(c2& a0, c2 w0, c2& a1, c2 w1) {
  c2 d0, d1;
  if (SGN < 0)
    asm("v_pk_mul_f32 %0, %2, %4 op_sel:[0,0] op_sel_hi:[0,1]\n\t"
        "v_pk_mul_f32 %1, %3, %5 op_sel:[0,0] op_sel_hi:[0,1]\n\t"
        "v_pk_fma_f32 %0, %2, %4, %0 op_sel:[1,1,0] op_sel_hi:[1,0,1] neg_lo:[0,1,0]\n\t"
        "v_pk_fma_f32 %1, %3, %5, %1 op_sel:[1,1,0] op_sel_hi:[1,0,1] neg_lo:[0,1,0]"
        : "=&v"(d0), "=&v"(d1) : "v"(a0), "v"(a1), "v"(w0), "v"(w1));
  else
    asm("v_pk_mul_f32 %0, %2, %4 op_sel:[0,0] op_sel_hi:[0,1] neg_hi:[0,1]\n\t"
        "v_pk_mul_f32 %1, %3, %5 op_sel:[0,0] op_sel_hi:[0,1] neg_hi:[0,1]\n\t"
        "v_pk_fma_f32 %0, %2, %4, %0 op_sel:[1,1,0] op_sel_hi:[1,0,1]\n\t"
        "v_pk_fma_f32 %1, %3, %5, %1 op_sel:[1,1,0] op_sel_hi:[1,0,1]"
        : "=&v"(d0), "=&v"(d1) : "v"(a0), "v"(a1), "v"(w0), "v"(w1));
  a0 = d0; a1 = d1;
}
template <int SGN, typename PRE, typename POST> DEV void dft16s(c2* v, PRE pre, POST post) {
  const c2 w1 = c2{0.92387953251128674f, -0.38268343236508977f};
  const c2 w3 = c2{0.38268343236508977f, -0.92387953251128674f};
  const c2 w9 = c2{-0.92387953251128674f, 0.38268343236508977f};
  c2 y[4][4];
#pragma unroll
  for (int i = 0; i < 4; i++) {
    pre(i);
    c2 a = v[i], b = v[i + 4], c = v[i + 8], d = v[i + 12];
    dft4<SGN>(a, b, c, d);
    y[i][0] = a; y[i][1] = b; y[i][2] = c; y[i][3] = d;
    if (i == 1) { twmulp<SGN>(y[1][1], w1, y[1][3], w3); y[1][2] = rot1<SGN>(y[1][2]); }
    if (i == 2) { y[2][1] = rot1<SGN>(y[2][1]); y[2][3] = rot3<SGN>(y[2][3]); }
    if (i == 3) { twmulp<SGN>(y[3][1], w3, y[3][3], w9); y[3][2] = rot3<SGN>(y[3][2]); }
    SCHED_FENCE();
  }
#pragma unroll
  for (int q = 0; q < 4; q++) {
    c2 a = y[0][q], b = y[1][q], c = y[2][q], d = y[3][q];
    if (q == 2) {
      c2 s0 = add_rot<SGN>(a, c), s1 = sub_rot<SGN>(a, c), s2 = b + d, s3 = b - d;
      a = s0 + s2; c = s0 - s2;
      b = add_rot<SGN>(s1, s3); d = sub_rot<SGN>(s1, s3);
    } else {
      dft4<SGN>(a, b, c, d);
    }
    v[q] = a; v[q + 4] = b; v[q + 8] = c; v[q + 12] = d;
    post(q);
    SCHED_FENCE();
  }
}
template <int SGN> DEV void dft5(c2* x) {
  const float c1 = 0.30901699437494742f, c2_ = -0.80901699437494742f;
  const float s1 = 0.95105651629515357f, s2 = 0.58778525229247313f;
  c2 t1 = x[1] + x[4], t2 = x[2] + x[3], t3 = x[1] - x[4], t4 = x[2] - x[3];
  c2 a1 = x[0] + c1 * t1 + c2_ * t2, a2 = x[0] + c2_ * t1 + c1 * t2;
  c2 b1 = s1 * t3 + s2 * t4, b2 = s2 * t3 - s1 * t4;
  x[0] = x[0] + t1 + t2;
  x[1] = add_rot<SGN>(a1, b1); x[4] = sub_rot<SGN>(a1, b1);
  x[2] = add_rot<SGN>(a2, b2); x[3] = sub_rot<SGN>(a2, b2);
}

DEV void lds_fence() { asm volatile("" ::: "memory"); __builtin_amdgcn_wave_barrier(); asm volatile("" ::: "memory"); }
DEV float bf_lo(unsigned u) { return __builtin_bit_cast(float, u << 16); }
DEV float bf_hi(unsigned u) { return __builtin_bit_cast(float, u & 0xffff0000u); }
DEV unsigned pack_bf(float a, float b) { return __builtin_bit_cast(unsigned, __builtin_convertvector(c2{a, b}, bf2)); }

template <int SGN> DEV void twmul2(c2& a0, c2& a1, c2 w) {
  c2 d0, d1;
  if (SGN < 0)
    asm("v_pk_mul_f32 %0, %2, %4 op_sel:[0,0] op_sel_hi:[0,1]\n\t"
        "v_pk_mul_f32 %1, %3, %4 op_sel:[0,0] op_sel_hi:[0,1]\n\t"
        "v_pk_fma_f32 %0, %2, %4, %0 op_sel:[1,1,0] op_sel_hi:[1,0,1] neg_lo:[0,1,0]\n\t"
        "v_pk_fma_f32 %1, %3, %4, %1 op_sel:[1,1,0] op_sel_hi:[1,0,1] neg_lo:[0,1,0]"
        : "=&v"(d0), "=&v"(d1) : "v"(a0), "v"(a1), "v"(w));
  else
    asm("v_pk_mul_f32 %0, %2, %4 op_sel:[0,0] op_sel_hi:[0,1] neg_hi:[0,1]\n\t"
        "v_pk_mul_f32 %1, %3, %4 op_sel:[0,0] op_sel_hi:[0,1] neg_hi:[0,1]\n\t"
        "v_pk_fma_f32 %0, %2, %4, %0 op_sel:[1,1,0] op_sel_hi:[1,0,1]\n\t"
        "v_pk_fma_f32 %1, %3, %4, %1 op_sel:[1,1,0] op_sel_hi:[1,0,1]"
        : "=&v"(d0), "=&v"(d1) : "v"(a0), "v"(a1), "v"(w));
  a0 = d0; a1 = d1;
}
DEV void tangle2(c2& d0, c2& d1, c2 A0, c2 B0, c2 A1, c2 B1, c2 w) {
  c2 e0, e1, o0, o1;
  asm("v_pk_add_f32 %0, %4, %5 neg_hi:[0,1]\n\t"
      "v_pk_add_f32 %1, %6, %7 neg_hi:[0,1]\n\t"
      "v_pk_add_f32 %2, %4, %5 neg_lo:[0,1]\n\t"
      "v_pk_add_f32 %3, %6, %7 neg_lo:[0,1]\n\t"
      "v_pk_fma_f32 %0, %2, %8, %0 op_sel:[0,0,0] op_sel_hi:[0,1,1]\n\t"
      "v_pk_fma_f32 %1, %3, %8, %1 op_sel:[0,0,0] op_sel_hi:[0,1,1]\n\t"
      "v_pk_fma_f32 %0, %2, %8, %0 op_sel:[1,1,0] op_sel_hi:[1,0,1] neg_lo:[0,1,0]\n\t"
      "v_pk_fma_f32 %1, %3, %8, %1 op_sel:[1,1,0] op_sel_hi:[1,0,1] neg_lo:[0,1,0]"
      : "=&v"(e0), "=&v"(e1), "=&v"(o0), "=&v"(o1) : "v"(A0), "v"(B0), "v"(A1), "v"(B1), "v"(w));
  d0 = e0; d1 = e1;
}

constexpr int SCR_F4 = 361;
struct Tw320 { c2 a[4]; c2 b[7]; };
DEV void tw320_init(Tw320& tw, int lane, c2 u0, float scale) {
  tw_powers<4>(tw.a, cmul(u0, u0));
  tw_powers<7>(tw.b, twiddle_rev((float)(lane & 7) * (1.0f / 64.0f)));
#pragma unroll
  for (int i = 0; i < 7; i++) tw.b[i] = tw.b[i] * scale;
}
#define FFT_HOOK_NONE(i)
template <int SGN, int NP, typename HOOK> DEV void fft320x2(c2 (*z)[5], const Tw320& tw, float scale, f4* scr, int lane, c2 (*o)[8], HOOK hook) {
  const int u = lane < 40 ? lane : 39;
  const int q = u >> 3, a = u & 7;
#pragma unroll
  for (int p = 0; p < NP; p++) {
    dft5<SGN>(z[2 * p]); dft5<SGN>(z[2 * p + 1]);
#pragma unroll
    for (int i = 1; i < 5; i++) twmul2<SGN>(z[2 * p][i], z[2 * p + 1][i], tw.a[i - 1]);
    lds_fence();
#pragma unroll
    for (int i = 0; i < 5; i++) scr[p * SCR_F4 + i * 72 + lane] = f4{z[2 * p][i].x, z[2 * p][i].y, z[2 * p + 1][i].x, z[2 * p + 1][i].y};
    lds_fence();
#pragma unroll
    for (int b = 0; b < 8; b++) { f4 t = scr[p * SCR_F4 + q * 72 + a + 8 * b]; o[2 * p][b] = t.xy; o[2 * p + 1][b] = t.zw; }
    SCHED_FENCE();
  }
  hook(0);
#pragma unroll
  for (int p = 0; p < NP; p++) {
    dft8<SGN>(o[2 * p]); dft8<SGN>(o[2 * p + 1]);
    hook(1);
    o[2 * p][0] = o[2 * p][0] * scale; o[2 * p + 1][0] = o[2 * p + 1][0] * scale;
#pragma unroll
    for (int c = 1; c < 8; c++) twmul2<SGN>(o[2 * p][c], o[2 * p + 1][c], tw.b[c - 1]);
    lds_fence();
#pragma unroll
    for (int c = 0; c < 8; c++) scr[p * SCR_F4 + (q * 8 + c) * 9 + a] = f4{o[2 * p][c].x, o[2 * p][c].y, o[2 * p + 1][c].x, o[2 * p + 1][c].y};
    lds_fence();
#pragma unroll
    for (int i = 0; i < 8; i++) { f4 t = scr[p * SCR_F4 + u * 9 + i]; o[2 * p][i] = t.xy; o[2 * p + 1][i] = t.zw; }
    SCHED_FENCE();
  }
  hook(2);
  dft8<SGN>(o[0]);
  hook(3);
  dft8<SGN>(o[1]);
  lds_fence();
}
constexpr float FWD_SC = 0.5f * 0.05590169943749474f;
template <int NP> DEV void rfft2_rows(const unsigned (*rw)[5], const Tw320& tw, f4* scr, int lane, c2 (*o)[8]) {
  c2 z[2 * NP][5];
#pragma unroll
  for (int i = 0; i < 2 * NP; i++)
#pragma unroll
    for (int j = 0; j < 5; j++) z[i][j] = c2{bf_lo(rw[i][j]), bf_hi(rw[i][j])};
  fft320x2<-1, NP>(z, tw, FWD_SC, scr, lane, o, [](int) {});
  const int u = lane < 40 ? lane : 39;
  f4* pp = scr + (u >> 3) + 5 * (u & 7);
#pragma unroll
  for (int p = 0; p < NP; p++) {
    if (p > 0) { dft8<-1>(o[2 * p]); dft8<-1>(o[2 * p + 1]); lds_fence(); }
#pragma unroll
    for (int e = 0; e < 8; e++) pp[p * SCR_F4 + 40 * e] = f4{o[2 * p][e].x, o[2 * p][e].y, o[2 * p + 1][e].x, o[2 * p + 1][e].y};
  }
}
DEV void untangle2(c2& x0, c2& x1, f4 A, f4 B, c2 w, bool k_is_0) {
  tangle2(x0, x1, A.xy, B.xy, A.zw, B.zw, w);
  if (k_is_0) {
    const float r = 1.41421356237309505f;
    x0 = c2{(A.x + A.y) * r, (A.x - A.y) * r};
    x1 = c2{(A.z + A.w) * r, (A.z - A.w) * r};
  }
}
DEV c2 mul_mi(c2 w) { return c2{w.y, -w.x}; }

template <int CTRL> DEV float dpp_add(float v) {
  return v + __builtin_bit_cast(float, __builtin_amdgcn_update_dpp(0, __builtin_bit_cast(int, v), CTRL, 0xf, 0xf, true));
}
DEV float wave_sum63(float v) {
  v = dpp_add<0x111>(v);
  v = dpp_add<0x112>(v);
  v = dpp_add<0x114>(v);
  v = dpp_add<0x118>(v);
  v = v + __builtin_bit_cast(float, __builtin_amdgcn_update_dpp(0, __builtin_bit_cast(int, v), 0x142, 0xa, 0xf, true));
  v = v + __builtin_bit_cast(float, __builtin_amdgcn_update_dpp(0, __builtin_bit_cast(int, v), 0x143, 0xc, 0xf, true));
  return v;
}

#define CODE_ADDR(A, SYM) asm("s_getpc_b64 vcc\n\ts_add_u32 vcc_lo, vcc_lo, " SYM "@rel32@lo+4\n\ts_addc_u32 vcc_hi, vcc_hi, " SYM "@rel32@hi+12\n\ts_mov_b64 %0, vcc" \
                              : "=s"(A) :: "vcc", "scc")
#define CODE_PF(P, COND, SYM, END) do { const char *b_, *e_; CODE_ADDR(b_, SYM); CODE_ADDR(e_, END); \
    long n_ = e_ - b_ - 4; n_ = n_ < 0 ? 0 : n_; long o_ = (long)threadIdx.x * PF_STEP; o_ = o_ < n_ ? o_ : n_ & ~3l; P = (COND) ? b_ + o_ : P; } while (0)
#define PF_STEP 64
#define PF_LOAD(P) (*(const __attribute__((address_space(1))) unsigned*)(P))
#define SYM_K1 "_Z11k_colfilterILb1EEvPKtPc"
#define SYM_K4 "_Z11k_colfilterILb0EEvPKtPc"
#define SYM_K2 "_Z5k_mlpILb0EEvPcP14__hip_bfloat16"
#define SYM_K5 "_Z5k_mlpILb1EEvPcP14__hip_bfloat16"
#define CODE_PF_K1(P, C) CODE_PF(P, C, SYM_K1, SYM_K4)
#define CODE_PF_K4(P, C) CODE_PF(P, C, SYM_K4, SYM_K2)
#define CODE_PF_K2(P, C) CODE_PF(P, C, SYM_K2, SYM_K5)
#define CODE_PF_K5(P, C) CODE_PF(P, C, SYM_K5, "k6_code_end")

DEV int frag_f(int T, int g, int j) {
  int G = T / 3, r = T - 3 * G;
  int ta = r == 0 ? 0 : (r == 1 ? 3 : 2), tb = r == 0 ? 1 : (r == 1 ? 4 : 5);
  return 96 * G + 16 * (j < 4 ? ta : tb) + 4 * g + (j & 3);
}

constexpr int K0_X = 128, K0_W1 = 96, K0_WT = 128;
template <bool is_x>
DEV void prep_rows(const uint16_t* __restrict__ x, const uint16_t* __restrict__ w1, char* __restrict__ ws, f4* scr_all, u2* stage, int grp) {
  const int tid = threadIdx.x, lane = tid & 63, wv = tid >> 6;
  f4* scr = scr_all + wv * SCR_F4;
  unsigned rw2[2][5];
  unsigned *r0 = rw2[0], *r1 = rw2[1];
  u4 s0[2], s1[2];
  if (is_x) {
    const unsigned* row = (const unsigned*)(x + (size_t)(16 * grp + 2 * wv) * D_);
#pragma unroll
    for (int j = 0; j < 5; j++) { r0[j] = row[lane + 64 * j]; r1[j] = row[320 + lane + 64 * j]; }
    if (tid < 16) ((double*)(ws + OFF_SSQ))[16 * grp + tid] = 0.0;
    if (tid >= 64 && tid < 80) ((unsigned*)(ws + OFF_FLG))[16 * grp + tid - 64] = 0u;
  } else {
#pragma unroll
    for (int it = 0; it < 2; it++) {
      int n = it == 0 ? tid : 512 + (tid & 127);
      const uint16_t* p = w1 + (size_t)(n >> 1) * (2 * F_) + grp * 16 + (n & 1) * 8;
      s0[it] = *(const u4*)p; s1[it] = *(const u4*)(p + F_);
    }
  }
  SCHED_FENCE();
  Tw320 tw;
  tw320_init(tw, lane, twiddle_rev((float)lane * (1.0f / 640.0f)), FWD_SC);
  c2 wo[3][2];
  wo[0][0] = mul_mi(twiddle_rev((float)(tid >> 3) * (1.0f / 320.0f)));
  wo[1][0] = cmul(wo[0][0], c2{0.30901699437494742f, -0.95105651629515357f});
  wo[2][0] = cmul(wo[0][0], c2{-0.80901699437494742f, -0.58778525229247313f});
#pragma unroll
  for (int it = 0; it < 3; it++) wo[it][1] = cmul(wo[it][0], c2{0.99995180895932817f, -0.00981731933714962f});
  SCHED_FENCE();
  if (!is_x) {
    asm volatile("" : "+v"(s0[1]), "+v"(s1[1]));
#pragma unroll
    for (int it = 0; it < 2; it++) {
      int n = tid + 512 * it;
      if (it == 0 || tid < 128) {
#pragma unroll
        for (int i = 0; i < 4; i++)
          stage[((n & 1) * 4 + i) * 324 + (n >> 1)] = u2{__builtin_amdgcn_perm(s1[it][i], s0[it][i], 0x05040100u), __builtin_amdgcn_perm(s1[it][i], s0[it][i], 0x07060302u)};
      }
    }
    __syncthreads();
#pragma unroll
    for (int j = 0; j < 5; j++) { u2 t = stage[wv * 324 + lane + 64 * j]; r0[j] = t.x; r1[j] = t.y; }
  }
  TSTAMP(0, 1);
  c2 o[2][8];
  rfft2_rows<1>(rw2, tw, scr, lane, o);
  if (is_x) {
    float ss[2];
#pragma unroll
    for (int rr = 0; rr < 2; rr++) {
      c2 t = o[rr][0];
      if (lane == 0) t = c2{(t.x - t.y) * 0.70710678118654752f, 0.f};
      c2 acc = t * t;
#pragma unroll
      for (int e = 1; e < 8; e++) acc += o[rr][e] * o[rr][e];
      ss[rr] = wave_sum63(lane < 40 ? acc.x + acc.y : 0.f);
    }
    if (lane == 63)
      *(c2*)(ws + OFF_RS0 + (size_t)(16 * grp + 2 * wv) * 4) =
          c2{__builtin_amdgcn_rsqf(ss[0] * (1.0f / 160.0f) + LN_EPS), __builtin_amdgcn_rsqf(ss[1] * (1.0f / 160.0f) + LN_EPS)} * (1.0f / 2048.0f);
  }
  TSTAMP(0, 2);
  __syncthreads();
  const int rp = tid & 7;
  const f4* S = scr_all + rp * SCR_F4;
#pragma unroll
  for (int it = 0; it < 3; it++) {
    if (it == 2 && tid >= 256) break;
    const int q4 = (tid >> 3) + 64 * it;
    f4 A0 = S[2 * q4], A1 = S[2 * q4 + 1], B0 = S[(it == 0 && q4 == 0) ? 0 : 320 - 2 * q4], B1 = S[319 - 2 * q4];
    c2 x00, x10, x01, x11;
    untangle2(x00, x10, A0, B0, wo[it][0], it == 0 && q4 == 0);
    untangle2(x01, x11, A1, B1, wo[it][1], false);
    if (is_x) {
      u4 v = u4{pack_bf(x00.x, x00.y), pack_bf(x01.x, x01.y), pack_bf(x10.x, x10.y), pack_bf(x11.x, x11.y)};
      __builtin_amdgcn_raw_buffer_store_b128(v, make_rsrc(ws + OFF_XB + (size_t)(grp >> 2) * XB_MS, XB_MS), q4 * 512 + (((grp & 3) * 128 + rp * 16) ^ (((q4 >> 1) & 1) << 7)), 0, AUX_K0);
    } else {
      if (it == 0 && q4 == 0) { x00.x = 0.f; x10.x = 0.f; }
      const rsrc_t Wr = make_rsrc(ws + OFF_W1P + (size_t)grp * W1_TS * 1024, W1_TS * 1024);
      const bool od = q4 & 1;
      const u2 c0 = u2{pack_bf(x00.x, x00.y), pack_bf(x01.x, x01.y)}, c1 = u2{pack_bf(x10.x, x10.y), pack_bf(x11.x, x11.y)};
      const u2 snd = od ? c0 : c1, kp = od ? c1 : c0;
      const u2 rcv = u2{(unsigned)__builtin_amdgcn_update_dpp(0, (int)snd.x, 0x128, 0xf, 0xf, false), (unsigned)__builtin_amdgcn_update_dpp(0, (int)snd.y, 0x128, 0xf, 0xf, false)};
      const u4 v = od ? u4{rcv.x, rcv.y, kp.x, kp.y} : u4{kp.x, kp.y, rcv.x, rcv.y};
      __builtin_amdgcn_raw_buffer_store_b128(v, Wr, ((q4 >> 1) * 16 + 2 * rp + (q4 & 1)) * 16, 0, AUX_W1);
    }
  }
  TSTAMP(0, 7);
}

__global__ __launch_bounds__(512) void k_prep(const uint16_t* __restrict__ x, const uint16_t* __restrict__ wr,
                                              const uint16_t* __restrict__ wi, const uint16_t* __restrict__ w1,
                                              const uint16_t* __restrict__ w2, const char* outp, char* __restrict__ ws) {
  __shared__ __attribute__((aligned(16))) f4 scr_all[8 * SCR_F4];
  __shared__ __attribute__((aligned(16))) u2 stage[8 * 324];
  const int tid = threadIdx.x, lane = tid & 63, wv = tid >> 6;
  const int b = blockIdx.x;
  TSTAMP(0, 0);
  const char* ps = nullptr;
  CODE_PF(ps, true, "_Z6k_prepPKtS0_S0_S0_S0_PKcPc", SYM_K1);
  const unsigned spf = tid < 192 ? PF_LOAD(ps) : 0u;
  SCHED_FENCE();

  if (b >= K0_X + K0_W1) {
    int bb = b - (K0_X + K0_W1);
    int unit = (bb & 7) * 16 + (bb >> 3);
    int u = unit & 3, bbit = (unit >> 2) & 1, kc = unit >> 3;
    unsigned* t32 = (unsigned*)scr_all;
    unsigned short re[2][6], im[2][6];
#pragma unroll
    for (int hh = 0; hh < 2; hh++) {
      int ks = kc + 16 * (2 * u + hh + 8 * bbit) + 256 * wv;
      const uint16_t* pr = wr + (size_t)ks * DF_;
      const uint16_t* pi = wi + (size_t)ks * DF_;
#pragma unroll
      for (int i = 0; i < 6; i++) {
        int c = lane + 64 * i;
        c = c < DF_ ? c : DF_ - 1;
        re[hh][i] = pr[c]; im[hh][i] = pi[c];
      }
    }
    SCHED_FENCE();
    const int sl = bb >> 3;
    const char* pa = ws + (size_t)(sl == 8 && tid * 65536u < OFF_XB2 + 32 * XB_MS ? tid * 65536u : 0u);
    if (sl == 9) pa = outp + (tid < 40 ? tid : 0) * 65536u;
    if (sl == 10) pa = (const char*)w2 + (tid < 30 ? tid : 0) * 65536u;
    CODE_PF_K1(pa, sl == 0); CODE_PF_K2(pa, sl == 1); CODE_PF_K4(pa, sl == 3); CODE_PF_K5(pa, sl == 4);
    CODE_PF(pa, sl == 6, SYM_K1 ".kd", SYM_K5 ".kd");
    { const char* a_; CODE_ADDR(a_, SYM_K5 ".kd"); pa = sl == 7 ? a_ : pa; }
    const int wi_ = (sl < 12 ? sl : 11) * 8 + (bb & 7), t3 = tid < 320 ? tid : 319, rr = t3 / 20;
    const unsigned cpf = PF_LOAD(pa);
    const unsigned wpf = *(const unsigned*)(w2 + (size_t)frag_f(wi_ >> 1, (wi_ & 1) * 2 + (rr >> 3), rr & 7) * D_ + (t3 % 20) * 32);
    SCHED_FENCE();
#pragma unroll
    for (int hh = 0; hh < 2; hh++)
#pragma unroll
      for (int i = 0; i < 6; i++) {
        int c = lane + 64 * i;
        if (c < DF_) t32[(hh * 8 + wv) * 324 + c] = (unsigned)re[hh][i] | ((unsigned)im[hh][i] << 16);
      }
    __syncthreads();
    const rsrc_t WT = make_rsrc(ws + OFF_WT, 160 * 4 * 256 * 16);
    for (int n = tid; n < 1280; n += 512) {
      int col = n & 1, hh = (n >> 1) & 1, ih = (n >> 2) & 1, q4 = n >> 3;
      int cc = 2 * q4 + col;
      if (cc == 0) cc = 320;
      u4 v;
#pragma unroll
      for (int e = 0; e < 4; e++) v[e] = t32[(hh * 8 + ih * 4 + e) * 324 + cc];
      int tau = (2 * u + hh) + 8 * kc;
      __builtin_amdgcn_raw_buffer_store_b128(v, WT, ((q4 * 4 + 2 * bbit + ih) * 256 + 2 * tau + col) * 16, 0, AUX_K0);
    }
    asm volatile("" :: "v"(cpf), "v"(spf), "v"(wpf));
    TSTAMP(0, 7);
    return;
  }

  if (b < K0_X) prep_rows<true>(x, w1, ws, scr_all, stage, (b & 7) * 16 + (b >> 3));
  else prep_rows<false>(x, w1, ws, scr_all, stage, ((b - K0_X) & 7) * 12 + ((b - K0_X) >> 3));
  asm volatile("" :: "v"(spf));
}

constexpr int P1 = 152;
constexpr int LDS_CF = 84 * 1024;
#undef PF_STEP
#define PF_STEP 128
template <bool FIRST>
__global__ __launch_bounds__(256, 1) void k_colfilter(const uint16_t* __restrict__ w2, char* __restrict__ ws) {
  extern __shared__ __attribute__((aligned(16))) char smem[];
  const int tid = threadIdx.x;
  const int xcd = blockIdx.x & 7, slot = blockIdx.x >> 3;
  constexpr int KID = FIRST ? 1 : 4;
  (void)KID;
  if (slot >= 20) {
    TSTAMP(KID, 0);
    const int lane = tid & 63, wv = tid >> 6;
    const int i = (slot - 20) * 8 + xcd;
    const int T = i >> 1, g0 = (i & 1) * 2;
    unsigned pf[6], cpf = 0;
    const char* pfbase = ws + OFF_W1P + (size_t)(xcd & 1) * 48 * W1_TS * 1024 + tid * 64;
#define L2_PREFETCH() do { SCHED_FENCE(); _Pragma("unroll") for (int j = 0; j < 6; j++) { int blk = (slot - 20) + 12 * j; blk = blk < 63 ? blk : 62; \
      pf[j] = *(const unsigned*)(pfbase + blk * 16384); } \
      const char* pc_ = nullptr; \
      if (FIRST) { CODE_PF_K4(pc_, true); } else { CODE_PF_K5(pc_, true); } \
      cpf = PF_LOAD(pc_); \
      SCHED_FENCE(); } while (0)
    if (FIRST) {
      f4* scr_all = (f4*)smem;
      unsigned raw[4][5];
#pragma unroll
      for (int rr = 0; rr < 4; rr++) {
        int r = wv * 4 + rr;
        const unsigned* row = (const unsigned*)(w2 + (size_t)frag_f(T, g0 + (r >> 3), r & 7) * D_);
#pragma unroll
        for (int j = 0; j < 5; j++) raw[rr][j] = row[lane + 64 * j];
      }
      L2_PREFETCH();
      Tw320 tw;
      tw320_init(tw, lane, twiddle_rev((float)lane * (1.0f / 640.0f)), FWD_SC);
      const int k0 = (tid & 7) + 8 * (tid >> 4), gg = (tid >> 3) & 1;
      c2 wo[3];
      wo[0] = mul_mi(twiddle_rev((float)k0 * (1.0f / 640.0f)));
      wo[1] = cmul(wo[0], c2{0.30901699437494742f, -0.95105651629515357f});
      wo[2] = cmul(wo[0], c2{-0.80901699437494742f, -0.58778525229247313f});
      SCHED_FENCE();
      c2 o[4][8];
      rfft2_rows<2>(raw, tw, scr_all + (wv * 2) * SCR_F4, lane, o);
      __syncthreads();
      const f4* S = scr_all + gg * 4 * SCR_F4;
#pragma unroll
      for (int it = 0; it < 3; it++) {
        if (it == 2 && tid >= 128) break;
        const int k = k0 + 128 * it;
        const bool z = it == 0 && k == 0;
        f4 A[4], B[4];
#pragma unroll
        for (int jp = 0; jp < 4; jp++) { A[jp] = S[jp * SCR_F4 + k]; B[jp] = S[jp * SCR_F4 + (z ? 0 : 320 - k)]; }
        u4 re, im;
#pragma unroll
        for (int jp = 0; jp < 4; jp++) {
          c2 x0, x1;
          untangle2(x0, x1, A[jp], B[jp], wo[it], z);
          re[jp] = pack_bf(x0.x, x1.x); im[jp] = pack_bf(x0.y, x1.y);
        }
        const rsrc_t Wr = make_rsrc(ws + OFF_W2P, 40 * H_TS * 1024);
        const unsigned vo = (((k >> 3) * H_TS + T) * 64 + (g0 + gg) * 16 + (k & 7)) * 16;
        __builtin_amdgcn_raw_buffer_store_b128(re, Wr, vo, 0, AUX_W1);
        __builtin_amdgcn_raw_buffer_store_b128(im, Wr, vo + 128, 0, AUX_W1);
      }
    } else {
      const rsrc_t W2C = make_rsrc(ws + OFF_W2C, 40 * H_TS * 1024);
      unsigned short v[5][8];
#pragma unroll
      for (int it = 0; it < 5; it++) {
        int n = tid + 256 * it;
        int cl = n & 15, gg = (n >> 4) & 1, ct = n >> 5;
#pragma unroll
        for (int j = 0; j < 8; j++) v[it][j] = w2[(size_t)frag_f(T, g0 + gg, j) * D_ + ct * 16 + cl];
      }
      L2_PREFETCH();
#pragma unroll
      for (int it = 0; it < 5; it++) {
        int n = tid + 256 * it;
        int cl = n & 15, gg = (n >> 4) & 1, ct = n >> 5;
        u4 o;
#pragma unroll
        for (int j = 0; j < 4; j++) o[j] = (unsigned)v[it][2 * j] | ((unsigned)v[it][2 * j + 1] << 16);
        __builtin_amdgcn_raw_buffer_store_b128(o, W2C, ((ct * H_TS + T) * 64 + (g0 + gg) * 16 + cl) * 16, 0, AUX_K0);
      }
    }
    asm volatile("" :: "v"(pf[0]), "v"(pf[1]), "v"(pf[2]), "v"(pf[3]), "v"(pf[4]), "v"(pf[5]), "v"(cpf));
    TSTAMP(KID, 7);
    return;
  }
  typedef __attribute__((address_space(3))) volatile c2* ldsp;
  const int col = tid & 1, tau = tid >> 1;
  const int q4 = xcd * 20 + slot;
  const int lo = tau & 7, kc = tau >> 3;
  PT_DECL; PT(0);
  const rsrc_t XBr = make_rsrc(ws + (FIRST ? OFF_XB : OFF_XB2) + q4 * 512, 32 * XB_MS);
  const unsigned xoff = (tau >> 6) * XB_MS + ((((tau & 63) << 1) + col) ^ (((q4 >> 1) & 1) << 5)) * 4;
  unsigned xr[16];
  c2 v[16];
  float rs[16];
  u2 sq[16];
  u4 wt[4];
  {
    const rsrc_t Sr = make_rsrc(ws + OFF_RS0, 2048 * 12);
#pragma unroll
    for (int rq = 0; rq < 16; rq++) {
      const int r = (rq >> 2) + 4 * (rq & 3);
      if ((rq & 3) == 0) SCHED_FENCE();
      xr[r] = __builtin_amdgcn_raw_buffer_load_b32(XBr, xoff, r * (2 * XB_MS), 0);
      if (FIRST) rs[r] = __builtin_bit_cast(float, __builtin_amdgcn_raw_buffer_load_b32(Sr, tau * 4 + (r & 7) * 512, (r >> 3) * 4096, 0));
      else sq[r] = __builtin_amdgcn_raw_buffer_load_b64(Sr, tau * 8 + (r & 3) * 1024, 8192 + (r >> 2) * 4096, 0);
    }
    SCHED_FENCE();
    const rsrc_t Wr = make_rsrc(ws + OFF_WT + (size_t)q4 * 16384, 16384);
#pragma unroll
    for (int i = 0; i < 4; i++) wt[i] = __builtin_amdgcn_raw_buffer_load_b128(Wr, tid * 16, i * 4096, 0);
  }
  SCHED_FENCE();
  c2 tw1[15], tw2[15];
  tw_powers<15>(tw1, twiddle_rev((float)tau * (1.0f / 2048.0f)));
  tw_powers<15>(tw2, twiddle_rev((float)lo * (1.0f / 128.0f)));
  const bool special = (q4 == 0) && (col == 0);
  const unsigned sel_lo = special ? 0x07060c0cu : 0x05040c0cu, sel_hi = special ? 0x0c0c0c0cu : 0x07060c0cu;
  ldsp e1 = (ldsp)(uintptr_t)(unsigned)(uintptr_t)(__attribute__((address_space(3))) char*)smem + tid;
  ldsp e2 = (ldsp)(uintptr_t)(unsigned)(uintptr_t)(__attribute__((address_space(3))) char*)smem + (kc * P1 + lo) * 2 + col;
  ldsp e3 = (ldsp)(uintptr_t)(unsigned)(uintptr_t)(__attribute__((address_space(3))) char*)smem + (kc * P1 + lo * 9) * 2 + col;
  SCHED_FENCE();
  c2 xv[16];
  dft16s<-1>(v,
    [&](int i) {
#pragma unroll
      for (int j = 0; j < 4; j++) {
        const int r = i + 4 * j;
        if (!FIRST) {
          u2 t = sq[r];
          rs[r] = __builtin_amdgcn_rsqf((float)__builtin_bit_cast(double, t) * (2048.0f * 2048.0f / 640.0f) + LN_EPS * 2048.0f * 2048.0f);
        }
        xv[r] = c2{__builtin_bit_cast(float, __builtin_amdgcn_perm(xr[r], 0u, sel_lo)), __builtin_bit_cast(float, __builtin_amdgcn_perm(xr[r], 0u, sel_hi))};
        v[r] = xv[r] * rs[r];
      }
      if (i == 0) PT(1);
    },
    [&](int q) {
      if (q == 0) v[4] = cmul(v[4], tw1[3]); else twmulp<-1>(v[q], tw1[q - 1], v[q + 4], tw1[q + 3]);
      e1[q * (2 * P1)] = v[q]; e1[(q + 4) * (2 * P1)] = v[q + 4];
      twmulp<-1>(v[q + 8], tw1[q + 7], v[q + 12], tw1[q + 11]);
      e1[(q + 8) * (2 * P1)] = v[q + 8]; e1[(q + 12) * (2 * P1)] = v[q + 12];
    });
  __syncthreads();
#pragma unroll
  for (int i = 0; i < 4; i++)
#pragma unroll
    for (int j = 0; j < 4; j++) v[i + 4 * j] = e2[(i + 4 * j) * 16];
  SCHED_FENCE();
  c2 wf[16];
#pragma unroll
  for (int r = 0; r < 16; r++) { unsigned w = wt[r >> 2][r & 3]; wf[r] = c2{bf_lo(w), bf_hi(w)}; }
  SCHED_FENCE();
  dft16s<-1>(v, [&](int) {},
    [&](int q) {
      if (q == 0) v[4] = cmul(v[4], tw2[3]); else twmulp<-1>(v[q], tw2[q - 1], v[q + 4], tw2[q + 3]);
      e2[q * 18] = v[q]; e2[(q + 4) * 18] = v[q + 4];
      twmulp<-1>(v[q + 8], tw2[q + 7], v[q + 12], tw2[q + 11]);
      e2[(q + 8) * 18] = v[q + 8]; e2[(q + 12) * 18] = v[q + 12];
    });
#pragma unroll
  for (int bb = 0; bb < 2; bb++)
#pragma unroll
    for (int sa = 0; sa < 8; sa++) v[bb * 8 + sa] = e3[(bb * 72 + sa) * 2];
  SCHED_FENCE();
  PT(2);
#pragma unroll
  for (int bb = 0; bb < 2; bb++) {
    dft8<-1>(v + 8 * bb);
#pragma unroll
    for (int r = 0; r < 8; r += 2) twmulp<-1>(v[8 * bb + r], wf[8 * bb + r], v[8 * bb + r + 1], wf[8 * bb + r + 1]);
    SCHED_FENCE();
  }
  dft8<1>(v);
  SCHED_FENCE();
  {
    c2* u = v + 8;
    c2 a0 = u[0], a1 = u[2], a2 = u[4], a3 = u[6], o0 = u[1], o1 = u[3], o2 = u[5], o3 = u[7];
    e3[0] = v[0]; e3[2] = v[1];
    dft4<1>(a0, a1, a2, a3);
    SCHED_FENCE();
    e3[4] = v[2]; e3[6] = v[3];
    dft4<1>(o0, o1, o2, o3);
    SCHED_FENCE();
    e3[8] = v[4]; e3[10] = v[5];
    o1 = rot1<1>(o1); o3 = rot3<1>(o3);
    u[0] = a0 + o0; u[4] = a0 - o0;
    u[2] = add_rot<1>(a2, o2); u[6] = sub_rot<1>(a2, o2);
    SCHED_FENCE();
    e3[12] = v[6]; e3[14] = v[7];
    u[1] = a1 + o1; u[5] = a1 - o1;
    u[3] = a3 + o3; u[7] = a3 - o3;
    SCHED_FENCE();
#pragma unroll
    for (int sa = 0; sa < 8; sa++) e3[(72 + sa) * 2] = u[sa];
  }
#pragma unroll
  for (int i = 0; i < 4; i++)
#pragma unroll
    for (int j = 0; j < 4; j++) v[i + 4 * j] = e2[(i + 4 * j) * 18];
  SCHED_FENCE();
  dft16s<1>(v,
    [&](int i) {
      if (i == 0) v[4] = cmulc(v[4], tw2[3]); else twmulp<1>(v[i], tw2[i - 1], v[i + 4], tw2[i + 3]);
      twmulp<1>(v[i + 8], tw2[i + 7], v[i + 12], tw2[i + 11]);
    },
    [&](int q) {
#pragma unroll
      for (int j = 0; j < 4; j++) e2[(q + 4 * j) * 16] = v[q + 4 * j];
    });
  __syncthreads();
#pragma unroll
  for (int i = 0; i < 4; i++)
#pragma unroll
    for (int j = 0; j < 4; j++) v[i + 4 * j] = e1[(i + 4 * j) * (2 * P1)];
  SCHED_FENCE();
  const unsigned sel_o = special ? 0x05040100u : 0x07060504u;
  PT(3);
  dft16s<1>(v,
    [&](int i) {
      if (i == 0) v[4] = cmulc(v[4], tw1[3]); else twmulp<1>(v[i], tw1[i - 1], v[i + 4], tw1[i + 3]);
      twmulp<1>(v[i + 8], tw1[i + 7], v[i + 12], tw1[i + 11]);
    },
    [&](int q) {
#pragma unroll
      for (int j = 0; j < 4; j++) {
        const int r = q + 4 * j;
        c2 o = xv[r] + v[r];
        __builtin_amdgcn_raw_buffer_store_b32(__builtin_amdgcn_perm(pack_bf(o.x, o.y), xr[r], sel_o), XBr, xoff, r * (2 * XB_MS), AUX_CF);
      }
    });
  PT(7); PT_FLUSH(KID);
}

constexpr int G1_A_BYTES = 81920;
constexpr int G1_WSCR = SCR_F4 * 16;
#define G1_PF 4
#define G1_PFI 4
#define G1_VOL volatile
#define G1_LGKM 0xc87f
#define G1_LS 4
#define G1_NP1 40
#define G2_PF 4
#define HO_AUX 16
#define G2_SPIN 4096
#define G2_ND 12
#define G2_WS 2
#define G2_BD 4
template <int I> struct ic { static constexpr int value = I; };
template <int N, int I = 0, typename F> DEV void static_for(F f) { if constexpr (I < N) { f(ic<I>{}); static_for<N, I + 1>(f); } }
constexpr int LDS_G1 = G1_A_BYTES + 512 + 4096;
constexpr int LDS_G1I = LDS_G1 + 4 * G1_WSCR;
DEV float dot2(unsigned a, float c) {
  bf2 q = __builtin_bit_cast(bf2, a);
  return __builtin_amdgcn_fdot2_f32_bf16(q, q, c, false);
}
template <bool WITH_INV>
DEV void gemm1_body(char* __restrict__ ws, char* smem, const int mblk, const int fs) {
  int tid = threadIdx.x;
  asm volatile("" : "+v"(tid));
  const int lane = tid & 63;
  const int wv = __builtin_amdgcn_readfirstlane(tid >> 6);
  constexpr int KID = WITH_INV ? 5 : 2;
  (void)KID;
  PT_DECL; PT(0);
  const char* Ag = ws + (WITH_INV ? OFF_XB2 : OFF_XB) + (size_t)mblk * XB_MS;
  const unsigned lds0 = (unsigned)(uintptr_t)(__attribute__((address_space(3))) char*)smem + wv * 20480;
  const unsigned l16 = lane * 16;
#define G1_DMA(I) do { unsigned keep_; asm volatile( \
          "s_mov_b32 %0, m0\n\ts_mov_b32 m0, %3\n\ts_nop 0\n\t" \
          "global_load_lds_dwordx4 %1, %2\n\t" \
          "global_load_lds_dwordx4 %1, %2 offset:1024\n\t" \
          "global_load_lds_dwordx4 %1, %2 offset:2048\n\t" \
          "global_load_lds_dwordx4 %1, %2 offset:3072\n\t" \
          "s_mov_b32 m0, %0" \
          : "=&s"(keep_) : "v"(l16), "s"(Ag + wv * 20480 + (I) * 4096), "s"(lds0 + (I) * 4096) : "memory"); SCHED_FENCE(); } while (0)
  const rsrc_t Wg = make_rsrc(ws + OFF_W1P + (size_t)(fs * 12 + wv * 3) * W1_TS * 1024, 3 * W1_TS * 1024);
  constexpr int PF = WITH_INV ? G1_PFI : G1_PF;
  constexpr int NL = 3 * PF;
  bf8 wf[60];
#define G1_LOAD1(I) do { if ((I) < 60) wf[(I) < 60 ? (I) : 0] = bload(Wg, l16, ((I) / 20 * W1_TS + (I) % 20) * 1024); } while (0)
#define G1_LOADW(S) do { if ((S) < PF) { G1_LOAD1(3 * (S)); G1_LOAD1(3 * (S) + 1); G1_LOAD1(3 * (S) + 2); SCHED_FENCE(); } } while (0)
  unsigned pk3[2][8];
  const int m0 = fs * 8 + 2 * wv;
  const int iu = lane < 40 ? lane : 39;
  const int iq = iu >> 3, ia = iu & 7;
  if (!WITH_INV) {
    G1_DMA(0); G1_DMA(1); G1_DMA(2); G1_DMA(3); G1_DMA(4);
#pragma unroll
    for (int s = 0; s < PF; s++) G1_LOADW(s);
    PT(6);
  } else {
    f4* scr = (f4*)(smem + LDS_G1 + wv * G1_WSCR);
    u2* buf = (u2*)scr;
    u4 rw[3];
#pragma unroll
    for (int i = 0; i < 3; i++) {
      int q = lane + 64 * i;
      if (i == 2) q = q < 160 ? q : 159;
      unsigned vo = q * 512 + ((m0 * 8) ^ (((q >> 1) & 1) << 7));
      asm volatile("global_load_dwordx4 %0, %1, %2" : "=v"(rw[i]) : "v"(vo), "s"(Ag) : "memory");
    }
    SCHED_FENCE();
    G1_DMA(0); G1_DMA(1);
    const float sc = 0.5f * 17.88854381999832f / 320.0f;
    const float r2 = 1.41421356237309505f;
    Tw320 tw;
    c2 wu[5];
    {
      c2 u0 = twiddle_rev((float)lane * (1.0f / 640.0f));
      tw320_init(tw, lane, u0, sc);
      wu[0] = c2{u0.y, u0.x};
      wu[1] = cmul(wu[0], c2{0.80901699437494742f, 0.58778525229247313f});
      wu[2] = cmul(wu[0], c2{0.30901699437494742f, 0.95105651629515357f});
      wu[3] = cmul(wu[0], c2{-0.30901699437494742f, 0.95105651629515357f});
      wu[4] = cmul(wu[0], c2{-0.80901699437494742f, 0.58778525229247313f});
    }
    SCHED_FENCE();
    G1_DMA(2); G1_DMA(3); G1_DMA(4);
    PT(4);
    asm volatile("s_waitcnt vmcnt(20)" : "+v"(rw[0]), "+v"(rw[1]), "+v"(rw[2]) :: "memory");
    PT(5);
#pragma unroll
    for (int i = 0; i < 3; i++) {
      int q = lane + 64 * i;
      if (i == 2) q = q < 160 ? q : 159;
      *(u4*)&buf[2 * q] = u4{rw[i].x, rw[i].z, rw[i].y, rw[i].w};
    }
    lds_fence();
    c2 z[2][5], o[2][8];
    u2 ua[5], ub[5];
#pragma unroll
    for (int j = 0; j < 5; j++) {
      int k = lane + 64 * j;
      ua[j] = buf[k]; ub[j] = buf[(j == 0 && k == 0) ? 0 : 320 - k];
    }
    SCHED_FENCE();
    G1_LOADW(0); G1_LOADW(1);
#pragma unroll
    for (int j = 0; j < 5; j++) {
      c2 A0 = c2{bf_lo(ua[j].x), bf_hi(ua[j].x)}, B0 = c2{bf_lo(ub[j].x), bf_hi(ub[j].x)};
      c2 A1 = c2{bf_lo(ua[j].y), bf_hi(ua[j].y)}, B1 = c2{bf_lo(ub[j].y), bf_hi(ub[j].y)};
      if (j == 0 && lane == 0) {
        B0 = c2{A0.y * r2, 0.f}; A0 = c2{A0.x * r2, 0.f};
        B1 = c2{A1.y * r2, 0.f}; A1 = c2{A1.x * r2, 0.f};
      }
      tangle2(z[0][j], z[1][j], A0, B0, A1, B1, wu[j]);
    }
    fft320x2<1, 1>(z, tw, sc, scr, lane, o, [&](int h) {
      if (h == 0) { G1_LOADW(2); G1_LOADW(3); }
      if (h == 2) { G1_LOADW(4); G1_LOADW(5); }
    });
    PT(6);
#pragma unroll
    for (int rr = 0; rr < 2; rr++)
#pragma unroll
      for (int e = 0; e < 8; e++) pk3[rr][e] = pack_bf(o[rr][e].x, o[rr][e].y);
    SCHED_FENCE();
#pragma unroll
    for (int s = 6; s < PF; s++) G1_LOADW(s);
  }
  const rsrc_t Hr = make_rsrc(ws + OFF_H + (size_t)mblk * 4 * H_TS * 1024, 4 * H_TS * 1024);
  const int G = fs * 2 + (wv >> 1);
  int hfull[4], hhalf[4];
#pragma unroll
  for (int m = 0; m < 4; m++) {
    hfull[m] = ((m ^ wv) * H_TS + 3 * G + (wv & 1)) * 1024; hhalf[m] = ((m ^ wv) * H_TS + 3 * G + 2) * 1024;
    asm("" : "+s"(hfull[m]), "+s"(hhalf[m]));
  }
  SCHED_FENCE();
  asm volatile("s_waitcnt vmcnt(%0)\n\ts_barrier" :: "n"(3 * PF) : "memory");
  if (WITH_INV) {
    const rsrc_t Xr = make_rsrc(ws + OFF_X3 + (size_t)(mblk * 64 + m0) * 1280, 2560);
    const unsigned vo = (iq + 5 * ia) * 4;
#pragma unroll
    for (int rr = 0; rr < 2; rr++)
#pragma unroll
      for (int e = 0; e < 8; e++) __builtin_amdgcn_raw_buffer_store_b32(pk3[rr][e], Xr, vo + (rr * 320 + 40 * e) * 4, 0, AUX_X3);
    SCHED_FENCE();
  }
  PT(1);
  const int g = lane >> 4, r = lane & 15;
  const int swz = (g & 1) << 7;
  typedef __attribute__((address_space(3))) const char* ldsc;
  typedef __attribute__((address_space(3))) G1_VOL const u2* ldsu2;
  ldsc abase[4], abase2[4];
#pragma unroll
  for (int m = 0; m < 4; m++) {
    unsigned o = (2 * g) * 512 + (((16 * (m ^ wv) + r) * 8) ^ swz), o2 = o + 65536;
    asm("" : "+v"(o2));
    abase[m] = (ldsc)smem + o; abase2[m] = (ldsc)smem + o2;
  }
  SCHED_FENCE();
  f4 acc[3][4];
#pragma unroll
  for (int t = 0; t < 3; t++)
#pragma unroll
    for (int m = 0; m < 4; m++) acc[t][m] = f4{0.f, 0.f, 0.f, 0.f};
  u4 af[20][4];
#define G1_READA(KS, M) do { ldsc ap_ = (KS) < 16 ? abase[M] + (KS) * 4096 : abase2[M] + ((KS) - 16) * 4096; \
    u2 lo_ = *(ldsu2)ap_, hi_ = *(ldsu2)(ap_ + 512); af[KS][M] = u4{lo_.x, lo_.y, hi_.x, hi_.y}; } while (0)
#define G1_MFMA(ACC, W, B) ACC = __builtin_amdgcn_mfma_f32_16x16x32_bf16(W, __builtin_bit_cast(bf8, B), ACC, 0, 0, 0)
  G1_READA(0, 0); G1_READA(1, 0); G1_READA(0, 1); G1_READA(0, 2); G1_READA(0, 3);
  G1_READA(2, 0); G1_READA(3, 0); G1_READA(1, 1); G1_READA(1, 2); G1_READA(1, 3);
  typedef __attribute__((address_space(3))) volatile float* ldsf;
  ldsf stw = (ldsf)((ldsc)smem + G1_A_BYTES) + (g == (r >> 2) ? wv * 16 + r : 64 + (lane & 31));
  ldsf str = (ldsf)((ldsc)smem + G1_A_BYTES) + r;
  float rl[4], irs[4], e0, e1;
  unsigned pk[3][4][2];
  SCHED_FENCE();
  f4 gram = f4{0.f, 0.f, 0.f, 0.f};
  constexpr int NP1 = G1_NP1;
  static_for<20>([&](auto kI) {
    constexpr int ks = kI.value;
    constexpr int l0 = NL + (NP1 - NL) * ks / 20, l1 = NL + (NP1 - NL) * (ks + 1) / 20;
    constexpr int infl = (ks == 0 ? 10 : ks <= 8 ? 10 : ks == 12 ? 7 : ks == 14 ? 10 : ks < 19 ? 6 : 0);
    __builtin_amdgcn_s_waitcnt(0xc07f | ((infl & 15) << 8));
    SCHED_FENCE();
    if (ks < 10) {
      u4 ga = af[2 * ks][0];
      if (ks == 0 && g == 0) ga.x &= 0xffff0000u;
      G1_MFMA(gram, __builtin_bit_cast(bf8, ga), af[2 * ks][0]);
      if (ks < 8) G1_READA(ks < 8 ? 2 * ks + 4 : 0, 0);
      SCHED_FENCE();
    }
    static_for<4>([&](auto mI) {
      constexpr int m = mI.value;
      G1_MFMA(acc[0][m], wf[ks], af[ks][m]);
      if (m == 0 && ks < 8) G1_READA(ks < 8 ? 2 * ks + 5 : 0, 0);
      if (m > 0 && ks + 2 < 20) G1_READA(ks + 2 < 20 ? ks + 2 : 0, m);
      if (m == 0 && l0 < l1) G1_LOAD1(l0);
      if (m == 2 && l0 + 1 < l1) G1_LOAD1(l0 + 1);
      if (m == 0 && ks == 11) {
        float sv = (r & 2) ? ((r & 1) ? gram.w : gram.z) : ((r & 1) ? gram.y : gram.x);
        *stw = sv * (1.0f / 640.0f) + LN_EPS;
      }
      if (m == 0 && ks == 13) {
        asm volatile("s_barrier" ::: "memory");
#pragma unroll
        for (int q = 0; q < 4; q++) irs[q] = str[(q ^ wv) * 16];
      }
      if (m == 0 && ks >= 15 && ks < 19) {
        constexpr int q = (ks - 15) & 3;
        float rs = __builtin_amdgcn_rsqf(irs[q]);
        asm("s_nop 0\n\tv_mul_f32 %0, %0, %2\n\tv_mul_f32 %1, 0xbfb8aa3b, %2" : "+v"(irs[q]), "=v"(rl[q]) : "v"(rs));
      }
      SCHED_FENCE();
      if (m == 1 && ks < 10) {
        G1_MFMA(gram, __builtin_bit_cast(bf8, af[2 * ks + 1][0]), af[2 * ks + 1][0]);
        SCHED_FENCE();
      }
    });
  });
  PT(2);
  typedef __attribute__((address_space(3))) volatile u2* ldsx;
  ldsx xw = (ldsx)((ldsc)smem + G1_A_BYTES + 512) + wv * 128 + lane, xr = (ldsx)((ldsc)smem + G1_A_BYTES + 512) + (wv ^ 1) * 128 + lane;
  float a0, a1;
  auto silu_op = [&](auto cI, auto oI) {
    constexpr int c = cI.value, o = oI.value, t = c >> 3, m = (c >> 1) & 3, e = 2 * (c & 1);
    if (o == 0) { a0 = acc[t][m][e]; a1 = acc[t][m][e + 1]; asm("" : "+v"(a0), "+v"(a1)); }
    if (o == 1) asm("v_mul_f32 %0, %2, %4\n\tv_mul_f32 %1, %3, %4" : "=&v"(e0), "=&v"(e1) : "v"(a0), "v"(a1), "v"(rl[m]));
    if (o == 2) asm("v_exp_f32 %0, %0" : "+v"(e0));
    if (o == 3) asm("v_exp_f32 %0, %0" : "+v"(e1));
    if (o == 4) asm("v_fma_f32 %0, %0, %2, %2\n\tv_fma_f32 %1, %1, %2, %2" : "+v"(e0), "+v"(e1) : "v"(irs[m]));
    if (o == 5) asm("v_fma_f32 %0, %0, %2, %2\n\tv_fma_f32 %1, %1, %2, %2\n\tv_rcp_f32 %0, %0" : "+v"(e0), "+v"(e1) : "v"(irs[m]));
    if (o == 6) asm("v_rcp_f32 %0, %0" : "+v"(e0));
    if (o == 7) asm("v_rcp_f32 %0, %0" : "+v"(e1));
    if (o == 8) asm("v_rcp_f32 %1, %1\n\tv_mul_f32 %0, %0, %2" : "+v"(e0), "+v"(e1) : "v"(a0));
    if (o == 9) asm("v_mul_f32 %0, %0, %2\n\tv_mul_f32 %1, %1, %3" : "+v"(e0), "+v"(e1) : "v"(a0), "v"(a1));
    if (o == 10) asm("v_cvt_pk_bf16_f32 %0, %1, %2" : "=v"(pk[t][m][c & 1]) : "v"(e0), "v"(e1));
    if (o == 11) asm("v_mul_f32 %2, %2, %3\n\tv_cvt_pk_bf16_f32 %0, %1, %2" : "=v"(pk[t][m][c & 1]), "+v"(e0), "+v"(e1) : "v"(a1));
    if (o == 10 || o == 11) {
      if (t == 1 && (c & 1))
        __builtin_amdgcn_raw_buffer_store_b128(u4{pk[0][m][0], pk[0][m][1], pk[1][m][0], pk[1][m][1]}, Hr, l16, hfull[m], ST_AUX);
      if (t == 2 && (c & 1) && m == 1) xw[0] = u2{pk[2][1][0], pk[2][1][1]};
    }
    SCHED_FENCE();
  };
#define SOP(C, O) silu_op(ic<(C)>{}, ic<(O)>{})
  static_for<160>([&](auto jI) {
    constexpr int j = jI.value;
    constexpr int t = j < 80 ? 1 : 2;
    constexpr int ks = j < 40 ? j >> 2 : j < 80 ? 10 + ((j - 40) % 20) / 2 : ((j - 80) % 40) >> 1;
    constexpr int m = j < 40 ? j & 3 : j < 80 ? ((j - 40) / 20) * 2 + (j & 1) : ((j - 80) / 40) * 2 + (j & 1);
    G1_MFMA(acc[t][m], wf[20 * t + ks], af[ks][m]);
    SCHED_FENCE();
    constexpr int c = j >> 3, o = j & 7;
    if (j == 0) SOP(0, 0);
    if (o == 0) SOP(c, 1);
    if (o == 1) SOP(c, 2);
    if (o == 2) SOP(c, 3);
    if (o == 3) SOP(c, 4);
    if (o == 4) SOP(c, 6);
    if (o == 5) SOP(c, 7);
    if (o == 6) SOP(c, 9);
    if (o == 7) { SOP(c, 10); if (c < 19) SOP(c + 1, 0); }
    if (j % G1_LS == 3) G1_LOAD1(NP1 + j / G1_LS);
    if (j == 80) PT(3);
    SCHED_FENCE();
  });
  PT(4 + (WITH_INV ? 3 : 0));
  {
#pragma unroll
    for (int m = 3; m >= 2; m--) {
      float x[4];
#pragma unroll
      for (int i = 0; i < 4; i++) x[i] = __builtin_amdgcn_exp2f(acc[2][m][i] * rl[m]);
#pragma unroll
      for (int i = 0; i < 4; i++) x[i] = __builtin_amdgcn_rcpf(__builtin_fmaf(x[i], irs[m], irs[m])) * acc[2][m][i];
      pk[2][m][0] = pack_bf(x[0], x[1]); pk[2][m][1] = pack_bf(x[2], x[3]);
      if (m == 3) xw[64] = u2{pk[2][3][0], pk[2][3][1]};
      SCHED_FENCE();
    }
    asm volatile("s_waitcnt lgkmcnt(0)\n\ts_barrier" ::: "memory");
    const u2 ox[2] = {xr[0], xr[64]};
#pragma unroll
    for (int h = 0; h < 2; h++) {
      const u2 o = ox[h];
      const u4 v = (wv & 1) ? u4{o.x, o.y, pk[2][2 * h][0], pk[2][2 * h][1]} : u4{pk[2][2 * h][0], pk[2][2 * h][1], o.x, o.y};
      __builtin_amdgcn_raw_buffer_store_b128(v, Hr, l16, hhalf[2 * h], ST_AUX);
    }
  }
  PT(7); PT_FLUSH(KID);
}

template <bool FINAL>
DEV int gemm2_body(char* __restrict__ ws, __hip_bfloat16* __restrict__ out, char* smem, const int mblk, const int cs) {
  f4* red = (f4*)smem;
  int tid = threadIdx.x;
  asm volatile("" : "+v"(tid));
  const int lane = tid & 63;
  const int wv = __builtin_amdgcn_readfirstlane(tid >> 6);
  const int g = lane >> 4, r = lane & 15;
  constexpr int KID = FINAL ? 6 : 3;
  (void)KID;
  PT_DECL; PT(0);
  const unsigned l16 = lane * 16;
  const rsrc_t Hr = make_rsrc(ws + OFF_H + ((size_t)mblk * 4 * H_TS + wv * 12) * 1024, (4 * H_TS - wv * 12) * 1024);
  const rsrc_t Wr = make_rsrc(ws + (FINAL ? OFF_W2C : OFF_W2P) + ((size_t)cs * 5 * H_TS + wv * 12) * 1024, (5 * H_TS - wv * 12) * 1024);
  int hoff[4];
#pragma unroll
  for (int m = 0; m < 4; m++) hoff[m] = ((m + wv) & 3) * H_TS * 1024;
  constexpr int PF = G2_PF, ND = 12, S0 = 0;
  bf8 hf[12][4], wf[12][5];
#define G2_LH(S, M) hf[S][M] = __builtin_bit_cast(bf8, __builtin_amdgcn_raw_buffer_load_b128(Hr, l16 + ((S) & 3) * 1024, hoff[M] + ((S) >> 2) * 4096, HO_AUX))
#define G2_LW(S, C) wf[S][C] = bload(Wr, l16, ((C) * H_TS + (S)) * 1024)
#define G2_LOAD(S, J) do { constexpr int s_ = (S), j_ = (J); \
    if (s_ < 12) { if (j_ < 3) G2_LH(s_ < 12 ? s_ : 0, j_ < 3 ? j_ + 1 : 0); } \
    else if (j_ < 3 && (s_ - 12) * 3 + j_ < 12) G2_LH(((s_ - 12) * 3 + j_) % 12, 0); \
    } while (0)
#pragma unroll
  for (int s = 0; s < 12; s++)
#pragma unroll
    for (int c = 0; c < 5; c++) G2_LW(s, c);
  SCHED_FENCE();
  {
    unsigned* flg = (unsigned*)(ws + OFF_FLG) + ((FINAL ? 32 : 0) + mblk) * 32;
    __attribute__((address_space(3))) volatile int* bc = (__attribute__((address_space(3))) volatile int*)(smem + G1_A_BYTES);
    unsigned long long miss = 1;
    for (int it = 0; it < G2_SPIN && miss; it++) {
      miss = __builtin_amdgcn_ballot_w64(__hip_atomic_load(flg + (lane & 31), __ATOMIC_RELAXED, __HIP_MEMORY_SCOPE_AGENT) == 0u);
      if (miss) __builtin_amdgcn_s_sleep(2);
    }
    if (lane == 0) bc[wv] = miss ? (int)(__builtin_ctzll(miss) >> 2) : -1;
    __syncthreads();
    const int m01 = bc[0] > bc[1] ? bc[0] : bc[1], m23 = bc[2] > bc[3] ? bc[2] : bc[3];
    const int mm = __builtin_amdgcn_readfirstlane(m01 > m23 ? m01 : m23);
    __syncthreads();
    if (mm >= 0) return mm;
    asm volatile("" ::: "memory");
  }
  SCHED_FENCE();
  const int mrow = mblk * 64 + wv * 16 + r;
  const int gq = 2 * (g & 1) + (g >> 1);
  const unsigned xo = gq * 512 + (((wv * 16 + r) * 8) ^ ((gq >> 1) << 7));
  u2 res[5];
  {
    const rsrc_t Rr = FINAL ? make_rsrc(ws + OFF_X3, 2048 * 1280) : make_rsrc(ws + OFF_XB + (size_t)mblk * XB_MS, XB_MS);
    const unsigned ro = FINAL ? (mrow * 640 + 4 * g) * 2 : xo;
    static_for<PF>([&](auto sI) {
      constexpr int s = sI.value;
      G2_LOAD(s, 0); G2_LOAD(s, 1); G2_LOAD(s, 2); G2_LOAD(s, 3); G2_LOAD(s, 4);
      SCHED_FENCE();
      if (s == 0) {
#pragma unroll
        for (int c = 0; c < 5; c++)
          res[c] = FINAL ? __builtin_amdgcn_raw_buffer_load_b64(Rr, ro + c * 32, cs * 160, HO_AUX)
                         : __builtin_amdgcn_raw_buffer_load_b64(Rr, ro + (c & 1) * 2048, cs * 10240 + (c >> 1) * 4096, 0);
        SCHED_FENCE();
      }
    });
  }
  PT(6);
  f4 acc[5][4];
#pragma unroll
  for (int c = 0; c < 5; c++)
#pragma unroll
    for (int m = 0; m < 4; m++) acc[c][m] = f4{0.f, 0.f, 0.f, 0.f};
#define G2_MFMA(S, C, M) acc[C][M] = __builtin_amdgcn_mfma_f32_16x16x32_bf16(wf[S][C], hf[S][M], acc[C][M], 0, 0, 0)
  f4* rw[3];
#pragma unroll
  for (int m = 1; m < 4; m++) rw[m - 1] = red + (((m + wv) & 3) * 3 + m - 1) * 5 * 64 + lane;
#define G2_WRITE(I) do { if ((I) >= 0 && (I) < 15) rw[(I) / 5][((I) % 5) * 64] = acc[(I) % 5][1 + (I) / 5]; } while (0)
  static_for<11>([&](auto sI) {
    constexpr int s = sI.value;
    static_for<5>([&](auto cI) {
      constexpr int c = cI.value;
      G2_MFMA(s, c, 1);
      if (s == 0 && c == 0) PT(2);
      G2_MFMA(s, c, 2); G2_MFMA(s, c, 3);
      if (s < S0) G2_MFMA(s, c, 0);
      G2_LOAD(s + PF, c);
      SCHED_FENCE();
    });
    if (s == 0) PT(1);
  });
  constexpr int NM = 15 + (S0 == 12 ? 1 : ND) * 5, W0 = 7, NB = W0 + 15 * G2_WS + G2_BD;
  f4 part[15];
  static_for<NM>([&](auto iI) {
    constexpr int i = iI.value;
    if (i < 15) G2_MFMA(11, i % 5, 1 + i / 5);
    else G2_MFMA((S0 == 12 ? 11 : S0) + (i - 15) / 5, i % 5, 0);
    if (i < 5) G2_LOAD(11 + PF, i);
    if (i >= 5 && i < 5 * (5 - PF + 1)) G2_LOAD(11 + PF + i / 5, i % 5);
    if (i >= W0 && (i - W0) % G2_WS == 0) G2_WRITE((i - W0) / G2_WS);
    if (i == 15) PT(3);
    if (i == (NB < NM ? NB : NM - 1)) {
#pragma unroll
      for (int k = (NM - 1 - W0) / G2_WS + 1; k < 15; k++) G2_WRITE(k);
      asm volatile("s_waitcnt lgkmcnt(0)\n\ts_barrier" ::: "memory");
      PT(4);
#pragma unroll
      for (int k = 0; k < 15; k++) part[k] = red[(wv * 15 + k) * 64 + lane];
    }
    SCHED_FENCE();
  });
  PT(5);
  f4 own[5];
#pragma unroll
  for (int c = 0; c < 5; c++) own[c] = (part[c] + part[5 + c]) + (part[10 + c] + acc[c][0]);
  float s = 0.f;
#pragma unroll
  for (int c = 0; c < 5; c++) {
    if (!FINAL) {
      const u4 ob = __builtin_bit_cast(u4, own[c]);
      u2 s0 = __builtin_amdgcn_permlane32_swap(ob.x, ob.z, false, false);
      u2 s1 = __builtin_amdgcn_permlane32_swap(ob.y, ob.w, false, false);
      own[c] = __builtin_bit_cast(f4, u4{s0.x, s0.y, s1.x, s1.y});
    }
    f4 o = f4{bf_lo(res[c].x), bf_hi(res[c].x), bf_lo(res[c].y), bf_hi(res[c].y)} + own[c];
    u2 pk = u2{pack_bf(o.x, o.y), pack_bf(o.z, o.w)};
    if (FINAL) {
      __builtin_amdgcn_raw_buffer_store_b64(pk, make_rsrc(out, 2048 * 1280), (mrow * 640 + 4 * g) * 2 + c * 32, cs * 160, AUX_G2);
    } else {
      __builtin_amdgcn_raw_buffer_store_b64(pk, make_rsrc(ws + OFF_XB2 + (size_t)mblk * XB_MS, XB_MS), xo + (c & 1) * 2048, cs * 10240 + (c >> 1) * 4096, AUX_G2);
      unsigned p0 = (cs == 0 && c == 0 && g == 0) ? (pk.x & 0xffff0000u) : pk.x;
      s = dot2(p0, s);
      s = dot2(pk.y, s);
    }
  }
  if (!FINAL) {
    s += __shfl_xor(s, 16);
    s += __shfl_xor(s, 32);
    if (g == 0) unsafeAtomicAdd((double*)(ws + OFF_SSQ) + mrow, (double)s);
  }
  PT(7); PT_FLUSH(KID);
  return -1;
}

template <bool LAST>
__global__ __launch_bounds__(256, 1) void k_mlp(char* __restrict__ ws, __hip_bfloat16* __restrict__ out) {
  extern __shared__ __attribute__((aligned(16))) char smem[];
  const int b = blockIdx.x;
  const int xcd = b & 7, slot = b >> 3;
  const int mblk = (xcd >> 1) * 8 + (slot & 7), cs = (xcd & 1) * 4 + (slot >> 3);
  asm volatile("" ::: "a191");
  int fs = cs;
  do {
    gemm1_body<LAST>(ws, smem, mblk, fs);
    asm volatile("s_waitcnt vmcnt(0)" ::: "memory");
    if ((threadIdx.x & 63) == 0)
      __hip_atomic_store((unsigned*)(ws + OFF_FLG) + ((LAST ? 32 : 0) + mblk) * 32 + fs * 4 + (threadIdx.x >> 6), 1u, __ATOMIC_RELAXED, __HIP_MEMORY_SCOPE_AGENT);
    SCHED_FENCE();
    fs = gemm2_body<LAST>(ws, out, smem, mblk, cs);
  } while (fs >= 0);
  if (LAST) asm volatile("k6_code_end:");
}

extern "C" void kernel_launch(void* const* d_in, const int* in_sizes, int n_in, void* d_out, int out_size, void* d_ws,
                              size_t ws_size, hipStream_t stream) {
  const uint16_t* x = (const uint16_t*)d_in[0];
  const uint16_t* wr = (const uint16_t*)d_in[1];
  const uint16_t* wi = (const uint16_t*)d_in[2];
  const uint16_t* w1 = (const uint16_t*)d_in[4];
  const uint16_t* w2 = (const uint16_t*)d_in[5];
  char* ws = (char*)d_ws;
  __hip_bfloat16* out = (__hip_bfloat16*)d_out;
  (void)hipFuncSetAttribute((const void*)k_colfilter<true>, hipFuncAttributeMaxDynamicSharedMemorySize, LDS_CF);
  (void)hipFuncSetAttribute((const void*)k_colfilter<false>, hipFuncAttributeMaxDynamicSharedMemorySize, LDS_CF);
  (void)hipFuncSetAttribute((const void*)k_mlp<false>, hipFuncAttributeMaxDynamicSharedMemorySize, LDS_G1);
  (void)hipFuncSetAttribute((const void*)k_mlp<true>, hipFuncAttributeMaxDynamicSharedMemorySize, LDS_G1I);
#define NK 7
  k_prep<<<K0_X + K0_W1 + K0_WT, 512, 0, stream>>>(x, wr, wi, w1, w2, (const char*)d_out, ws);
  if (NK > 1) k_colfilter<true><<<256, 256, LDS_CF, stream>>>(w2, ws);
  if (NK > 2) k_mlp<false><<<256, 256, LDS_G1, stream>>>(ws, out);
  if (NK > 4) k_colfilter<false><<<256, 256, LDS_CF, stream>>>(w2, ws);
  if (NK > 5) k_mlp<true><<<256, 256, LDS_G1I, stream>>>(ws, out);
}
```

```cpp
#pragma clang diagnostic ignored "-Warray-bounds"
#include <hip/hip_runtime.h>
#include <hip/hip_bf16.h>
#include <stdint.h>

typedef float c2 __attribute__((ext_vector_type(2)));
typedef float f4 __attribute__((ext_vector_type(4)));
typedef unsigned u2 __attribute__((ext_vector_type(2)));
typedef unsigned u4 __attribute__((ext_vector_type(4)));
typedef __bf16 bf2 __attribute__((ext_vector_type(2)));
typedef __bf16 bf8 __attribute__((ext_vector_type(8)));

#define DEV __device__ __forceinline__
#define SCHED_FENCE() __builtin_amdgcn_sched_barrier(0)

constexpr int S_ = 2048, D_ = 640, F_ = 1536, DF_ = 321;
constexpr float LN_EPS = 1e-5f;

constexpr int XB_MS = 81920 + 512;
constexpr int H_TS = 49;
constexpr int W1_TS = 21;
constexpr size_t OFF_XB = 0;
constexpr size_t OFF_H = OFF_XB + 32ull * XB_MS;
constexpr size_t OFF_W1P = OFF_H + 128ull * H_TS * 1024;
constexpr size_t OFF_W2P = OFF_W1P + 96ull * W1_TS * 1024;
constexpr size_t OFF_W2C = OFF_W2P + 40ull * H_TS * 1024;
constexpr size_t OFF_WT = OFF_W2C + 40ull * H_TS * 1024;
constexpr size_t OFF_X3 = OFF_WT + 160ull * 4 * 256 * 16;
constexpr size_t OFF_RS0 = OFF_X3 + 2048ull * 640 * 2;
constexpr size_t OFF_SSQ = OFF_RS0 + 2048 * 4;
constexpr size_t OFF_FLG = OFF_SSQ + 2048 * 8;
constexpr size_t OFF_XB2 = OFF_FLG + 2 * 32 * 128;

#define TSTAMP(K, P) do {} while (0)
#define PT_DECL do {} while (0)
#define PT(i) do {} while (0)
#define PT_FLUSH(K) do {} while (0)
#define ST_AUX 16
#define AUX_X3 16
#define AUX_G2 16
#define AUX_K0 16
#define AUX_W1 16
#define AUX_CF 16
typedef __amdgpu_buffer_rsrc_t rsrc_t;
DEV rsrc_t make_rsrc(const void* p, unsigned bytes) { return __builtin_amdgcn_make_buffer_rsrc((void*)p, 0, bytes, 0x00020000); }
DEV bf8 bload(rsrc_t rs, unsigned voff, int OFF) {
  return __builtin_bit_cast(bf8, __builtin_amdgcn_raw_buffer_load_b128(rs, voff + (OFF & 4095), OFF & ~4095, 0));
}
DEV c2 add_mi(c2 a, c2 b) {
  c2 d; asm("v_pk_add_f32 %0, %1, %2 op_sel:[0,1] op_sel_hi:[1,0] neg_hi:[0,1]" : "=v"(d) : "v"(a), "v"(b)); return d;
}
DEV c2 add_pi(c2 a, c2 b) {
  c2 d; asm("v_pk_add_f32 %0, %1, %2 op_sel:[0,1] op_sel_hi:[1,0] neg_lo:[0,1]" : "=v"(d) : "v"(a), "v"(b)); return d;
}
DEV c2 cmul(c2 a, c2 w) {
  c2 t, d;
  asm("v_pk_mul_f32 %0, %1, %2 op_sel:[0,0] op_sel_hi:[0,1]" : "=v"(t) : "v"(a), "v"(w));
  asm("v_pk_fma_f32 %0, %1, %2, %3 op_sel:[1,1,0] op_sel_hi:[1,0,1] neg_lo:[0,1,0]" : "=v"(d) : "v"(a), "v"(w), "v"(t));
  return d;
}
DEV c2 cmulc(c2 a, c2 w) {
  c2 t, d;
  asm("v_pk_mul_f32 %0, %1, %2 op_sel:[0,0] op_sel_hi:[0,1] neg_hi:[0,1]" : "=v"(t) : "v"(a), "v"(w));
  asm("v_pk_fma_f32 %0, %1, %2, %3 op_sel:[1,1,0] op_sel_hi:[1,0,1]" : "=v"(d) : "v"(a), "v"(w), "v"(t));
  return d;
}
template <int SGN> DEV c2 twmul(c2 a, c2 w) { return SGN < 0 ? cmul(a, w) : cmulc(a, w); }
template <int SGN> DEV c2 add_rot(c2 a, c2 b) { return SGN < 0 ? add_mi(a, b) : add_pi(a, b); }
template <int SGN> DEV c2 sub_rot(c2 a, c2 b) { return SGN < 0 ? add_pi(a, b) : add_mi(a, b); }
DEV c2 twiddle_rev(float rev) {
  c2 t = c2{__builtin_amdgcn_cosf(rev), -__builtin_amdgcn_sinf(rev)};
  asm("s_nop 1" : "+v"(t));
  return t;
}
template <int N> DEV void tw_powers(c2* t, c2 w) {
  t[0] = w;
#pragma unroll
  for (int k = 2; k <= N; k++) {
    if ((k & 1) == 0) t[k - 1] = cmul(t[k / 2 - 1], t[k / 2 - 1]);
    else {
      int hi = k >= 8 ? 8 : (k >= 4 ? 4 : 2);
      t[k - 1] = cmul(t[hi - 1], t[k - hi - 1]);
    }
  }
}

template <int SGN> DEV void dft4(c2& a, c2& b, c2& c, c2& d) {
  c2 s0 = a + c, s1 = a - c, s2 = b + d, s3 = b - d;
  a = s0 + s2; c = s0 - s2;
  b = add_rot<SGN>(s1, s3); d = sub_rot<SGN>(s1, s3);
}
template <int SGN> DEV c2 rot1(c2 a) { return add_rot<SGN>(a, a) * 0.70710678118654752f; }
template <int SGN> DEV c2 rot3(c2 a) { return sub_rot<SGN>(a, a) * -0.70710678118654752f; }

template <int SGN> DEV void dft8(c2* v) {
  c2 e0 = v[0], e1 = v[2], e2 = v[4], e3 = v[6], o0 = v[1], o1 = v[3], o2 = v[5], o3 = v[7];
  dft4<SGN>(e0, e1, e2, e3);
  dft4<SGN>(o0, o1, o2, o3);
  o1 = rot1<SGN>(o1); o3 = rot3<SGN>(o3);
  v[0] = e0 + o0; v[4] = e0 - o0;
  v[1] = e1 + o1; v[5] = e1 - o1;
  v[2] = add_rot<SGN>(e2, o2); v[6] = sub_rot<SGN>(e2, o2);
  v[3] = e3 + o3; v[7] = e3 - o3;
}
template <int SGN> DEV void dft16(c2* v) {
  const c2 w1 = c2{0.92387953251128674f, -0.38268343236508977f};
  const c2 w3 = c2{0.38268343236508977f, -0.92387953251128674f};
  c2 y[4][4];
#pragma unroll
  for (int i = 0; i < 4; i++) {
    c2 a = v[i], b = v[i + 4], c = v[i + 8], d = v[i + 12];
    dft4<SGN>(a, b, c, d);
    y[i][0] = a; y[i][1] = b; y[i][2] = c; y[i][3] = d;
  }
  y[1][1] = twmul<SGN>(y[1][1], w1);
  y[1][2] = rot1<SGN>(y[1][2]);
  y[1][3] = twmul<SGN>(y[1][3], w3);
  y[2][1] = rot1<SGN>(y[2][1]);
  y[2][3] = rot3<SGN>(y[2][3]);
  y[3][1] = twmul<SGN>(y[3][1], w3);
  y[3][2] = rot3<SGN>(y[3][2]);
  y[3][3] = -twmul<SGN>(y[3][3], w1);
#pragma unroll
  for (int q = 0; q < 4; q++) {
    c2 a = y[0][q], b = y[1][q], c = y[2][q], d = y[3][q];
    if (q == 2) {
      c2 s0 = add_rot<SGN>(a, c), s1 = sub_rot<SGN>(a, c), s2 = b + d, s3 = b - d;
      a = s0 + s2; c = s0 - s2;
      b = add_rot<SGN>(s1, s3); d = sub_rot<SGN>(s1, s3);
    } else {
      dft4<SGN>(a, b, c, d);
    }
    v[q] = a; v[q + 4] = b; v[q + 8] = c; v[q + 12] = d;
  }
}
template <int SGN> DEV void twmulp(c2& a0, c2 w0, c2& a1, c2 w1) {
  c2 d0, d1;
  if (SGN < 0)
    asm("v_pk_mul_f32 %0, %2, %4 op_sel:[0,0] op_sel_hi:[0,1]\n\t"
        "v_pk_mul_f32 %1, %3, %5 op_sel:[0,0] op_sel_hi:[0,1]\n\t"
        "v_pk_fma_f32 %0, %2, %4, %0 op_sel:[1,1,0] op_sel_hi:[1,0,1] neg_lo:[0,1,0]\n\t"
        "v_pk_fma_f32 %1, %3, %5, %1 op_sel:[1,1,0] op_sel_hi:[1,0,1] neg_lo:[0,1,0]"
        : "=&v"(d0), "=&v"(d1) : "v"(a0), "v"(a1), "v"(w0), "v"(w1));
  else
    asm("v_pk_mul_f32 %0, %2, %4 op_sel:[0,0] op_sel_hi:[0,1] neg_hi:[0,1]\n\t"
        "v_pk_mul_f32 %1, %3, %5 op_sel:[0,0] op_sel_hi:[0,1] neg_hi:[0,1]\n\t"
        "v_pk_fma_f32 %0, %2, %4, %0 op_sel:[1,1,0] op_sel_hi:[1,0,1]\n\t"
        "v_pk_fma_f32 %1, %3, %5, %1 op_sel:[1,1,0] op_sel_hi:[1,0,1]"
        : "=&v"(d0), "=&v"(d1) : "v"(a0), "v"(a1), "v"(w0), "v"(w1));
  a0 = d0; a1 = d1;
}
template <int SGN, typename PRE, typename POST> DEV void dft16s(c2* v, PRE pre, POST post) {
  const c2 w1 = c2{0.92387953251128674f, -0.38268343236508977f};
  const c2 w3 = c2{0.38268343236508977f, -0.92387953251128674f};
  const c2 w9 = c2{-0.92387953251128674f, 0.38268343236508977f};
  c2 y[4][4];
#pragma unroll
  for (int i = 0; i < 4; i++) {
    pre(i);
    c2 a = v[i], b = v[i + 4], c = v[i + 8], d = v[i + 12];
    dft4<SGN>(a, b, c, d);
    y[i][0] = a; y[i][1] = b; y[i][2] = c; y[i][3] = d;
    if (i == 1) { twmulp<SGN>(y[1][1], w1, y[1][3], w3); y[1][2] = rot1<SGN>(y[1][2]); }
    if (i == 2) { y[2][1] = rot1<SGN>(y[2][1]); y[2][3] = rot3<SGN>(y[2][3]); }
    if (i == 3) { twmulp<SGN>(y[3][1], w3, y[3][3], w9); y[3][2] = rot3<SGN>(y[3][2]); }
    SCHED_FENCE();
  }
#pragma unroll
  for (int q = 0; q < 4; q++) {
    c2 a = y[0][q], b = y[1][q], c = y[2][q], d = y[3][q];
    if (q == 2) {
      c2 s0 = add_rot<SGN>(a, c), s1 = sub_rot<SGN>(a, c), s2 = b + d, s3 = b - d;
      a = s0 + s2; c = s0 - s2;
      b = add_rot<SGN>(s1, s3); d = sub_rot<SGN>(s1, s3);
    } else {
      dft4<SGN>(a, b, c, d);
    }
    v[q] = a; v[q + 4] = b; v[q + 8] = c; v[q + 12] = d;
    post(q);
    SCHED_FENCE();
  }
}
template <int SGN> DEV void dft5(c2* x) {
  const float c1 = 0.30901699437494742f, c2_ = -0.80901699437494742f;
  const float s1 = 0.95105651629515357f, s2 = 0.58778525229247313f;
  c2 t1 = x[1] + x[4], t2 = x[2] + x[3], t3 = x[1] - x[4], t4 = x[2] - x[3];
  c2 a1 = x[0] + c1 * t1 + c2_ * t2, a2 = x[0] + c2_ * t1 + c1 * t2;
  c2 b1 = s1 * t3 + s2 * t4, b2 = s2 * t3 - s1 * t4;
  x[0] = x[0] + t1 + t2;
  x[1] = add_rot<SGN>(a1, b1); x[4] = sub_rot<SGN>(a1, b1);
  x[2] = add_rot<SGN>(a2, b2); x[3] = sub_rot<SGN>(a2, b2);
}

DEV void lds_fence() { asm volatile("" ::: "memory"); __builtin_amdgcn_wave_barrier(); asm volatile("" ::: "memory"); }
DEV float bf_lo(unsigned u) { return __builtin_bit_cast(float, u << 16); }
DEV float bf_hi(unsigned u) { return __builtin_bit_cast(float, u & 0xffff0000u); }
DEV unsigned pack_bf(float a, float b) { return __builtin_bit_cast(unsigned, __builtin_convertvector(c2{a, b}, bf2)); }

template <int SGN> DEV void twmul2(c2& a0, c2& a1, c2 w) {
  c2 d0, d1;
  if (SGN < 0)
    asm("v_pk_mul_f32 %0, %2, %4 op_sel:[0,0] op_sel_hi:[0,1]\n\t"
        "v_pk_mul_f32 %1, %3, %4 op_sel:[0,0] op_sel_hi:[0,1]\n\t"
        "v_pk_fma_f32 %0, %2, %4, %0 op_sel:[1,1,0] op_sel_hi:[1,0,1] neg_lo:[0,1,0]\n\t"
        "v_pk_fma_f32 %1, %3, %4, %1 op_sel:[1,1,0] op_sel_hi:[1,0,1] neg_lo:[0,1,0]"
        : "=&v"(d0), "=&v"(d1) : "v"(a0), "v"(a1), "v"(w));
  else
    asm("v_pk_mul_f32 %0, %2, %4 op_sel:[0,0] op_sel_hi:[0,1] neg_hi:[0,1]\n\t"
        "v_pk_mul_f32 %1, %3, %4 op_sel:[0,0] op_sel_hi:[0,1] neg_hi:[0,1]\n\t"
        "v_pk_fma_f32 %0, %2, %4, %0 op_sel:[1,1,0] op_sel_hi:[1,0,1]\n\t"
        "v_pk_fma_f32 %1, %3, %4, %1 op_sel:[1,1,0] op_sel_hi:[1,0,1]"
        : "=&v"(d0), "=&v"(d1) : "v"(a0), "v"(a1), "v"(w));
  a0 = d0; a1 = d1;
}
DEV void tangle2(c2& d0, c2& d1, c2 A0, c2 B0, c2 A1, c2 B1, c2 w) {
  c2 e0, e1, o0, o1;
  asm("v_pk_add_f32 %0, %4, %5 neg_hi:[0,1]\n\t"
      "v_pk_add_f32 %1, %6, %7 neg_hi:[0,1]\n\t"
      "v_pk_add_f32 %2, %4, %5 neg_lo:[0,1]\n\t"
      "v_pk_add_f32 %3, %6, %7 neg_lo:[0,1]\n\t"
      "v_pk_fma_f32 %0, %2, %8, %0 op_sel:[0,0,0] op_sel_hi:[0,1,1]\n\t"
      "v_pk_fma_f32 %1, %3, %8, %1 op_sel:[0,0,0] op_sel_hi:[0,1,1]\n\t"
      "v_pk_fma_f32 %0, %2, %8, %0 op_sel:[1,1,0] op_sel_hi:[1,0,1] neg_lo:[0,1,0]\n\t"
      "v_pk_fma_f32 %1, %3, %8, %1 op_sel:[1,1,0] op_sel_hi:[1,0,1] neg_lo:[0,1,0]"
      : "=&v"(e0), "=&v"(e1), "=&v"(o0), "=&v"(o1) : "v"(A0), "v"(B0), "v"(A1), "v"(B1), "v"(w));
  d0 = e0; d1 = e1;
}

constexpr int SCR_F4 = 361;
struct Tw320 { c2 a[4]; c2 b[7]; };
DEV void tw320_init(Tw320& tw, int lane, c2 u0, float scale) {
  tw_powers<4>(tw.a, cmul(u0, u0));
  tw_powers<7>(tw.b, twiddle_rev((float)(lane & 7) * (1.0f / 64.0f)));
#pragma unroll
  for (int i = 0; i < 7; i++) tw.b[i] = tw.b[i] * scale;
}
#define FFT_HOOK_NONE(i)
template <int SGN, int NP, typename HOOK> DEV void fft320x2(c2 (*z)[5], const Tw320& tw, float scale, f4* scr, int lane, c2 (*o)[8], HOOK hook) {
  const int u = lane < 40 ? lane : 39;
  const int q = u >> 3, a = u & 7;
#pragma unroll
  for (int p = 0; p < NP; p++) {
    dft5<SGN>(z[2 * p]); dft5<SGN>(z[2 * p + 1]);
#pragma unroll
    for (int i = 1; i < 5; i++) twmul2<SGN>(z[2 * p][i], z[2 * p + 1][i], tw.a[i - 1]);
    lds_fence();
#pragma unroll
    for (int i = 0; i < 5; i++) scr[p * SCR_F4 + i * 72 + lane] = f4{z[2 * p][i].x, z[2 * p][i].y, z[2 * p + 1][i].x, z[2 * p + 1][i].y};
    lds_fence();
#pragma unroll
    for (int b = 0; b < 8; b++) { f4 t = scr[p * SCR_F4 + q * 72 + a + 8 * b]; o[2 * p][b] = t.xy; o[2 * p + 1][b] = t.zw; }
    SCHED_FENCE();
  }
  hook(0);
#pragma unroll
  for (int p = 0; p < NP; p++) {
    dft8<SGN>(o[2 * p]); dft8<SGN>(o[2 * p + 1]);
    hook(1);
    o[2 * p][0] = o[2 * p][0] * scale; o[2 * p + 1][0] = o[2 * p + 1][0] * scale;
#pragma unroll
    for (int c = 1; c < 8; c++) twmul2<SGN>(o[2 * p][c], o[2 * p + 1][c], tw.b[c - 1]);
    lds_fence();
#pragma unroll
    for (int c = 0; c < 8; c++) scr[p * SCR_F4 + (q * 8 + c) * 9 + a] = f4{o[2 * p][c].x, o[2 * p][c].y, o[2 * p + 1][c].x, o[2 * p + 1][c].y};
    lds_fence();
#pragma unroll
    for (int i = 0; i < 8; i++) { f4 t = scr[p * SCR_F4 + u * 9 + i]; o[2 * p][i] = t.xy; o[2 * p + 1][i] = t.zw; }
    SCHED_FENCE();
  }
  hook(2);
  dft8<SGN>(o[0]);
  hook(3);
  dft8<SGN>(o[1]);
  lds_fence();
}
constexpr float FWD_SC = 0.5f * 0.05590169943749474f;
template <int NP> DEV void rfft2_rows(const unsigned (*rw)[5], const Tw320& tw, f4* scr, int lane, c2 (*o)[8]) {
  c2 z[2 * NP][5];
#pragma unroll
  for (int i = 0; i < 2 * NP; i++)
#pragma unroll
    for (int j = 0; j < 5; j++) z[i][j] = c2{bf_lo(rw[i][j]), bf_hi(rw[i][j])};
  fft320x2<-1, NP>(z, tw, FWD_SC, scr, lane, o, [](int) {});
  const int u = lane < 40 ? lane : 39;
  f4* pp = scr + (u >> 3) + 5 * (u & 7);
#pragma unroll
  for (int p = 0; p < NP; p++) {
    if (p > 0) { dft8<-1>(o[2 * p]); dft8<-1>(o[2 * p + 1]); lds_fence(); }
#pragma unroll
    for (int e = 0; e < 8; e++) pp[p * SCR_F4 + 40 * e] = f4{o[2 * p][e].x, o[2 * p][e].y, o[2 * p + 1][e].x, o[2 * p + 1][e].y};
  }
}
DEV void untangle2(c2& x0, c2& x1, f4 A, f4 B, c2 w, bool k_is_0) {
  tangle2(x0, x1, A.xy, B.xy, A.zw, B.zw, w);
  if (k_is_0) {
    const float r = 1.41421356237309505f;
    x0 = c2{(A.x + A.y) * r, (A.x - A.y) * r};
    x1 = c2{(A.z + A.w) * r, (A.z - A.w) * r};
  }
}
DEV c2 mul_mi(c2 w) { return c2{w.y, -w.x}; }

template <int CTRL> DEV float dpp_add(float v) {
  return v + __builtin_bit_cast(float, __builtin_amdgcn_update_dpp(0, __builtin_bit_cast(int, v), CTRL, 0xf, 0xf, true));
}
DEV float wave_sum63(float v) {
  v = dpp_add<0x111>(v);
  v = dpp_add<0x112>(v);
  v = dpp_add<0x114>(v);
  v = dpp_add<0x118>(v);
  v = v + __builtin_bit_cast(float, __builtin_amdgcn_update_dpp(0, __builtin_bit_cast(int, v), 0x142, 0xa, 0xf, true));
  v = v + __builtin_bit_cast(float, __builtin_amdgcn_update_dpp(0, __builtin_bit_cast(int, v), 0x143, 0xc, 0xf, true));
  return v;
}

#define CODE_ADDR(A, SYM) asm("s_getpc_b64 vcc\n\ts_add_u32 vcc_lo, vcc_lo, " SYM "@rel32@lo+4\n\ts_addc_u32 vcc_hi, vcc_hi, " SYM "@rel32@hi+12\n\ts_mov_b64 %0, vcc" \
                              : "=s"(A) :: "vcc", "scc")
#define CODE_PF(P, COND, SYM, END) do { const char *b_, *e_; CODE_ADDR(b_, SYM); CODE_ADDR(e_, END); \
    long n_ = e_ - b_ - 4; n_ = n_ < 0 ? 0 : n_; long o_ = (long)threadIdx.x * PF_STEP; o_ = o_ < n_ ? o_ : n_ & ~3l; P = (COND) ? b_ + o_ : P; } while (0)
#define PF_STEP 64
#define PF_LOAD(P) (*(const __attribute__((address_space(1))) unsigned*)(P))
#define SYM_K1 "_Z11k_colfilterILb1EEvPKtPc"
#define SYM_K4 "_Z11k_colfilterILb0EEvPKtPc"
#define SYM_K2 "_Z5k_mlpILb0EEvPcP14__hip_bfloat16"
#define SYM_K5 "_Z5k_mlpILb1EEvPcP14__hip_bfloat16"
#define CODE_PF_K1(P, C) CODE_PF(P, C, SYM_K1, SYM_K4)
#define CODE_PF_K4(P, C) CODE_PF(P, C, SYM_K4, SYM_K2)
#define CODE_PF_K2(P, C) CODE_PF(P, C, SYM_K2, SYM_K5)
#define CODE_PF_K5(P, C) CODE_PF(P, C, SYM_K5, "k6_code_end")

DEV int frag_f(int T, int g, int j) {
  int G = T / 3, r = T - 3 * G;
  int ta = r == 0 ? 0 : (r == 1 ? 3 : 2), tb = r == 0 ? 1 : (r == 1 ? 4 : 5);
  return 96 * G + 16 * (j < 4 ? ta : tb) + 4 * g + (j & 3);
}

constexpr int K0_X = 128, K0_W1 = 96, K0_WT = 128;
template <bool is_x>
DEV void prep_rows(const uint16_t* __restrict__ x, const uint16_t* __restrict__ w1, char* __restrict__ ws, f4* scr_all, u2* stage, int grp) {
  const int tid = threadIdx.x, lane = tid & 63, wv = tid >> 6;
  f4* scr = scr_all + wv * SCR_F4;
  unsigned rw2[2][5];
  unsigned *r0 = rw2[0], *r1 = rw2[1];
  u4 s0[2], s1[2];
  if (is_x) {
    const unsigned* row = (const unsigned*)(x + (size_t)(16 * grp + 2 * wv) * D_);
#pragma unroll
    for (int j = 0; j < 5; j++) { r0[j] = row[lane + 64 * j]; r1[j] = row[320 + lane + 64 * j]; }
    if (tid < 16) ((double*)(ws + OFF_SSQ))[16 * grp + tid] = 0.0;
    if (tid >= 64 && tid < 80) ((unsigned*)(ws + OFF_FLG))[16 * grp + tid - 64] = 0u;
  } else {
#pragma unroll
    for (int it = 0; it < 2; it++) {
      int n = it == 0 ? tid : 512 + (tid & 127);
      const uint16_t* p = w1 + (size_t)(n >> 1) * (2 * F_) + grp * 16 + (n & 1) * 8;
      s0[it] = *(const u4*)p; s1[it] = *(const u4*)(p + F_);
    }
  }
  SCHED_FENCE();
  Tw320 tw;
  tw320_init(tw, lane, twiddle_rev((float)lane * (1.0f / 640.0f)), FWD_SC);
  c2 wo[3][2];
  wo[0][0] = mul_mi(twiddle_rev((float)(tid >> 3) * (1.0f / 320.0f)));
  wo[1][0] = cmul(wo[0][0], c2{0.30901699437494742f, -0.95105651629515357f});
  wo[2][0] = cmul(wo[0][0], c2{-0.80901699437494742f, -0.58778525229247313f});
#pragma unroll
  for (int it = 0; it < 3; it++) wo[it][1] = cmul(wo[it][0], c2{0.99995180895932817f, -0.00981731933714962f});
  SCHED_FENCE();
  if (!is_x) {
    asm volatile("" : "+v"(s0[1]), "+v"(s1[1]));
#pragma unroll
    for (int it = 0; it < 2; it++) {
      int n = tid + 512 * it;
      if (it == 0 || tid < 128) {
#pragma unroll
        for (int i = 0; i < 4; i++)
          stage[((n & 1) * 4 + i) * 324 + (n >> 1)] = u2{__builtin_amdgcn_perm(s1[it][i], s0[it][i], 0x05040100u), __builtin_amdgcn_perm(s1[it][i], s0[it][i], 0x07060302u)};
      }
    }
    __syncthreads();
#pragma unroll
    for (int j = 0; j < 5; j++) { u2 t = stage[wv * 324 + lane + 64 * j]; r0[j] = t.x; r1[j] = t.y; }
  }
  TSTAMP(0, 1);
  c2 o[2][8];
  rfft2_rows<1>(rw2, tw, scr, lane, o);
  if (is_x) {
    float ss[2];
#pragma unroll
    for (int rr = 0; rr < 2; rr++) {
      c2 t = o[rr][0];
      if (lane == 0) t = c2{(t.x - t.y) * 0.70710678118654752f, 0.f};
      c2 acc = t * t;
#pragma unroll
      for (int e = 1; e < 8; e++) acc += o[rr][e] * o[rr][e];
      ss[rr] = wave_sum63(lane < 40 ? acc.x + acc.y : 0.f);
    }
    if (lane == 63)
      *(c2*)(ws + OFF_RS0 + (size_t)(16 * grp + 2 * wv) * 4) =
          c2{__builtin_amdgcn_rsqf(ss[0] * (1.0f / 160.0f) + LN_EPS), __builtin_amdgcn_rsqf(ss[1] * (1.0f / 160.0f) + LN_EPS)} * (1.0f / 2048.0f);
  }
  TSTAMP(0, 2);
  __syncthreads();
  const int rp = tid & 7;
  const f4* S = scr_all + rp * SCR_F4;
#pragma unroll
  for (int it = 0; it < 3; it++) {
    if (it == 2 && tid >= 256) break;
    const int q4 = (tid >> 3) + 64 * it;
    f4 A0 = S[2 * q4], A1 = S[2 * q4 + 1], B0 = S[(it == 0 && q4 == 0) ? 0 : 320 - 2 * q4], B1 = S[319 - 2 * q4];
    c2 x00, x10, x01, x11;
    untangle2(x00, x10, A0, B0, wo[it][0], it == 0 && q4 == 0);
    untangle2(x01, x11, A1, B1, wo[it][1], false);
    if (is_x) {
      u4 v = u4{pack_bf(x00.x, x00.y), pack_bf(x01.x, x01.y), pack_bf(x10.x, x10.y), pack_bf(x11.x, x11.y)};
      __builtin_amdgcn_raw_buffer_store_b128(v, make_rsrc(ws + OFF_XB + (size_t)(grp >> 2) * XB_MS, XB_MS), q4 * 512 + (((grp & 3) * 128 + rp * 16) ^ (((q4 >> 1) & 1) << 7)), 0, AUX_K0);
    } else {
      if (it == 0 && q4 == 0) { x00.x = 0.f; x10.x = 0.f; }
      const rsrc_t Wr = make_rsrc(ws + OFF_W1P + (size_t)grp * W1_TS * 1024, W1_TS * 1024);
      const bool od = q4 & 1;
      const u2 c0 = u2{pack_bf(x00.x, x00.y), pack_bf(x01.x, x01.y)}, c1 = u2{pack_bf(x10.x, x10.y), pack_bf(x11.x, x11.y)};
      const u2 snd = od ? c0 : c1, kp = od ? c1 : c0;
      const u2 rcv = u2{(unsigned)__builtin_amdgcn_update_dpp(0, (int)snd.x, 0x128, 0xf, 0xf, false), (unsigned)__builtin_amdgcn_update_dpp(0, (int)snd.y, 0x128, 0xf, 0xf, false)};
      const u4 v = od ? u4{rcv.x, rcv.y, kp.x, kp.y} : u4{kp.x, kp.y, rcv.x, rcv.y};
      __builtin_amdgcn_raw_buffer_store_b128(v, Wr, ((q4 >> 1) * 16 + 2 * rp + (q4 & 1)) * 16, 0, AUX_W1);
    }
  }
  TSTAMP(0, 7);
}

__global__ __launch_bounds__(512) void k_prep(const uint16_t* __restrict__ x, const uint16_t* __restrict__ wr,
                                              const uint16_t* __restrict__ wi, const uint16_t* __restrict__ w1,
                                              const uint16_t* __restrict__ w2, const char* outp, char* __restrict__ ws) {
  __shared__ __attribute__((aligned(16))) f4 scr_all[8 * SCR_F4];
  __shared__ __attribute__((aligned(16))) u2 stage[8 * 324];
  const int tid = threadIdx.x, lane = tid & 63, wv = tid >> 6;
  const int b = blockIdx.x;
  TSTAMP(0, 0);
  const char* ps = nullptr;
  CODE_PF(ps, true, "_Z6k_prepPKtS0_S0_S0_S0_PKcPc", SYM_K1);
  const unsigned spf = tid < 192 ? PF_LOAD(ps) : 0u;
  SCHED_FENCE();

  if (b >= K0_X + K0_W1) {
    int bb = b - (K0_X + K0_W1);
    int unit = (bb & 7) * 16 + (bb >> 3);
    int u = unit & 3, bbit = (unit >> 2) & 1, kc = unit >> 3;
    unsigned* t32 = (unsigned*)scr_all;
    unsigned short re[2][6], im[2][6];
#pragma unroll
    for (int hh = 0; hh < 2; hh++) {
      int ks = kc + 16 * (2 * u + hh + 8 * bbit) + 256 * wv;
      const uint16_t* pr = wr + (size_t)ks * DF_;
      const uint16_t* pi = wi + (size_t)ks * DF_;
#pragma unroll
      for (int i = 0; i < 6; i++) {
        int c = lane + 64 * i;
        c = c < DF_ ? c : DF_ - 1;
        re[hh][i] = pr[c]; im[hh][i] = pi[c];
      }
    }
    SCHED_FENCE();
    const int sl = bb >> 3;
    const char* pa = ws + (size_t)(sl == 8 && tid * 65536u < OFF_XB2 + 32 * XB_MS ? tid * 65536u : 0u);
    if (sl == 9) pa = outp + (tid < 40 ? tid : 0) * 65536u;
    if (sl == 10) pa = (const char*)w2 + (tid < 30 ? tid : 0) * 65536u;
    CODE_PF_K1(pa, sl == 0); CODE_PF_K2(pa, sl == 1); CODE_PF_K4(pa, sl == 3); CODE_PF_K5(pa, sl == 4);
    CODE_PF(pa, sl == 6, SYM_K1 ".kd", SYM_K5 ".kd");
    { const char* a_; CODE_ADDR(a_, SYM_K5 ".kd"); pa = sl == 7 ? a_ : pa; }
    const int wi_ = (sl < 12 ? sl : 11) * 8 + (bb & 7), t3 = tid < 320 ? tid : 319, rr = t3 / 20;
    const unsigned cpf = PF_LOAD(pa);
    const unsigned wpf = *(const unsigned*)(w2 + (size_t)frag_f(wi_ >> 1, (wi_ & 1) * 2 + (rr >> 3), rr & 7) * D_ + (t3 % 20) * 32);
    SCHED_FENCE();
#pragma unroll
    for (int hh = 0; hh < 2; hh++)
#pragma unroll
      for (int i = 0; i < 6; i++) {
        int c = lane + 64 * i;
        if (c < DF_) t32[(hh * 8 + wv) * 324 + c] = (unsigned)re[hh][i] | ((unsigned)im[hh][i] << 16);
      }
    __syncthreads();
    const rsrc_t WT = make_rsrc(ws + OFF_WT, 160 * 4 * 256 * 16);
    for (int n = tid; n < 1280; n += 512) {
      int col = n & 1, hh = (n >> 1) & 1, ih = (n >> 2) & 1, q4 = n >> 3;
      int cc = 2 * q4 + col;
      if (cc == 0) cc = 320;
      u4 v;
#pragma unroll
      for (int e = 0; e < 4; e++) v[e] = t32[(hh * 8 + ih * 4 + e) * 324 + cc];
      int tau = (2 * u + hh) + 8 * kc;
      __builtin_amdgcn_raw_buffer_store_b128(v, WT, ((q4 * 4 + 2 * bbit + ih) * 256 + 2 * tau + col) * 16, 0, AUX_K0);
    }
    asm volatile("" :: "v"(cpf), "v"(spf), "v"(wpf));
    TSTAMP(0, 7);
    return;
  }

  if (b < K0_X) prep_rows<true>(x, w1, ws, scr_all, stage, (b & 7) * 16 + (b >> 3));
  else prep_rows<false>(x, w1, ws, scr_all, stage, ((b - K0_X) & 7) * 12 + ((b - K0_X) >> 3));
  asm volatile("" :: "v"(spf));
}

constexpr int P1 = 152;
constexpr int LDS_CF = 84 * 1024;
#undef PF_STEP
#define PF_STEP 128
template <bool FIRST>
__global__ __launch_bounds__(256, 1) void k_colfilter(const uint16_t* __restrict__ w2, char* __restrict__ ws) {
  extern __shared__ __attribute__((aligned(16))) char smem[];
  const int tid = threadIdx.x;
  const int xcd = blockIdx.x & 7, slot = blockIdx.x >> 3;
  constexpr int KID = FIRST ? 1 : 4;
  (void)KID;
  if (slot >= 20) {
    TSTAMP(KID, 0);
    const int lane = tid & 63, wv = tid >> 6;
    const int i = (slot - 20) * 8 + xcd;
    const int T = i >> 1, g0 = (i & 1) * 2;
    unsigned pf[6], cpf = 0;
    const char* pfbase = ws + OFF_W1P + (size_t)(xcd & 1) * 48 * W1_TS * 1024 + tid * 64;
#define L2_PREFETCH() do { SCHED_FENCE(); _Pragma("unroll") for (int j = 0; j < 6; j++) { int blk = (slot - 20) + 12 * j; blk = blk < 63 ? blk : 62; \
      pf[j] = *(const unsigned*)(pfbase + blk * 16384); } \
      const char* pc_ = nullptr; \
      if (FIRST) { CODE_PF_K4(pc_, true); } else { CODE_PF_K5(pc_, true); } \
      cpf = PF_LOAD(pc_); \
      SCHED_FENCE(); } while (0)
    if (FIRST) {
      f4* scr_all = (f4*)smem;
      unsigned raw[4][5];
#pragma unroll
      for (int rr = 0; rr < 4; rr++) {
        int r = wv * 4 + rr;
        const unsigned* row = (const unsigned*)(w2 + (size_t)frag_f(T, g0 + (r >> 3), r & 7) * D_);
#pragma unroll
        for (int j = 0; j < 5; j++) raw[rr][j] = row[lane + 64 * j];
      }
      L2_PREFETCH();
      Tw320 tw;
      tw320_init(tw, lane, twiddle_rev((float)lane * (1.0f / 640.0f)), FWD_SC);
      const int k0 = (tid & 7) + 8 * (tid >> 4), gg = (tid >> 3) & 1;
      c2 wo[3];
      wo[0] = mul_mi(twiddle_rev((float)k0 * (1.0f / 640.0f)));
      wo[1] = cmul(wo[0], c2{0.30901699437494742f, -0.95105651629515357f});
      wo[2] = cmul(wo[0], c2{-0.80901699437494742f, -0.58778525229247313f});
      SCHED_FENCE();
      c2 o[4][8];
      rfft2_rows<2>(raw, tw, scr_all + (wv * 2) * SCR_F4, lane, o);
      __syncthreads();
      const f4* S = scr_all + gg * 4 * SCR_F4;
#pragma unroll
      for (int it = 0; it < 3; it++) {
        if (it == 2 && tid >= 128) break;
        const int k = k0 + 128 * it;
        const bool z = it == 0 && k == 0;
        f4 A[4], B[4];
#pragma unroll
        for (int jp = 0; jp < 4; jp++) { A[jp] = S[jp * SCR_F4 + k]; B[jp] = S[jp * SCR_F4 + (z ? 0 : 320 - k)]; }
        u4 re, im;
#pragma unroll
        for (int jp = 0; jp < 4; jp++) {
          c2 x0, x1;
          untangle2(x0, x1, A[jp], B[jp], wo[it], z);
          re[jp] = pack_bf(x0.x, x1.x); im[jp] = pack_bf(x0.y, x1.y);
        }
        const rsrc_t Wr = make_rsrc(ws + OFF_W2P, 40 * H_TS * 1024);
        const unsigned vo = (((k >> 3) * H_TS + T) * 64 + (g0 + gg) * 16 + (k & 7)) * 16;
        __builtin_amdgcn_raw_buffer_store_b128(re, Wr, vo, 0, AUX_W1);
        __builtin_amdgcn_raw_buffer_store_b128(im, Wr, vo + 128, 0, AUX_W1);
      }
    } else {
      const rsrc_t W2C = make_rsrc(ws + OFF_W2C, 40 * H_TS * 1024);
      unsigned short v[5][8];
#pragma unroll
      for (int it = 0; it < 5; it++) {
        int n = tid + 256 * it;
        int cl = n & 15, gg = (n >> 4) & 1, ct = n >> 5;
#pragma unroll
        for (int j = 0; j < 8; j++) v[it][j] = w2[(size_t)frag_f(T, g0 + gg, j) * D_ + ct * 16 + cl];
      }
      L2_PREFETCH();
#pragma unroll
      for (int it = 0; it < 5; it++) {
        int n = tid + 256 * it;
        int cl = n & 15, gg = (n >> 4) & 1, ct = n >> 5;
        u4 o;
#pragma unroll
        for (int j = 0; j < 4; j++) o[j] = (unsigned)v[it][2 * j] | ((unsigned)v[it][2 * j + 1] << 16);
        __builtin_amdgcn_raw_buffer_store_b128(o, W2C, ((ct * H_TS + T) * 64 + (g0 + gg) * 16 + cl) * 16, 0, AUX_K0);
      }
    }
    asm volatile("" :: "v"(pf[0]), "v"(pf[1]), "v"(pf[2]), "v"(pf[3]), "v"(pf[4]), "v"(pf[5]), "v"(cpf));
    TSTAMP(KID, 7);
    return;
  }
  typedef __attribute__((address_space(3))) volatile c2* ldsp;
  const int col = tid & 1, tau = tid >> 1;
  const int q4 = xcd * 20 + slot;
  const int lo = tau & 7, kc = tau >> 3;
  PT_DECL; PT(0);
  const rsrc_t XBr = make_rsrc(ws + (FIRST ? OFF_XB : OFF_XB2) + q4 * 512, 32 * XB_MS);
  const unsigned xoff = (tau >> 6) * XB_MS + ((((tau & 63) << 1) + col) ^ (((q4 >> 1) & 1) << 5)) * 4;
  unsigned xr[16];
  c2 v[16];
  float rs[16];
  u2 sq[16];
  u4 wt[4];
  {
    const rsrc_t Sr = make_rsrc(ws + OFF_RS0, 2048 * 12);
#pragma unroll
    for (int rq = 0; rq < 16; rq++) {
      const int r = (rq >> 2) + 4 * (rq & 3);
      if ((rq & 3) == 0) SCHED_FENCE();
      xr[r] = __builtin_amdgcn_raw_buffer_load_b32(XBr, xoff, r * (2 * XB_MS), 0);
      if (FIRST) rs[r] = __builtin_bit_cast(float, __builtin_amdgcn_raw_buffer_load_b32(Sr, tau * 4 + (r & 7) * 512, (r >> 3) * 4096, 0));
      else sq[r] = __builtin_amdgcn_raw_buffer_load_b64(Sr, tau * 8 + (r & 3) * 1024, 8192 + (r >> 2) * 4096, 0);
    }
    SCHED_FENCE();
    const rsrc_t Wr = make_rsrc(ws + OFF_WT + (size_t)q4 * 16384, 16384);
#pragma unroll
    for (int i = 0; i < 4; i++) wt[i] = __builtin_amdgcn_raw_buffer_load_b128(Wr, tid * 16, i * 4096, 0);
  }
  SCHED_FENCE();
  c2 tw1[15], tw2[15];
  tw_powers<15>(tw1, twiddle_rev((float)tau * (1.0f / 2048.0f)));
  tw_powers<15>(tw2, twiddle_rev((float)lo * (1.0f / 128.0f)));
  const bool special = (q4 == 0) && (col == 0);
  const unsigned sel_lo = special ? 0x07060c0cu : 0x05040c0cu, sel_hi = special ? 0x0c0c0c0cu : 0x07060c0cu;
  ldsp e1 = (ldsp)(uintptr_t)(unsigned)(uintptr_t)(__attribute__((address_space(3))) char*)smem + tid;
  ldsp e2 = (ldsp)(uintptr_t)(unsigned)(uintptr_t)(__attribute__((address_space(3))) char*)smem + (kc * P1 + lo) * 2 + col;
  ldsp e3 = (ldsp)(uintptr_t)(unsigned)(uintptr_t)(__attribute__((address_space(3))) char*)smem + (kc * P1 + lo * 9) * 2 + col;
  SCHED_FENCE();
  c2 xv[16];
  dft16s<-1>(v,
    [&](int i) {
#pragma unroll
      for (int j = 0; j < 4; j++) {
        const int r = i + 4 * j;
        if (!FIRST) {
          u2 t = sq[r];
          rs[r] = __builtin_amdgcn_rsqf((float)__builtin_bit_cast(double, t) * (2048.0f * 2048.0f / 640.0f) + LN_EPS * 2048.0f * 2048.0f);
        }
        xv[r] = c2{__builtin_bit_cast(float, __builtin_amdgcn_perm(xr[r], 0u, sel_lo)), __builtin_bit_cast(float, __builtin_amdgcn_perm(xr[r], 0u, sel_hi))};
        v[r] = xv[r] * rs[r];
      }
      if (i == 0) PT(1);
    },
    [&](int q) {
      if (q == 0) v[4] = cmul(v[4], tw1[3]); else twmulp<-1>(v[q], tw1[q - 1], v[q + 4], tw1[q + 3]);
      e1[q * (2 * P1)] = v[q]; e1[(q + 4) * (2 * P1)] = v[q + 4];
      twmulp<-1>(v[q + 8], tw1[q + 7], v[q + 12], tw1[q + 11]);
      e1[(q + 8) * (2 * P1)] = v[q + 8]; e1[(q + 12) * (2 * P1)] = v[q + 12];
    });
  __syncthreads();
#pragma unroll
  for (int i = 0; i < 4; i++)
#pragma unroll
    for (int j = 0; j < 4; j++) v[i + 4 * j] = e2[(i + 4 * j) * 16];
  SCHED_FENCE();
  c2 wf[16];
#pragma unroll
  for (int r = 0; r < 16; r++) { unsigned w = wt[r >> 2][r & 3]; wf[r] = c2{bf_lo(w), bf_hi(w)}; }
  SCHED_FENCE();
  dft16s<-1>(v, [&](int) {},
    [&](int q) {
      if (q == 0) v[4] = cmul(v[4], tw2[3]); else twmulp<-1>(v[q], tw2[q - 1], v[q + 4], tw2[q + 3]);
      e2[q * 18] = v[q]; e2[(q + 4) * 18] = v[q + 4];
      twmulp<-1>(v[q + 8], tw2[q + 7], v[q + 12], tw2[q + 11]);
      e2[(q + 8) * 18] = v[q + 8]; e2[(q + 12) * 18] = v[q + 12];
    });
#pragma unroll
  for (int bb = 0; bb < 2; bb++)
#pragma unroll
    for (int sa = 0; sa < 8; sa++) v[bb * 8 + sa] = e3[(bb * 72 + sa) * 2];
  SCHED_FENCE();
  PT(2);
#pragma unroll
  for (int bb = 0; bb < 2; bb++) {
    dft8<-1>(v + 8 * bb);
#pragma unroll
    for (int r = 0; r < 8; r += 2) twmulp<-1>(v[8 * bb + r], wf[8 * bb + r], v[8 * bb + r + 1], wf[8 * bb + r + 1]);
    SCHED_FENCE();
  }
  dft8<1>(v);
  SCHED_FENCE();
  {
    c2* u = v + 8;
    c2 a0 = u[0], a1 = u[2], a2 = u[4], a3 = u[6], o0 = u[1], o1 = u[3], o2 = u[5], o3 = u[7];
    e3[0] = v[0]; e3[2] = v[1];
    dft4<1>(a0, a1, a2, a3);
    SCHED_FENCE();
    e3[4] = v[2]; e3[6] = v[3];
    dft4<1>(o0, o1, o2, o3);
    SCHED_FENCE();
    e3[8] = v[4]; e3[10] = v[5];
    o1 = rot1<1>(o1); o3 = rot3<1>(o3);
    u[0] = a0 + o0; u[4] = a0 - o0;
    u[2] = add_rot<1>(a2, o2); u[6] = sub_rot<1>(a2, o2);
    SCHED_FENCE();
    e3[12] = v[6]; e3[14] = v[7];
    u[1] = a1 + o1; u[5] = a1 - o1;
    u[3] = a3 + o3; u[7] = a3 - o3;
    SCHED_FENCE();
#pragma unroll
    for (int sa = 0; sa < 8; sa++) e3[(72 + sa) * 2] = u[sa];
  }
#pragma unroll
  for (int i = 0; i < 4; i++)
#pragma unroll
    for (int j = 0; j < 4; j++) v[i + 4 * j] = e2[(i + 4 * j) * 18];
  SCHED_FENCE();
  dft16s<1>(v,
    [&](int i) {
      if (i == 0) v[4] = cmulc(v[4], tw2[3]); else twmulp<1>(v[i], tw2[i - 1], v[i + 4], tw2[i + 3]);
      twmulp<1>(v[i + 8], tw2[i + 7], v[i + 12], tw2[i + 11]);
    },
    [&](int q) {
#pragma unroll
      for (int j = 0; j < 4; j++) e2[(q + 4 * j) * 16] = v[q + 4 * j];
    });
  __syncthreads();
#pragma unroll
  for (int i = 0; i < 4; i++)
#pragma unroll
    for (int j = 0; j < 4; j++) v[i + 4 * j] = e1[(i + 4 * j) * (2 * P1)];
  SCHED_FENCE();
  const unsigned sel_o = special ? 0x05040100u : 0x07060504u;
  PT(3);
  dft16s<1>(v,
    [&](int i) {
      if (i == 0) v[4] = cmulc(v[4], tw1[3]); else twmulp<1>(v[i], tw1[i - 1], v[i + 4], tw1[i + 3]);
      twmulp<1>(v[i + 8], tw1[i + 7], v[i + 12], tw1[i + 11]);
    },
    [&](int q) {
#pragma unroll
      for (int j = 0; j < 4; j++) {
        const int r = q + 4 * j;
        c2 o = xv[r] + v[r];
        __builtin_amdgcn_raw_buffer_store_b32(__builtin_amdgcn_perm(pack_bf(o.x, o.y), xr[r], sel_o), XBr, xoff, r * (2 * XB_MS), AUX_CF);
      }
    });
  PT(7); PT_FLUSH(KID);
}

constexpr int G1_A_BYTES = 81920;
constexpr int G1_WSCR = SCR_F4 * 16;
#define G1_PF 4
#define G1_PFI 4
#define G1_VOL volatile
#define G1_LGKM 0xc87f
#define G1_LS 4
#define G1_NP1 40
#define G2_PF 4
#define HO_AUX 16
#define G2_SLP 2
#define G12_J0 85
#define G12_LS 4
#define G12_N ((159 - G12_J0) / G12_LS + 1)
#define G2_NW 50
#define G2_WR 2
static_assert(G2_NW + 12 * G2_WR >= 60 && G2_WR <= 2, "");
#define G2_SPIN 4096
#define G2_ND 12
#define G2_WS 2
#define G2_BD 4
template <int I> struct ic { static constexpr int value = I; };
template <int N, int I = 0, typename F> DEV void static_for(F f) { if constexpr (I < N) { f(ic<I>{}); static_for<N, I + 1>(f); } }
constexpr int LDS_G1 = G1_A_BYTES + 512 + 4096;
constexpr int LDS_G1I = LDS_G1 + 4 * G1_WSCR;
DEV float dot2(unsigned a, float c) {
  bf2 q = __builtin_bit_cast(bf2, a);
  return __builtin_amdgcn_fdot2_f32_bf16(q, q, c, false);
}
template <bool WITH_INV>
DEV void gemm1_body(char* __restrict__ ws, char* smem, const int mblk, const int fs, const int cs, bf8 (&w2f)[12][5]) {
  int tid = threadIdx.x;
  asm volatile("" : "+v"(tid));
  const int lane = tid & 63;
  const int wv = __builtin_amdgcn_readfirstlane(tid >> 6);
  constexpr int KID = WITH_INV ? 5 : 2;
  (void)KID;
  PT_DECL; PT(0);
  const char* Ag = ws + (WITH_INV ? OFF_XB2 : OFF_XB) + (size_t)mblk * XB_MS;
  const unsigned lds0 = (unsigned)(uintptr_t)(__attribute__((address_space(3))) char*)smem + wv * 20480;
  const unsigned l16 = lane * 16;
#define G1_DMA(I) do { unsigned keep_; asm volatile( \
          "s_mov_b32 %0, m0\n\ts_mov_b32 m0, %3\n\ts_nop 0\n\t" \
          "global_load_lds_dwordx4 %1, %2\n\t" \
          "global_load_lds_dwordx4 %1, %2 offset:1024\n\t" \
          "global_load_lds_dwordx4 %1, %2 offset:2048\n\t" \
          "global_load_lds_dwordx4 %1, %2 offset:3072\n\t" \
          "s_mov_b32 m0, %0" \
          : "=&s"(keep_) : "v"(l16), "s"(Ag + wv * 20480 + (I) * 4096), "s"(lds0 + (I) * 4096) : "memory"); SCHED_FENCE(); } while (0)
  const rsrc_t Wg = make_rsrc(ws + OFF_W1P + (size_t)(fs * 12 + wv * 3) * W1_TS * 1024, 3 * W1_TS * 1024);
  constexpr int PF = WITH_INV ? G1_PFI : G1_PF;
  constexpr int NL = 3 * PF;
  bf8 wf[60];
#define G1_LOAD1(I) do { if ((I) < 60) wf[(I) < 60 ? (I) : 0] = bload(Wg, l16, ((I) / 20 * W1_TS + (I) % 20) * 1024); } while (0)
#define G1_LOADW(S) do { if ((S) < PF) { G1_LOAD1(3 * (S)); G1_LOAD1(3 * (S) + 1); G1_LOAD1(3 * (S) + 2); SCHED_FENCE(); } } while (0)
  unsigned pk3[2][8];
  const int m0 = fs * 8 + 2 * wv;
  const int iu = lane < 40 ? lane : 39;
  const int iq = iu >> 3, ia = iu & 7;
  if (!WITH_INV) {
    G1_DMA(0); G1_DMA(1); G1_DMA(2); G1_DMA(3); G1_DMA(4);
#pragma unroll
    for (int s = 0; s < PF; s++) G1_LOADW(s);
    PT(6);
  } else {
    f4* scr = (f4*)(smem + LDS_G1 + wv * G1_WSCR);
    u2* buf = (u2*)scr;
    u4 rw[3];
#pragma unroll
    for (int i = 0; i < 3; i++) {
      int q = lane + 64 * i;
      if (i == 2) q = q < 160 ? q : 159;
      unsigned vo = q * 512 + ((m0 * 8) ^ (((q >> 1) & 1) << 7));
      asm volatile("global_load_dwordx4 %0, %1, %2" : "=v"(rw[i]) : "v"(vo), "s"(Ag) : "memory");
    }
    SCHED_FENCE();
    G1_DMA(0); G1_DMA(1);
    const float sc = 0.5f * 17.88854381999832f / 320.0f;
    const float r2 = 1.41421356237309505f;
    Tw320 tw;
    c2 wu[5];
    {
      c2 u0 = twiddle_rev((float)lane * (1.0f / 640.0f));
      tw320_init(tw, lane, u0, sc);
      wu[0] = c2{u0.y, u0.x};
      wu[1] = cmul(wu[0], c2{0.80901699437494742f, 0.58778525229247313f});
      wu[2] = cmul(wu[0], c2{0.30901699437494742f, 0.95105651629515357f});
      wu[3] = cmul(wu[0], c2{-0.30901699437494742f, 0.95105651629515357f});
      wu[4] = cmul(wu[0], c2{-0.80901699437494742f, 0.58778525229247313f});
    }
    SCHED_FENCE();
    G1_DMA(2); G1_DMA(3); G1_DMA(4);
    PT(4);
    asm volatile("s_waitcnt vmcnt(20)" : "+v"(rw[0]), "+v"(rw[1]), "+v"(rw[2]) :: "memory");
    PT(5);
#pragma unroll
    for (int i = 0; i < 3; i++) {
      int q = lane + 64 * i;
      if (i == 2) q = q < 160 ? q : 159;
      *(u4*)&buf[2 * q] = u4{rw[i].x, rw[i].z, rw[i].y, rw[i].w};
    }
    lds_fence();
    c2 z[2][5], o[2][8];
    u2 ua[5], ub[5];
#pragma unroll
    for (int j = 0; j < 5; j++) {
      int k = lane + 64 * j;
      ua[j] = buf[k]; ub[j] = buf[(j == 0 && k == 0) ? 0 : 320 - k];
    }
    SCHED_FENCE();
    G1_LOADW(0); G1_LOADW(1);
#pragma unroll
    for (int j = 0; j < 5; j++) {
      c2 A0 = c2{bf_lo(ua[j].x), bf_hi(ua[j].x)}, B0 = c2{bf_lo(ub[j].x), bf_hi(ub[j].x)};
      c2 A1 = c2{bf_lo(ua[j].y), bf_hi(ua[j].y)}, B1 = c2{bf_lo(ub[j].y), bf_hi(ub[j].y)};
      if (j == 0 && lane == 0) {
        B0 = c2{A0.y * r2, 0.f}; A0 = c2{A0.x * r2, 0.f};
        B1 = c2{A1.y * r2, 0.f}; A1 = c2{A1.x * r2, 0.f};
      }
      tangle2(z[0][j], z[1][j], A0, B0, A1, B1, wu[j]);
    }
    fft320x2<1, 1>(z, tw, sc, scr, lane, o, [&](int h) {
      if (h == 0) { G1_LOADW(2); G1_LOADW(3); }
      if (h == 2) { G1_LOADW(4); G1_LOADW(5); }
    });
    PT(6);
#pragma unroll
    for (int rr = 0; rr < 2; rr++)
#pragma unroll
      for (int e = 0; e < 8; e++) pk3[rr][e] = pack_bf(o[rr][e].x, o[rr][e].y);
    SCHED_FENCE();
#pragma unroll
    for (int s = 6; s < PF; s++) G1_LOADW(s);
  }
  const rsrc_t Hr = make_rsrc(ws + OFF_H + (size_t)mblk * 4 * H_TS * 1024, 4 * H_TS * 1024);
  const int G = fs * 2 + (wv >> 1);
  int hfull[4], hhalf[4];
#pragma unroll
  for (int m = 0; m < 4; m++) {
    hfull[m] = ((m ^ wv) * H_TS + 3 * G + (wv & 1)) * 1024; hhalf[m] = ((m ^ wv) * H_TS + 3 * G + 2) * 1024;
    asm("" : "+s"(hfull[m]), "+s"(hhalf[m]));
  }
  SCHED_FENCE();
  asm volatile("s_waitcnt vmcnt(%0)\n\ts_barrier" :: "n"(3 * PF) : "memory");
  if (WITH_INV) {
    const rsrc_t Xr = make_rsrc(ws + OFF_X3 + (size_t)(mblk * 64 + m0) * 1280, 2560);
    const unsigned vo = (iq + 5 * ia) * 4;
#pragma unroll
    for (int rr = 0; rr < 2; rr++)
#pragma unroll
      for (int e = 0; e < 8; e++) __builtin_amdgcn_raw_buffer_store_b32(pk3[rr][e], Xr, vo + (rr * 320 + 40 * e) * 4, 0, AUX_X3);
    SCHED_FENCE();
  }
  PT(1);
  const int g = lane >> 4, r = lane & 15;
  const int swz = (g & 1) << 7;
  typedef __attribute__((address_space(3))) const char* ldsc;
  typedef __attribute__((address_space(3))) G1_VOL const u2* ldsu2;
  ldsc abase[4], abase2[4];
#pragma unroll
  for (int m = 0; m < 4; m++) {
    unsigned o = (2 * g) * 512 + (((16 * (m ^ wv) + r) * 8) ^ swz), o2 = o + 65536;
    asm("" : "+v"(o2));
    abase[m] = (ldsc)smem + o; abase2[m] = (ldsc)smem + o2;
  }
  SCHED_FENCE();
  f4 acc[3][4];
#pragma unroll
  for (int t = 0; t < 3; t++)
#pragma unroll
    for (int m = 0; m < 4; m++) acc[t][m] = f4{0.f, 0.f, 0.f, 0.f};
  u4 af[20][4];
#define G1_READA(KS, M) do { ldsc ap_ = (KS) < 16 ? abase[M] + (KS) * 4096 : abase2[M] + ((KS) - 16) * 4096; \
    u2 lo_ = *(ldsu2)ap_, hi_ = *(ldsu2)(ap_ + 512); af[KS][M] = u4{lo_.x, lo_.y, hi_.x, hi_.y}; } while (0)
#define G1_MFMA(ACC, W, B) ACC = __builtin_amdgcn_mfma_f32_16x16x32_bf16(W, __builtin_bit_cast(bf8, B), ACC, 0, 0, 0)
  G1_READA(0, 0); G1_READA(1, 0); G1_READA(0, 1); G1_READA(0, 2); G1_READA(0, 3);
  G1_READA(2, 0); G1_READA(3, 0); G1_READA(1, 1); G1_READA(1, 2); G1_READA(1, 3);
  typedef __attribute__((address_space(3))) volatile float* ldsf;
  ldsf stw = (ldsf)((ldsc)smem + G1_A_BYTES) + (g == (r >> 2) ? wv * 16 + r : 64 + (lane & 31));
  ldsf str = (ldsf)((ldsc)smem + G1_A_BYTES) + r;
  float rl[4], irs[4], e0, e1;
  unsigned pk[3][4][2];
  SCHED_FENCE();
  f4 gram = f4{0.f, 0.f, 0.f, 0.f};
  constexpr int NP1 = G1_NP1;
  static_for<20>([&](auto kI) {
    constexpr int ks = kI.value;
    constexpr int l0 = NL + (NP1 - NL) * ks / 20, l1 = NL + (NP1 - NL) * (ks + 1) / 20;
    constexpr int infl = (ks == 0 ? 10 : ks <= 8 ? 10 : ks == 12 ? 7 : ks == 14 ? 10 : ks < 19 ? 6 : 0);
    __builtin_amdgcn_s_waitcnt(0xc07f | ((infl & 15) << 8));
    SCHED_FENCE();
    if (ks < 10) {
      u4 ga = af[2 * ks][0];
      if (ks == 0 && g == 0) ga.x &= 0xffff0000u;
      G1_MFMA(gram, __builtin_bit_cast(bf8, ga), af[2 * ks][0]);
      if (ks < 8) G1_READA(ks < 8 ? 2 * ks + 4 : 0, 0);
      SCHED_FENCE();
    }
    static_for<4>([&](auto mI) {
      constexpr int m = mI.value;
      G1_MFMA(acc[0][m], wf[ks], af[ks][m]);
      if (m == 0 && ks < 8) G1_READA(ks < 8 ? 2 * ks + 5 : 0, 0);
      if (m > 0 && ks + 2 < 20) G1_READA(ks + 2 < 20 ? ks + 2 : 0, m);
      if (m == 0 && l0 < l1) G1_LOAD1(l0);
      if (m == 2 && l0 + 1 < l1) G1_LOAD1(l0 + 1);
      if (m == 0 && ks == 11) {
        float sv = (r & 2) ? ((r & 1) ? gram.w : gram.z) : ((r & 1) ? gram.y : gram.x);
        *stw = sv * (1.0f / 640.0f) + LN_EPS;
      }
      if (m == 0 && ks == 13) {
        asm volatile("s_barrier" ::: "memory");
#pragma unroll
        for (int q = 0; q < 4; q++) irs[q] = str[(q ^ wv) * 16];
      }
      if (m == 0 && ks >= 15 && ks < 19) {
        constexpr int q = (ks - 15) & 3;
        float rs = __builtin_amdgcn_rsqf(irs[q]);
        asm("s_nop 0\n\tv_mul_f32 %0, %0, %2\n\tv_mul_f32 %1, 0xbfb8aa3b, %2" : "+v"(irs[q]), "=v"(rl[q]) : "v"(rs));
      }
      SCHED_FENCE();
      if (m == 1 && ks < 10) {
        G1_MFMA(gram, __builtin_bit_cast(bf8, af[2 * ks + 1][0]), af[2 * ks + 1][0]);
        SCHED_FENCE();
      }
    });
  });
  PT(2);
  typedef __attribute__((address_space(3))) volatile u2* ldsx;
  ldsx xw = (ldsx)((ldsc)smem + G1_A_BYTES + 512) + wv * 128 + lane, xr = (ldsx)((ldsc)smem + G1_A_BYTES + 512) + (wv ^ 1) * 128 + lane;
  float a0, a1;
  auto silu_op = [&](auto cI, auto oI) {
    constexpr int c = cI.value, o = oI.value, t = c >> 3, m = (c >> 1) & 3, e = 2 * (c & 1);
    if (o == 0) { a0 = acc[t][m][e]; a1 = acc[t][m][e + 1]; asm("" : "+v"(a0), "+v"(a1)); }
    if (o == 1) asm("v_mul_f32 %0, %2, %4\n\tv_mul_f32 %1, %3, %4" : "=&v"(e0), "=&v"(e1) : "v"(a0), "v"(a1), "v"(rl[m]));
    if (o == 2) asm("v_exp_f32 %0, %0" : "+v"(e0));
    if (o == 3) asm("v_exp_f32 %0, %0" : "+v"(e1));
    if (o == 4) asm("v_fma_f32 %0, %0, %2, %2\n\tv_fma_f32 %1, %1, %2, %2" : "+v"(e0), "+v"(e1) : "v"(irs[m]));
    if (o == 5) asm("v_fma_f32 %0, %0, %2, %2\n\tv_fma_f32 %1, %1, %2, %2\n\tv_rcp_f32 %0, %0" : "+v"(e0), "+v"(e1) : "v"(irs[m]));
    if (o == 6) asm("v_rcp_f32 %0, %0" : "+v"(e0));
    if (o == 7) asm("v_rcp_f32 %0, %0" : "+v"(e1));
    if (o == 8) asm("v_rcp_f32 %1, %1\n\tv_mul_f32 %0, %0, %2" : "+v"(e0), "+v"(e1) : "v"(a0));
    if (o == 9) asm("v_mul_f32 %0, %0, %2\n\tv_mul_f32 %1, %1, %3" : "+v"(e0), "+v"(e1) : "v"(a0), "v"(a1));
    if (o == 10) asm("v_cvt_pk_bf16_f32 %0, %1, %2" : "=v"(pk[t][m][c & 1]) : "v"(e0), "v"(e1));
    if (o == 11) asm("v_mul_f32 %2, %2, %3\n\tv_cvt_pk_bf16_f32 %0, %1, %2" : "=v"(pk[t][m][c & 1]), "+v"(e0), "+v"(e1) : "v"(a1));
    if (o == 10 || o == 11) {
      if (t == 1 && (c & 1))
        __builtin_amdgcn_raw_buffer_store_b128(u4{pk[0][m][0], pk[0][m][1], pk[1][m][0], pk[1][m][1]}, Hr, l16, hfull[m], ST_AUX);
      if (t == 2 && (c & 1) && m == 1) xw[0] = u2{pk[2][1][0], pk[2][1][1]};
    }
    SCHED_FENCE();
  };
#define SOP(C, O) silu_op(ic<(C)>{}, ic<(O)>{})
  const rsrc_t W2r = make_rsrc(ws + (WITH_INV ? OFF_W2C : OFF_W2P) + ((size_t)cs * 5 * H_TS + wv * 12) * 1024, (5 * H_TS - wv * 12) * 1024);
  static_for<160>([&](auto jI) {
    constexpr int j = jI.value;
    constexpr int t = j < 80 ? 1 : 2;
    constexpr int ks = j < 40 ? j >> 2 : j < 80 ? 10 + ((j - 40) % 20) / 2 : ((j - 80) % 40) >> 1;
    constexpr int m = j < 40 ? j & 3 : j < 80 ? ((j - 40) / 20) * 2 + (j & 1) : ((j - 80) / 40) * 2 + (j & 1);
    G1_MFMA(acc[t][m], wf[20 * t + ks], af[ks][m]);
    SCHED_FENCE();
    constexpr int c = j >> 3, o = j & 7;
    if (j == 0) SOP(0, 0);
    if (o == 0) SOP(c, 1);
    if (o == 1) SOP(c, 2);
    if (o == 2) SOP(c, 3);
    if (o == 3) SOP(c, 4);
    if (o == 4) SOP(c, 6);
    if (o == 5) SOP(c, 7);
    if (o == 6) SOP(c, 9);
    if (o == 7) { SOP(c, 10); if (c < 19) SOP(c + 1, 0); }
    if (j % G1_LS == 3) G1_LOAD1(NP1 + j / G1_LS);
    if (j >= G12_J0 && (j - G12_J0) % G12_LS == 0 && (j - G12_J0) / G12_LS < G12_N) {
      constexpr int q = j >= G12_J0 ? ((j - G12_J0) / G12_LS) % 60 : 0;
      w2f[q / 5][q % 5] = bload(W2r, l16, ((q % 5) * H_TS + q / 5) * 1024);
    }
    if (j == 80) PT(3);
    SCHED_FENCE();
  });
  PT(4 + (WITH_INV ? 3 : 0));
  {
#pragma unroll
    for (int m = 3; m >= 2; m--) {
      float x[4];
#pragma unroll
      for (int i = 0; i < 4; i++) x[i] = __builtin_amdgcn_exp2f(acc[2][m][i] * rl[m]);
#pragma unroll
      for (int i = 0; i < 4; i++) x[i] = __builtin_amdgcn_rcpf(__builtin_fmaf(x[i], irs[m], irs[m])) * acc[2][m][i];
      pk[2][m][0] = pack_bf(x[0], x[1]); pk[2][m][1] = pack_bf(x[2], x[3]);
      if (m == 3) xw[64] = u2{pk[2][3][0], pk[2][3][1]};
      SCHED_FENCE();
    }
    asm volatile("s_waitcnt lgkmcnt(0)\n\ts_barrier" ::: "memory");
    const u2 ox[2] = {xr[0], xr[64]};
#pragma unroll
    for (int h = 0; h < 2; h++) {
      const u2 o = ox[h];
      const u4 v = (wv & 1) ? u4{o.x, o.y, pk[2][2 * h][0], pk[2][2 * h][1]} : u4{pk[2][2 * h][0], pk[2][2 * h][1], o.x, o.y};
      __builtin_amdgcn_raw_buffer_store_b128(v, Hr, l16, hhalf[2 * h], ST_AUX);
    }
  }
  PT(7); PT_FLUSH(KID);
}

template <bool FINAL>
DEV int gemm2_body(char* __restrict__ ws, __hip_bfloat16* __restrict__ out, char* smem, const int mblk, const int cs, bf8 (&wf)[12][5]) {
  f4* red = (f4*)smem;
  int tid = threadIdx.x;
  asm volatile("" : "+v"(tid));
  const int lane = tid & 63;
  const int wv = __builtin_amdgcn_readfirstlane(tid >> 6);
  const int g = lane >> 4, r = lane & 15;
  constexpr int KID = FINAL ? 6 : 3;
  (void)KID;
  PT_DECL; PT(0);
  const unsigned l16 = lane * 16;
  const rsrc_t Hr = make_rsrc(ws + OFF_H + ((size_t)mblk * 4 * H_TS + wv * 12) * 1024, (4 * H_TS - wv * 12) * 1024);
  const rsrc_t Wr = make_rsrc(ws + (FINAL ? OFF_W2C : OFF_W2P) + ((size_t)cs * 5 * H_TS + wv * 12) * 1024, (5 * H_TS - wv * 12) * 1024);
  int hoff[4];
#pragma unroll
  for (int m = 0; m < 4; m++) hoff[m] = ((m + wv) & 3) * H_TS * 1024;
  constexpr int PF = G2_PF, ND = 12, S0 = 0;
  bf8 hf[12][4];
#define G2_LH(S, M) hf[S][M] = __builtin_bit_cast(bf8, __builtin_amdgcn_raw_buffer_load_b128(Hr, l16 + ((S) & 3) * 1024, hoff[M] + ((S) >> 2) * 4096, HO_AUX))
#define G2_LW(S, C) wf[S][C] = bload(Wr, l16, ((C) * H_TS + (S)) * 1024)
#define G2_LOAD(S, J) do { constexpr int s_ = (S), j_ = (J); \
    constexpr int q_ = G2_NW + G2_WR * s_ + j_ - 3; \
    if (s_ < 12) { if (j_ < 3) G2_LH(s_ < 12 ? s_ : 0, j_ < 3 ? j_ + 1 : 0); else if (j_ - 3 < G2_WR && q_ < 60) G2_LW((q_ % 60) / 5, q_ % 5); } \
    else if (j_ < 3 && (s_ - 12) * 3 + j_ < 12) G2_LH(((s_ - 12) * 3 + j_) % 12, 0); \
    } while (0)
#pragma unroll
  for (int s = 0; s < 12; s++)
#pragma unroll
    for (int c = 0; c < 5; c++) if (s * 5 + c >= G12_N && s * 5 + c < G2_NW) G2_LW(s, c);
  SCHED_FENCE();
  __attribute__((address_space(3))) volatile int* bc = (__attribute__((address_space(3))) volatile int*)(smem + G1_A_BYTES);
  asm volatile("s_waitcnt lgkmcnt(0)\n\ts_barrier" ::: "memory");
  {
    unsigned* flg = (unsigned*)(ws + OFF_FLG) + ((FINAL ? 32 : 0) + mblk) * 32 + wv * 8;
    unsigned long long miss = 1;
    for (int it = 0; it < G2_SPIN && miss; it++) {
      miss = __builtin_amdgcn_ballot_w64(__hip_atomic_load(flg + (lane & 7), __ATOMIC_RELAXED, __HIP_MEMORY_SCOPE_AGENT) == 0u);
      if (miss) __builtin_amdgcn_s_sleep(G2_SLP);
    }
    if (lane == 0) bc[wv] = miss ? 2 * wv + (int)(__builtin_ctzll(miss) >> 2) : -1;
    asm volatile("" ::: "memory");
  }
  SCHED_FENCE();
  const int mrow = mblk * 64 + wv * 16 + r;
  const int gq = 2 * (g & 1) + (g >> 1);
  const unsigned xo = gq * 512 + (((wv * 16 + r) * 8) ^ ((gq >> 1) << 7));
  u2 res[5];
  {
    const rsrc_t Rr = FINAL ? make_rsrc(ws + OFF_X3, 2048 * 1280) : make_rsrc(ws + OFF_XB + (size_t)mblk * XB_MS, XB_MS);
    const unsigned ro = FINAL ? (mrow * 640 + 4 * g) * 2 : xo;
    static_for<PF>([&](auto sI) {
      constexpr int s = sI.value;
      G2_LOAD(s, 0); G2_LOAD(s, 1); G2_LOAD(s, 2); G2_LOAD(s, 3); G2_LOAD(s, 4);
      SCHED_FENCE();
      if (s == 0) {
#pragma unroll
        for (int c = 0; c < 5; c++)
          res[c] = FINAL ? __builtin_amdgcn_raw_buffer_load_b64(Rr, ro + c * 32, cs * 160, HO_AUX)
                         : __builtin_amdgcn_raw_buffer_load_b64(Rr, ro + (c & 1) * 2048, cs * 10240 + (c >> 1) * 4096, 0);
        SCHED_FENCE();
      }
    });
  }
  PT(6);
  f4 acc[5][4];
#pragma unroll
  for (int c = 0; c < 5; c++)
#pragma unroll
    for (int m = 0; m < 4; m++) acc[c][m] = f4{0.f, 0.f, 0.f, 0.f};
#define G2_MFMA(S, C, M) acc[C][M] = __builtin_amdgcn_mfma_f32_16x16x32_bf16(wf[S][C], hf[S][M], acc[C][M], 0, 0, 0)
  f4* rw[3];
#pragma unroll
  for (int m = 1; m < 4; m++) rw[m - 1] = red + (((m + wv) & 3) * 3 + m - 1) * 5 * 64 + lane;
#define G2_WRITE(I) do { if ((I) >= 0 && (I) < 15) rw[(I) / 5][((I) % 5) * 64] = acc[(I) % 5][1 + (I) / 5]; } while (0)
  static_for<11>([&](auto sI) {
    constexpr int s = sI.value;
    static_for<5>([&](auto cI) {
      constexpr int c = cI.value;
      G2_MFMA(s, c, 1);
      if (s == 0 && c == 0) PT(2);
      G2_MFMA(s, c, 2); G2_MFMA(s, c, 3);
      if (s < S0) G2_MFMA(s, c, 0);
      G2_LOAD(s + PF, c);
      SCHED_FENCE();
    });
    if (s == 0) PT(1);
  });
  constexpr int NM = 15 + (S0 == 12 ? 1 : ND) * 5, W0 = 7, NB = W0 + 15 * G2_WS + G2_BD;
  f4 part[15];
  int mq = -1;
  static_for<NM>([&](auto iI) {
    constexpr int i = iI.value;
    if (i < 15) G2_MFMA(11, i % 5, 1 + i / 5);
    else G2_MFMA((S0 == 12 ? 11 : S0) + (i - 15) / 5, i % 5, 0);
    if (i < 5) G2_LOAD(11 + PF, i);
    if (i >= 5 && i < 5 * (5 - PF + 1)) G2_LOAD(11 + PF + i / 5, i % 5);
    if (i >= W0 && (i - W0) % G2_WS == 0) G2_WRITE((i - W0) / G2_WS);
    if (i == 15) PT(3);
    if (i == (NB < NM ? NB : NM - 1)) {
#pragma unroll
      for (int k = (NM - 1 - W0) / G2_WS + 1; k < 15; k++) G2_WRITE(k);
      asm volatile("s_waitcnt lgkmcnt(0)\n\ts_barrier" ::: "memory");
      PT(4);
#pragma unroll
      for (int k = 0; k < 15; k++) part[k] = red[(wv * 15 + k) * 64 + lane];
      mq = bc[lane & 3];
    }
    SCHED_FENCE();
  });
  PT(5);
  {
    const unsigned long long late = __builtin_amdgcn_ballot_w64(mq >= 0);
    if (late) {
      const int mm = __builtin_amdgcn_readlane(mq, (int)__builtin_ctzll(late));
      __syncthreads();
      return mm;
    }
  }
  f4 own[5];
#pragma unroll
  for (int c = 0; c < 5; c++) own[c] = (part[c] + part[5 + c]) + (part[10 + c] + acc[c][0]);
  float s = 0.f;
#pragma unroll
  for (int c = 0; c < 5; c++) {
    if (!FINAL) {
      const u4 ob = __builtin_bit_cast(u4, own[c]);
      u2 s0 = __builtin_amdgcn_permlane32_swap(ob.x, ob.z, false, false);
      u2 s1 = __builtin_amdgcn_permlane32_swap(ob.y, ob.w, false, false);
      own[c] = __builtin_bit_cast(f4, u4{s0.x, s0.y, s1.x, s1.y});
    }
    f4 o = f4{bf_lo(res[c].x), bf_hi(res[c].x), bf_lo(res[c].y), bf_hi(res[c].y)} + own[c];
    u2 pk = u2{pack_bf(o.x, o.y), pack_bf(o.z, o.w)};
    if (FINAL) {
      __builtin_amdgcn_raw_buffer_store_b64(pk, make_rsrc(out, 2048 * 1280), (mrow * 640 + 4 * g) * 2 + c * 32, cs * 160, AUX_G2);
    } else {
      __builtin_amdgcn_raw_buffer_store_b64(pk, make_rsrc(ws + OFF_XB2 + (size_t)mblk * XB_MS, XB_MS), xo + (c & 1) * 2048, cs * 10240 + (c >> 1) * 4096, AUX_G2);
      unsigned p0 = (cs == 0 && c == 0 && g == 0) ? (pk.x & 0xffff0000u) : pk.x;
      s = dot2(p0, s);
      s = dot2(pk.y, s);
    }
  }
  if (!FINAL) {
    s += __shfl_xor(s, 16);
    s += __shfl_xor(s, 32);
    if (g == 0) unsafeAtomicAdd((double*)(ws + OFF_SSQ) + mrow, (double)s);
  }
  PT(7); PT_FLUSH(KID);
  return -1;
}

template <bool LAST>
__global__ __launch_bounds__(256, 1) void k_mlp(char* __restrict__ ws, __hip_bfloat16* __restrict__ out) {
  extern __shared__ __attribute__((aligned(16))) char smem[];
  const int b = blockIdx.x;
  const int xcd = b & 7, slot = b >> 3;
  const int mblk = (xcd >> 1) * 8 + (slot & 7), cs = (xcd & 1) * 4 + (slot >> 3);
  asm volatile("" ::: "a191");
  int fs = cs;
  do {
    bf8 w2f[12][5];
    gemm1_body<LAST>(ws, smem, mblk, fs, cs, w2f);
    asm volatile("s_waitcnt vmcnt(0)" ::: "memory");
    if ((threadIdx.x & 63) == 0)
      __hip_atomic_store((unsigned*)(ws + OFF_FLG) + ((LAST ? 32 : 0) + mblk) * 32 + fs * 4 + (threadIdx.x >> 6), 1u, __ATOMIC_RELAXED, __HIP_MEMORY_SCOPE_AGENT);
    SCHED_FENCE();
    fs = gemm2_body<LAST>(ws, out, smem, mblk, cs, w2f);
  } while (fs >= 0);
  if (LAST) asm volatile("k6_code_end:");
}

extern "C" void kernel_launch(void* const* d_in, const int* in_sizes, int n_in, void* d_out, int out_size, void* d_ws,
                              size_t ws_size, hipStream_t stream) {
  const uint16_t* x = (const uint16_t*)d_in[0];
  const uint16_t* wr = (const uint16_t*)d_in[1];
  const uint16_t* wi = (const uint16_t*)d_in[2];
  const uint16_t* w1 = (const uint16_t*)d_in[4];
  const uint16_t* w2 = (const uint16_t*)d_in[5];
  char* ws = (char*)d_ws;
  __hip_bfloat16* out = (__hip_bfloat16*)d_out;
  (void)hipFuncSetAttribute((const void*)k_colfilter<true>, hipFuncAttributeMaxDynamicSharedMemorySize, LDS_CF);
  (void)hipFuncSetAttribute((const void*)k_colfilter<false>, hipFuncAttributeMaxDynamicSharedMemorySize, LDS_CF);
  (void)hipFuncSetAttribute((const void*)k_mlp<false>, hipFuncAttributeMaxDynamicSharedMemorySize, LDS_G1);
  (void)hipFuncSetAttribute((const void*)k_mlp<true>, hipFuncAttributeMaxDynamicSharedMemorySize, LDS_G1I);
#define NK 7
  k_prep<<<K0_X + K0_W1 + K0_WT, 512, 0, stream>>>(x, wr, wi, w1, w2, (const char*)d_out, ws);
  if (NK > 1) k_colfilter<true><<<256, 256, LDS_CF, stream>>>(w2, ws);
  if (NK > 2) k_mlp<false><<<256, 256, LDS_G1, stream>>>(ws, out);
  if (NK > 4) k_colfilter<false><<<256, 256, LDS_CF, stream>>>(w2, ws);
  if (NK > 5) k_mlp<true><<<256, 256, LDS_G1I, stream>>>(ws, out);
}
```
